```python
import torch

def _softmax(x, axis=-1):
    return torch.softmax(x, dim=axis)
def _gelu(x):
    return torch.nn.functional.gelu(x, approximate="tanh")
def _silu(x):
    return torch.nn.functional.silu(x)
def _sigmoid(x):
    return torch.sigmoid(x)
def _layernorm(x, axis=-1, eps=1e-5):
    m = x.mean(dim=axis, keepdim=True)
    v = x.var(dim=axis, keepdim=True, unbiased=False)
    return (x - m) / torch.sqrt(v + eps)
def _scatter_add(tgt, idx, src, axis=0):
    return tgt.clone().index_add_(axis, idx, src)
def _segment_sum(src, seg, n):
    out = torch.zeros((n,) + src.shape[1:], dtype=src.dtype)
    return out.index_add_(0, seg, src)
def _pad(x, pw):
    flat = tuple(v for pair in reversed(pw) for v in pair)
    return torch.nn.functional.pad(x, flat)

kernel_name = 'recurrent_scan__mha_block_cached__glu_gated_geglu__prompt'

def setup_inputs():

    _w = (lambda a: (lambda t: t.to(torch.bfloat16) if t.is_floating_point() else t)(torch.as_tensor(a)))
    def _A_setup():
        B, Sq, D = 1, 2048, 512
        H, Sk, Dh = 32, 2048, 128
        return {
            "x": _w((lambda *s: torch.randn(*s, dtype=torch.bfloat16) * 0.05)(B, Sq, D)),
            "k_cache": _w((lambda *s: torch.randn(*s, dtype=torch.bfloat16) * 3.2)(B, H, Sk, Dh)),
            "v_cache": _w((lambda *s: torch.randn(*s, dtype=torch.bfloat16) * 3.2)(B, H, Sk, Dh)),
            "wq": _w((lambda *s: torch.randn(*s, dtype=torch.bfloat16) * 3.2)(D, H * Dh) * (D ** -0.5)),
            "wo": _w((lambda *s: torch.randn(*s, dtype=torch.bfloat16) * 3.2)(H * Dh, D) * ((H * Dh) ** -0.5)),
        }
    _A_in = _A_setup()

    def _B_setup():
        B, S, Dm, Df = 1, 2048, 512, 1792
        return {
            "x": _w((lambda *s: torch.randn(*s, dtype=torch.bfloat16) * 0.05)(B, S, Dm)),
            "w_gate": _w((lambda *s: torch.randn(*s, dtype=torch.bfloat16) * 0.4)(Dm, Df) * (Dm ** -0.5)),
            "w_up": _w((lambda *s: torch.randn(*s, dtype=torch.bfloat16) * 0.4)(Dm, Df) * (Dm ** -0.5)),
            "w_down": _w((lambda *s: torch.randn(*s, dtype=torch.bfloat16) * 0.4)(Df, Dm) * (Df ** -0.5)),
        }
    _B_in = _B_setup()

    return {
        "A_x": _A_in["x"],
        "A_k_cache": _A_in["k_cache"],
        "A_v_cache": _A_in["v_cache"],
        "A_wq": _A_in["wq"],
        "A_wo": _A_in["wo"],
        "B_x": _B_in["x"],
        "B_w_gate": _B_in["w_gate"],
        "B_w_up": _B_in["w_up"],
        "B_w_down": _B_in["w_down"],
    }

def reference(A_x, A_k_cache, A_v_cache, A_wq, A_wo, B_x, B_w_gate, B_w_up, B_w_down):
    def _A_ref(x, k_cache, v_cache, wq, wo):
        B, Sq, D = x.shape
        H, Dh = 32, 128
        q = (lambda x, *d: x.permute(*d))((x @ wq).reshape(B, Sq, H, Dh), 0, 2, 1, 3)
        scores = torch.einsum("bhqd,bhkd->bhqk", q, k_cache) * (Dh ** -0.5)
        p = _softmax(scores, axis=-1)
        o = torch.einsum("bhqk,bhkd->bhqd", p, v_cache)
        return (lambda x, *d: x.permute(*d))(o, 0, 2, 1, 3).reshape(B, Sq, H * Dh) @ wo

    def _B_ref(x, w_gate, w_up, w_down):
        g = torch.einsum("bsd,df->bsf", x, w_gate)
        u = torch.einsum("bsd,df->bsf", x, w_up)
        h = _gelu(g) * u
        return torch.einsum("bsf,fd->bsd", h, w_down)

    x = A_x
    xn = _layernorm(x, axis=-1)
    s = _A_ref(xn, A_k_cache, A_v_cache, A_wq, A_wo)
    g = _silu(_B_ref(xn, B_w_gate, B_w_up, B_w_down))
    return x + s * g
```

```cpp
#include <hip/hip_runtime.h>
#include <hip/hip_bf16.h>
#include <cstdio>

using bf16 = __hip_bfloat16;
using bf16x8 = __attribute__((ext_vector_type(8))) short;
using s16x4  = __attribute__((ext_vector_type(4))) short;
using f32x16 = __attribute__((ext_vector_type(16))) float;
using f32x8  = __attribute__((ext_vector_type(8))) float;
using u32x4  = __attribute__((ext_vector_type(4))) unsigned;
using u32x2  = __attribute__((ext_vector_type(2))) unsigned;
#define DI __device__ __forceinline__
#define SBAR() __builtin_amdgcn_sched_barrier(0)

constexpr int SQ = 2048, DM = 512, NH = 32, DH = 128, SK = 2048, DF = 1792, HD = NH * DH;

DI float bf2f(unsigned short u) { return __uint_as_float(((unsigned)u) << 16); }
DI unsigned cvtpk(float lo, float hi) { unsigned r; asm volatile("v_cvt_pk_bf16_f32 %0, %1, %2" : "=v"(r) : "v"(lo), "v"(hi)); return r; }
DI float rbf(float x) { return __uint_as_float(cvtpk(x, x) << 16); }
DI int crow(int r, int hi) { return (r & 3) + 8 * (r >> 2) + 4 * hi; }

__global__ __launch_bounds__(256) void ln_kernel(const unsigned short* __restrict__ x, unsigned short* __restrict__ xn) {
  const int row = blockIdx.x * 4 + (threadIdx.x >> 6), lane = threadIdx.x & 63;
  const u32x4 v = *(const u32x4*)(x + (long)row * DM + lane * 8);
  float f[8];
  for (int i = 0; i < 4; ++i) { f[2 * i] = __uint_as_float(v[i] << 16); f[2 * i + 1] = __uint_as_float(v[i] & 0xffff0000u); }
  float s = 0; for (int i = 0; i < 8; ++i) s += f[i];
  for (int o = 32; o; o >>= 1) s += __shfl_xor(s, o);
  const float mean = s * (1.f / DM);
  float q = 0; for (int i = 0; i < 8; ++i) { float d = f[i] - mean; q += d * d; }
  for (int o = 32; o; o >>= 1) q += __shfl_xor(q, o);
  const float mb = rbf(mean), vb = rbf(q * (1.f / DM));
  const float sd = rbf(sqrtf(rbf(vb + 1e-5f)));
  u32x4 w;
  for (int i = 0; i < 4; ++i) w[i] = cvtpk(rbf(f[2 * i] - mb) / sd, rbf(f[2 * i + 1] - mb) / sd);
  *(u32x4*)(xn + (long)row * DM + lane * 8) = w;
}

template <int OFF> DI s16x4 tr_read(unsigned vb) { s16x4 r; asm volatile("ds_read_b64_tr_b16 %0, %1 offset:%2" : "=&v"(r) : "v"(vb), "i"(OFF) : "memory"); return r; }

DI void gemm_tile(const unsigned short* __restrict__ A, long lda, const unsigned short* __restrict__ W, long ldw, int K,
                  f32x16 (&acc)[2][2], char* lds) {
  const int tid = threadIdx.x, wid = tid >> 6, lane = tid & 63, l32 = lane & 31, hi = lane >> 5, wm = wid >> 1, wn = wid & 1;
  char* As = lds; char* Ws = lds + 16384;
  for (int k0 = 0; k0 < K; k0 += 64) {
    __syncthreads();
    for (int i = 0; i < 4; ++i) { int c = tid + 256 * i, r = c >> 3, ch = c & 7;
      u32x4 v = *(const u32x4*)(A + (long)r * lda + k0 + ch * 8);
      *(u32x4*)(As + r * 128 + 16 * (ch ^ ((r >> 1) & 7))) = v; }
    for (int i = 0; i < 4; ++i) { int c = tid + 256 * i, k = c >> 4, ch = c & 15;
      u32x4 v = *(const u32x4*)(W + (long)(k0 + k) * ldw + ch * 8);
      *(u32x4*)(Ws + (k >> 3) * 2048 + (ch >> 2) * 512 + (k & 7) * 64 + (ch & 3) * 16) = v; }
    __syncthreads();
    const unsigned wb = (unsigned)(size_t)Ws + ((lane & 3) << 3) + (((lane >> 2) & 3) << 6) + (((lane >> 4) & 1) << 5) + (hi << 11) + wn * 1024;
#pragma unroll
    for (int ks = 0; ks < 4; ++ks) {
      bf16x8 bfr[2];
      for (int mt = 0; mt < 2; ++mt) { int r = wm * 64 + mt * 32 + l32; bfr[mt] = *(const bf16x8*)(As + r * 128 + 16 * ((2 * ks + hi) ^ ((r >> 1) & 7))); }
      s16x4 lo[2], up[2];
      if (ks == 0) { lo[0] = tr_read<0>(wb); up[0] = tr_read<256>(wb); lo[1] = tr_read<512>(wb); up[1] = tr_read<768>(wb); }
      if (ks == 1) { lo[0] = tr_read<4096>(wb); up[0] = tr_read<4096 + 256>(wb); lo[1] = tr_read<4096 + 512>(wb); up[1] = tr_read<4096 + 768>(wb); }
      if (ks == 2) { lo[0] = tr_read<8192>(wb); up[0] = tr_read<8192 + 256>(wb); lo[1] = tr_read<8192 + 512>(wb); up[1] = tr_read<8192 + 768>(wb); }
      if (ks == 3) { lo[0] = tr_read<12288>(wb); up[0] = tr_read<12288 + 256>(wb); lo[1] = tr_read<12288 + 512>(wb); up[1] = tr_read<12288 + 768>(wb); }
      asm volatile("s_waitcnt lgkmcnt(0)" ::: "memory"); SBAR();
      for (int nt = 0; nt < 2; ++nt) { bf16x8 af = __builtin_shufflevector(lo[nt], up[nt], 0, 1, 2, 3, 4, 5, 6, 7);
        for (int mt = 0; mt < 2; ++mt) acc[nt][mt] = __builtin_amdgcn_mfma_f32_32x32x16_bf16(af, bfr[mt], acc[nt][mt], 0, 0, 0); }
    }
  }
}
DI float gelu_t(float x) { float u = 0.7978845608028654f * (x + 0.044715f * x * x * x); float t = 1.f - 2.f / (__expf(2.f * u) + 1.f); return 0.5f * x * (1.f + t); }
DI float silu_f(float x) { return x / (1.f + __expf(-x)); }

__global__ __launch_bounds__(256) void qproj_kernel(const unsigned short* __restrict__ xn, const unsigned short* __restrict__ wq, unsigned short* __restrict__ q) {
  __shared__ __attribute__((aligned(16))) char lds[32768];
  const int bm = blockIdx.x, bn = blockIdx.y;
  f32x16 acc[2][2] = {};
  gemm_tile(xn + (long)bm * 128 * DM, DM, wq + bn * 128, HD, DM, acc, lds);
  const int tid = threadIdx.x, wid = tid >> 6, lane = tid & 63, l32 = lane & 31, hi = lane >> 5, wm = wid >> 1, wn = wid & 1;
  for (int nt = 0; nt < 2; ++nt) for (int mt = 0; mt < 2; ++mt) for (int g = 0; g < 4; ++g) {
    int m = bm * 128 + wm * 64 + mt * 32 + l32, n = bn * 128 + wn * 64 + nt * 32 + 8 * g + 4 * hi;
    u32x2 w = {cvtpk(acc[nt][mt][4 * g], acc[nt][mt][4 * g + 1]), cvtpk(acc[nt][mt][4 * g + 2], acc[nt][mt][4 * g + 3])};
    *(u32x2*)(q + (long)m * HD + n) = w; }
}
__global__ __launch_bounds__(256) void glu_kernel(const unsigned short* __restrict__ xn, const unsigned short* __restrict__ wg, const unsigned short* __restrict__ wu, unsigned short* __restrict__ h) {
  __shared__ __attribute__((aligned(16))) char lds[32768];
  const int bm = blockIdx.x, bn = blockIdx.y;
  f32x16 ag[2][2] = {}, au[2][2] = {};
  gemm_tile(xn + (long)bm * 128 * DM, DM, wg + bn * 128, DF, DM, ag, lds);
  gemm_tile(xn + (long)bm * 128 * DM, DM, wu + bn * 128, DF, DM, au, lds);
  const int tid = threadIdx.x, wid = tid >> 6, lane = tid & 63, l32 = lane & 31, hi = lane >> 5, wm = wid >> 1, wn = wid & 1;
  for (int nt = 0; nt < 2; ++nt) for (int mt = 0; mt < 2; ++mt) for (int g = 0; g < 4; ++g) {
    int m = bm * 128 + wm * 64 + mt * 32 + l32, n = bn * 128 + wn * 64 + nt * 32 + 8 * g + 4 * hi;
    float o[4]; for (int e = 0; e < 4; ++e) o[e] = rbf(gelu_t(rbf(ag[nt][mt][4 * g + e]))) * rbf(au[nt][mt][4 * g + e]);
    u32x2 w = {cvtpk(o[0], o[1]), cvtpk(o[2], o[3])};
    *(u32x2*)(h + (long)m * DF + n) = w; }
}
__global__ __launch_bounds__(256) void final_kernel(const unsigned short* __restrict__ o, const unsigned short* __restrict__ wo, const unsigned short* __restrict__ h,
                                                    const unsigned short* __restrict__ wd, const unsigned short* __restrict__ x, unsigned short* __restrict__ out) {
  __shared__ __attribute__((aligned(16))) char lds[32768];
  const int bm = blockIdx.x, bn = blockIdx.y;
  f32x16 as[2][2] = {}, ay[2][2] = {};
  gemm_tile(o + (long)bm * 128 * HD, HD, wo + bn * 128, DM, HD, as, lds);
  gemm_tile(h + (long)bm * 128 * DF, DF, wd + bn * 128, DM, DF, ay, lds);
  const int tid = threadIdx.x, wid = tid >> 6, lane = tid & 63, l32 = lane & 31, hi = lane >> 5, wm = wid >> 1, wn = wid & 1;
  for (int nt = 0; nt < 2; ++nt) for (int mt = 0; mt < 2; ++mt) for (int g = 0; g < 4; ++g) {
    int m = bm * 128 + wm * 64 + mt * 32 + l32, n = bn * 128 + wn * 64 + nt * 32 + 8 * g + 4 * hi;
    u32x2 xv = *(const u32x2*)(x + (long)m * DM + n);
    float xf[4] = {__uint_as_float(xv[0] << 16), __uint_as_float(xv[0] & 0xffff0000u), __uint_as_float(xv[1] << 16), __uint_as_float(xv[1] & 0xffff0000u)};
    float r[4]; for (int e = 0; e < 4; ++e) r[e] = xf[e] + rbf(rbf(as[nt][mt][4 * g + e]) * rbf(silu_f(rbf(ay[nt][mt][4 * g + e]))));
    u32x2 w = {cvtpk(r[0], r[1]), cvtpk(r[2], r[3])};
    *(u32x2*)(out + (long)m * DM + n) = w; }
}

constexpr int D = 128, NW = 8, QBLK = 32, KVBLK = 64;
constexpr float SCALE = 0.088388347648318440f;
#define ATTN_THR 8.f
constexpr float THR = ATTN_THR;
constexpr int LDQ = HD, LDK = D, LDO = HD;
constexpr size_t SHM_V = KVBLK * D * 2, SHM_K = KVBLK * D * 2, SHM_ATTN = 2 * SHM_V + 2 * SHM_K + NW * 64 * 4;
#define KSWZ(row, colB) ((row) * 256 + ((colB) ^ (((row) & 7) << 4)))
DI void partialSM(f32x16& p0, f32x16& p1, float& m_reg, float& mn, float& alpha) {
  constexpr float C = SCALE * 1.4426950408889634f;
  float pmax = p0[0]; for (int r = 1; r < 16; ++r) pmax = fmaxf(pmax, p0[r]); for (int r = 0; r < 16; ++r) pmax = fmaxf(pmax, p1[r]);
  { auto rr = __builtin_amdgcn_permlane32_swap(__float_as_uint(pmax), __float_as_uint(pmax), false, false);
    pmax = fmaxf(__uint_as_float(rr[0]), __uint_as_float(rr[1])); }
  if (__builtin_expect(__all(pmax - m_reg <= THR / SCALE), 1)) { mn = m_reg; alpha = 1.f; }
  else { mn = fmaxf(m_reg, pmax); alpha = __builtin_amdgcn_exp2f((m_reg - mn) * C); m_reg = mn; }
  float mnC = -mn * C;
  for (int r = 0; r < 16; ++r) p0[r] = fmaf(p0[r], C, mnC); for (int r = 0; r < 16; ++r) p1[r] = fmaf(p1[r], C, mnC);
  for (int r = 0; r < 16; ++r) p0[r] = __builtin_amdgcn_exp2f(p0[r]);
}
DI void finishSM(f32x16& p0, f32x16& p1, float alpha, float& l_reg, bf16x8& pa0, bf16x8& pa1, bf16x8& pa2, bf16x8& pa3) {
  for (int r = 0; r < 16; ++r) p1[r] = __builtin_amdgcn_exp2f(p1[r]);
  float ps = 0; for (int r = 0; r < 16; ++r) ps += p0[r]; for (int r = 0; r < 16; ++r) ps += p1[r];
  { auto rr = __builtin_amdgcn_permlane32_swap(__float_as_uint(ps), __float_as_uint(ps), false, false);
    ps = __uint_as_float(rr[0]) + __uint_as_float(rr[1]); }
  l_reg = l_reg * alpha + ps;
#define PK4(P, BASE, OUT) do { unsigned a0 = cvtpk(P[BASE + 0], P[BASE + 1]), a1 = cvtpk(P[BASE + 2], P[BASE + 3]);   \
    unsigned b0 = cvtpk(P[BASE + 4], P[BASE + 5]), b1 = cvtpk(P[BASE + 6], P[BASE + 7]);                              \
    auto r0 = __builtin_amdgcn_permlane32_swap(a0, b0, false, false); auto r1 = __builtin_amdgcn_permlane32_swap(a1, b1, false, false); \
    u32x4 w = {r0[0], r1[0], r0[1], r1[1]}; OUT = *reinterpret_cast<bf16x8*>(&w); } while (0)
  PK4(p0, 0, pa0); PK4(p0, 8, pa1); PK4(p1, 0, pa2); PK4(p1, 8, pa3);
#undef PK4
}
DI void qkt(f32x16& p0, f32x16& p1, const bf16* Ks, const bf16x8* qr, int r32, int hi) {
  p0 = f32x16{}; p1 = f32x16{};
  for (int d0 = 0; d0 < 8; ++d0) { int cb = (d0 * 16 + hi * 8) * 2;
    bf16x8 b0 = *reinterpret_cast<const bf16x8*>((const char*)Ks + KSWZ(r32, cb));
    bf16x8 b1 = *reinterpret_cast<const bf16x8*>((const char*)Ks + KSWZ(32 + r32, cb));
    p0 = __builtin_amdgcn_mfma_f32_32x32x16_bf16(b0, qr[d0], p0, 0, 0, 0);
    p1 = __builtin_amdgcn_mfma_f32_32x32x16_bf16(b1, qr[d0], p1, 0, 0, 0); }
}
DI int v_st(int k, int c) { const int kk = (k & ~0xC) | ((k & 4) << 1) | ((k & 8) >> 1); return ((kk >> 3) * 4 + (c >> 5)) * 512 + ((kk & 7) * 32 + (c & 31)) * 2; }
DI int v_rd_base(int lane) { return ((lane & 3) << 3) | (((lane >> 2) & 3) << 6) | (((lane >> 4) & 1) << 5) | (((lane >> 5) & 1) << 8); }
constexpr int v_rd_off(int d0, int ks, int half) { return d0 * 512 + ks * 4096 + half * 2048; }
template <int D0> DI void pv_one(f32x16& od, int vb, bf16x8 pa0, bf16x8 pa1, bf16x8 pa2, bf16x8 pa3) {
  const s16x4 l0 = tr_read<v_rd_off(D0, 0, 0)>(vb), h0 = tr_read<v_rd_off(D0, 0, 1)>(vb), l1 = tr_read<v_rd_off(D0, 1, 0)>(vb), h1 = tr_read<v_rd_off(D0, 1, 1)>(vb);
  const s16x4 l2 = tr_read<v_rd_off(D0, 2, 0)>(vb), h2 = tr_read<v_rd_off(D0, 2, 1)>(vb), l3 = tr_read<v_rd_off(D0, 3, 0)>(vb), h3 = tr_read<v_rd_off(D0, 3, 1)>(vb);
  asm volatile("s_waitcnt lgkmcnt(0)" ::: "memory"); SBAR();
#define PK(L, H) (bf16x8){L[0], L[1], L[2], L[3], H[0], H[1], H[2], H[3]}
  od = __builtin_amdgcn_mfma_f32_32x32x16_bf16(pa0, PK(l0, h0), od, 0, 0, 0);
  od = __builtin_amdgcn_mfma_f32_32x32x16_bf16(pa1, PK(l1, h1), od, 0, 0, 0);
  od = __builtin_amdgcn_mfma_f32_32x32x16_bf16(pa2, PK(l2, h2), od, 0, 0, 0);
  od = __builtin_amdgcn_mfma_f32_32x32x16_bf16(pa3, PK(l3, h3), od, 0, 0, 0);
#undef PK
}
DI void pv_d0(f32x16* o, int vb, bf16x8 pa0, bf16x8 pa1, bf16x8 pa2, bf16x8 pa3) {
  pv_one<0>(o[0], vb, pa0, pa1, pa2, pa3); pv_one<1>(o[1], vb, pa0, pa1, pa2, pa3); pv_one<2>(o[2], vb, pa0, pa1, pa2, pa3); pv_one<3>(o[3], vb, pa0, pa1, pa2, pa3);
}
DI void attn_dense_body(const bf16* __restrict__ Qb, const bf16* __restrict__ Kh, const bf16* __restrict__ Vh, unsigned short* __restrict__ Ob, int seq, char* lds) {
  const int tid = threadIdx.x, wid = tid >> 6, lane = tid & 63, r32 = lane & 31, hi = lane >> 5;
  bf16* V_lds = (bf16*)lds; bf16* K_lds = (bf16*)(lds + 2 * SHM_V);
  float* ws = (float*)(lds + 2 * SHM_V + 2 * SHM_K) + wid * 64; float* li_l = ws; float* al_l = ws + 32;
  float m_reg = -1e30f, l_reg = 0; f32x16 o[4] = {}; bf16x8 qr[8];
  const bf16* Qw = Qb + (long)(wid * QBLK + r32) * LDQ + hi * 8;
#pragma unroll
  for (int d0 = 0; d0 < 8; ++d0) qr[d0] = *reinterpret_cast<const bf16x8*>(Qw + d0 * 16);
  const int sr = tid >> 4, sc = (tid & 15) * 8, vst0 = v_st(sr, sc), vst1 = v_st(32 + sr, sc);
  const int vb0 = (int)(uintptr_t)V_lds + v_rd_base(lane);
  struct { bf16x8 vs0, vs1, ks0, ks1; } sr_[2];
#define LD8(p) (*reinterpret_cast<const bf16x8*>(p))
#define SLOAD(i, k0) do { sr_[i].vs0 = LD8(&Vh[(long)((k0) + sr) * LDK + sc]); sr_[i].vs1 = LD8(&Vh[(long)((k0) + 32 + sr) * LDK + sc]); \
    sr_[i].ks0 = LD8(&Kh[(long)((k0) + sr) * LDK + sc]); sr_[i].ks1 = LD8(&Kh[(long)((k0) + 32 + sr) * LDK + sc]); } while (0)
#define SWRITE(b, i) do { *(bf16x8*)((char*)V_lds + (b) * SHM_V + vst0) = sr_[i].vs0;          \
    *(bf16x8*)((char*)V_lds + (b) * SHM_V + vst1) = sr_[i].vs1; int kc = sc * 2;               \
    *(bf16x8*)((char*)K_lds + (b) * SHM_K + KSWZ(sr, kc)) = sr_[i].ks0;                       \
    *(bf16x8*)((char*)K_lds + (b) * SHM_K + KSWZ(32 + sr, kc)) = sr_[i].ks1; } while (0)
#define SWAIT() asm volatile("s_waitcnt vmcnt(4)" ::: "memory")
#define RESC(a) do { if (__any((a) < 1.f)) { if (hi == 0) al_l[r32] = (a); asm volatile("s_waitcnt lgkmcnt(0)" ::: "memory"); \
    for (int d = 0; d < 4; ++d) for (int r = 0; r < 16; ++r) o[d][r] *= al_l[crow(r, hi)]; } } while (0)
  f32x16 pA0, pA1, pB0, pB1; float mnA, mnB, alA, alB; bf16x8 pa0, pa1, pa2, pa3; const int NT = seq / KVBLK;
  constexpr int SE = 0, SO = 1;
  SLOAD(SE, 0); asm volatile("s_waitcnt vmcnt(0)" ::: "memory"); SWRITE(0, SE); __syncthreads();
  qkt(pA0, pA1, K_lds, qr, r32, hi); partialSM(pA0, pA1, m_reg, mnA, alA);
  SLOAD(SO, KVBLK); if (2 < NT) SLOAD(SE, 2 * KVBLK);
  SWAIT(); SWRITE(1, SO); __syncthreads();
  for (int j = 1; j + 1 < NT; j += 2) {
    SBAR(); qkt(pB0, pB1, (bf16*)((char*)K_lds + SHM_K), qr, r32, hi);
    finishSM(pA0, pA1, alA, l_reg, pa0, pa1, pa2, pa3); SBAR();
    SLOAD(SO, (j + 2) * KVBLK); SBAR();
    pv_d0(o, vb0, pa0, pa1, pa2, pa3); partialSM(pB0, pB1, m_reg, mnB, alB);
    __syncthreads(); SWAIT(); SWRITE(0, SE);
    RESC(alB); __syncthreads();
    SBAR(); qkt(pA0, pA1, K_lds, qr, r32, hi);
    finishSM(pB0, pB1, alB, l_reg, pa0, pa1, pa2, pa3); SBAR();
    if (j + 3 < NT) SLOAD(SE, (j + 3) * KVBLK); SBAR();
    pv_d0(o, vb0 + (int)SHM_V, pa0, pa1, pa2, pa3); partialSM(pA0, pA1, m_reg, mnA, alA);
    __syncthreads(); SWAIT(); SWRITE(1, SO);
    RESC(alA); __syncthreads();
  }
  SBAR(); qkt(pB0, pB1, (bf16*)((char*)K_lds + SHM_K), qr, r32, hi);
  finishSM(pA0, pA1, alA, l_reg, pa0, pa1, pa2, pa3); SBAR();
  pv_d0(o, vb0, pa0, pa1, pa2, pa3); partialSM(pB0, pB1, m_reg, mnB, alB);
  __syncthreads(); RESC(alB);
  finishSM(pB0, pB1, alB, l_reg, pa0, pa1, pa2, pa3); SBAR();
  pv_d0(o, vb0 + (int)SHM_V, pa0, pa1, pa2, pa3);
  if (hi == 0) li_l[r32] = l_reg; asm volatile("s_waitcnt lgkmcnt(0)" ::: "memory");
  float rli[16];
#pragma unroll
  for (int r = 0; r < 16; ++r) rli[r] = __builtin_amdgcn_rcpf(li_l[crow(r, hi)]);
  unsigned short* Ow = Ob + (long)(wid * QBLK) * LDO;
#pragma unroll
  for (int r = 0; r < 16; ++r) { int orow = crow(r, hi);
    for (int d0 = 0; d0 < 4; ++d0) Ow[(long)orow * LDO + d0 * 32 + r32] = (unsigned short)(cvtpk(o[d0][r] * rli[r], 0.f) & 0xffff); }
}
__global__ __launch_bounds__(64 * NW, 1)
void attn_fwd_dense(const bf16* __restrict__ Q, const bf16* __restrict__ K, const bf16* __restrict__ V, unsigned short* __restrict__ O) {
  extern __shared__ __attribute__((aligned(16))) char shm[];
  const int bid = blockIdx.x; const long h = (bid & 7) * 4 + ((bid >> 3) & 3), qb = bid >> 5, i0 = qb * 256;
  const long q0 = i0 * HD + h * D, k0 = h * SK * (long)D;
  attn_dense_body(Q + q0, K + k0, V + k0, O + q0, SK, shm);
}

extern "C" void kernel_launch(void* const* d_in, const int* in_sizes, int n_in, void* d_out, int out_size, void* d_ws, size_t ws_size, hipStream_t stream) {
  const unsigned short* x = (const unsigned short*)d_in[0];
  const bf16* kc = (const bf16*)d_in[1]; const bf16* vc = (const bf16*)d_in[2];
  const unsigned short* wq = (const unsigned short*)d_in[3]; const unsigned short* wo = (const unsigned short*)d_in[4];
  const unsigned short* wg = (const unsigned short*)d_in[6]; const unsigned short* wu = (const unsigned short*)d_in[7]; const unsigned short* wd = (const unsigned short*)d_in[8];
  char* ws = (char*)d_ws;
  unsigned short* xn = (unsigned short*)ws;
  unsigned short* q  = (unsigned short*)(ws + (2u << 20));
  unsigned short* o  = (unsigned short*)(ws + (18u << 20));
  unsigned short* h  = (unsigned short*)(ws + (34u << 20));
  hipFuncSetAttribute((const void*)attn_fwd_dense, hipFuncAttributeMaxDynamicSharedMemorySize, (int)SHM_ATTN);
  ln_kernel<<<SQ / 4, 256, 0, stream>>>(x, xn);
  qproj_kernel<<<dim3(SQ / 128, HD / 128), 256, 0, stream>>>(xn, wq, q);
  glu_kernel<<<dim3(SQ / 128, DF / 128), 256, 0, stream>>>(xn, wg, wu, h);
  attn_fwd_dense<<<256, 512, SHM_ATTN, stream>>>((const bf16*)q, kc, vc, o);
  final_kernel<<<dim3(SQ / 128, DM / 128), 256, 0, stream>>>(o, wo, h, wd, x, (unsigned short*)d_out);
}
```

```cpp
#include <hip/hip_runtime.h>
#include <hip/hip_bf16.h>

typedef unsigned u32x4 __attribute__((ext_vector_type(4)));
#define DI __device__ __forceinline__
constexpr int SQ = 2048, DM = 512, NH = 32, DH = 128, SK = 2048, DF = 1792, HD = NH * DH;
DI unsigned cvtpk(float lo, float hi) { unsigned r; asm volatile("v_cvt_pk_bf16_f32 %0, %1, %2" : "=v"(r) : "v"(lo), "v"(hi)); return r; }
DI float rbf(float x) { return __uint_as_float(cvtpk(x, x) << 16); }
DI unsigned rfl(unsigned x) { return __builtin_amdgcn_readfirstlane(x); }

__global__ __launch_bounds__(256) void ln_kernel(const unsigned short* __restrict__ x, unsigned short* __restrict__ xn, unsigned* __restrict__ cnt) {
  if (blockIdx.x == 0 && threadIdx.x < 64) cnt[threadIdx.x * 32] = 0u;
  const int row = blockIdx.x * 4 + (threadIdx.x >> 6), lane = threadIdx.x & 63;
  const u32x4 v = *(const u32x4*)(x + (long)row * DM + lane * 8);
  float f[8];
  for (int i = 0; i < 4; ++i) { f[2 * i] = __uint_as_float(v[i] << 16); f[2 * i + 1] = __uint_as_float(v[i] & 0xffff0000u); }
  float s = 0; for (int i = 0; i < 8; ++i) s += f[i];
  for (int o = 32; o; o >>= 1) s += __shfl_xor(s, o);
  const float mean = s * (1.f / DM);
  float q = 0; for (int i = 0; i < 8; ++i) { float d = f[i] - mean; q += d * d; }
  for (int o = 32; o; o >>= 1) q += __shfl_xor(q, o);
  const float mb = rbf(mean), vb = rbf(q * (1.f / DM));
  const float sd = rbf(sqrtf(rbf(vb + 1e-5f)));
  u32x4 w;
  for (int i = 0; i < 4; ++i) w[i] = cvtpk(rbf(f[2 * i] - mb) / sd, rbf(f[2 * i + 1] - mb) / sd);
  *(u32x4*)((char*)xn + (row >> 4) * 16384 + (lane >> 2) * 1024 + ((row & 15) + 16 * (lane & 3)) * 16) = w;
}

#define FUSED_ASM \
  "CSTART%=:\n" \
  "s_getpc_b64 s[98:99]\n" \
  "v_mbcnt_lo_u32_b32 v222, -1, 0\n" \
  "v_mbcnt_hi_u32_b32 v222, -1, v222\n" \
  "s_lshl_b32 s97, %6, 6\n" \
  "v_add_u32 v222, s97, v222\n" \
  "v_lshlrev_b32 v222, 7, v222\n" \
  "s_mov_b32 s97, CEND%=-CSTART%=-8\n" \
  "v_min_u32 v220, s97, v222\n" \
  "global_load_dword v220, v220, s[98:99]\n" \
  "v_add_u32 v222, 0x8000, v222\n" \
  "v_min_u32 v221, s97, v222\n" \
  "global_load_dword v221, v221, s[98:99]\n" \
  "s_mov_b32 s40, %0\n" \
  "s_mov_b32 s41, %1\n" \
  "s_mov_b32 s42, 0x80000\n" \
  "s_mov_b32 s43, 0x20000\n" \
  "s_mov_b32 s44, %2\n" \
  "s_mov_b32 s45, %3\n" \
  "s_mov_b32 s46, 0x80000\n" \
  "s_mov_b32 s47, 0x20000\n" \
  "s_mov_b32 s48, %4\n" \
  "s_mov_b32 s49, %5\n" \
  "s_mov_b32 s50, 16777216\n" \
  "s_mov_b32 s51, 0x20000\n" \
  "s_lshl_b32 s64, %6, 12\n" \
  "s_add_u32 s52, s64, 0xc000\n" \
  "s_add_u32 s53, s64, 0x8000\n" \
  "s_mov_b32 s54, s64\n" \
  "s_add_u32 s55, s64, 0x10000\n" \
  "s_mov_b32 s56, 0\n" \
  "s_mov_b32 s57, 0\n" \
  "s_mov_b32 s58, 8\n" \
  "s_mov_b32 s59, %9\n" \
  "s_mov_b32 s66, 0\n" \
  "s_mov_b32 s67, %7\n" \
  "s_mov_b32 s68, %8\n" \
  "s_mov_b32 s76, %12\n" \
  "s_mov_b32 s77, %13\n" \
  "s_mov_b32 s78, %14\n" \
  "s_mov_b32 s79, 0x20000\n" \
  "s_mov_b32 s80, %15\n" \
  "s_mov_b32 s81, %16\n" \
  "s_mov_b32 s82, %19\n" \
  "s_mov_b32 s83, 0x20000\n" \
  "s_mov_b32 s84, %17\n" \
  "s_mov_b32 s85, %18\n" \
  "s_mov_b32 s86, %19\n" \
  "s_mov_b32 s87, 0x20000\n" \
  "s_mov_b32 s72, %10\n" \
  "s_mov_b32 s73, %11\n" \
  "s_mov_b32 s74, 2097152\n" \
  "s_mov_b32 s75, 0x20000\n" \
  "s_lshl_b32 s95, %6, 12\n" \
  "s_mov_b32 s92, %22\n" \
  "s_lshl_b32 s93, %6, 17\n" \
  "s_mul_i32 s94, %6, 57344\n" \
  "s_mov_b32 s96, %8\n" \
  "v_mbcnt_lo_u32_b32 v248, -1, 0\n" \
  "v_mbcnt_hi_u32_b32 v248, -1, v248\n" \
  "v_and_b32 v249, 15, v248\n" \
  "v_lshrrev_b32 v250, 4, v248\n" \
  "v_bfe_u32 v251, v248, 2, 2\n" \
  "v_and_b32 v252, 3, v248\n" \
  "v_xor_b32 v253, 0, v252\n" \
  "v_lshlrev_b32 v236, 13, v251\n" \
  "v_lshl_add_u32 v236, v250, 6, v236\n" \
  "v_lshl_add_u32 v236, v253, 4, v236\n" \
  "v_add_u32 v236, 0, v236\n" \
  "v_xor_b32 v253, 0, v252\n" \
  "v_lshlrev_b32 v237, 13, v251\n" \
  "v_lshl_add_u32 v237, v250, 6, v237\n" \
  "v_lshl_add_u32 v237, v253, 4, v237\n" \
  "v_add_u32 v237, 31744, v237\n" \
  "v_xor_b32 v253, 2, v252\n" \
  "v_lshlrev_b32 v238, 13, v251\n" \
  "v_lshl_add_u32 v238, v250, 6, v238\n" \
  "v_lshl_add_u32 v238, v253, 4, v238\n" \
  "v_add_u32 v238, 63488, v238\n" \
  "v_xor_b32 v253, 2, v252\n" \
  "v_lshlrev_b32 v239, 13, v251\n" \
  "v_lshl_add_u32 v239, v250, 6, v239\n" \
  "v_lshl_add_u32 v239, v253, 4, v239\n" \
  "v_add_u32 v239, 95232, v239\n" \
  "v_lshrrev_b32 v253, 5, v248\n" \
  "v_lshl_add_u32 v253, v253, 2, v251\n" \
  "v_mul_u32_u24 v253, 3584, v253\n" \
  "v_and_b32 v254, 1, v250\n" \
  "v_lshl_add_u32 v253, v254, 6, v253\n" \
  "v_xor_b32 v240, 0, v252\n" \
  "v_lshl_add_u32 v240, v240, 4, v253\n" \
  "v_add_u32 v240, 0, v240\n" \
  "v_xor_b32 v241, 2, v252\n" \
  "v_lshl_add_u32 v241, v241, 4, v253\n" \
  "v_add_u32 v241, 27648, v241\n" \
  "v_lshlrev_b32 v232, 4, v248\n" \
  "v_add_u32 v233, 16384, v232\n" \
  "v_add_u32 v234, 32768, v232\n" \
  "v_add_u32 v235, 49152, v232\n" \
  "s_add_u32 m0, s95, 0\n" \
  "s_nop 0\n" \
  "buffer_load_dwordx4 v236, s[76:79], s93 offen lds\n" \
  "buffer_load_dwordx4 v237, s[76:79], s93 offen offset:1024 lds\n" \
  "buffer_load_dwordx4 v238, s[76:79], s93 offen offset:2048 lds\n" \
  "buffer_load_dwordx4 v239, s[76:79], s93 offen offset:3072 lds\n" \
  "s_lshr_b32 s97, s95, 1\n" \
  "s_add_u32 m0, s97, 16384\n" \
  "s_nop 0\n" \
  "buffer_load_dwordx4 v240, s[80:83], s94 offen lds\n" \
  "buffer_load_dwordx4 v241, s[80:83], s94 offen offset:1024 lds\n" \
  "s_add_u32 m0, s97, 24576\n" \
  "s_nop 0\n" \
  "buffer_load_dwordx4 v240, s[84:87], s94 offen lds\n" \
  "buffer_load_dwordx4 v241, s[84:87], s94 offen offset:1024 lds\n" \
  "s_add_u32 s93, s93, 0x80000\n" \
  "s_add_u32 s94, s94, 229376\n" \
  "buffer_load_dwordx4 v[128:131], v232, s[72:75], s92 offen offset:0\n" \
  "buffer_load_dwordx4 v[136:139], v233, s[72:75], s92 offen offset:0\n" \
  "buffer_load_dwordx4 v[144:147], v234, s[72:75], s92 offen offset:0\n" \
  "buffer_load_dwordx4 v[152:155], v235, s[72:75], s92 offen offset:0\n" \
  "buffer_load_dwordx4 v[132:135], v232, s[72:75], s92 offen offset:1024\n" \
  "buffer_load_dwordx4 v[140:143], v233, s[72:75], s92 offen offset:1024\n" \
  "buffer_load_dwordx4 v[148:151], v234, s[72:75], s92 offen offset:1024\n" \
  "buffer_load_dwordx4 v[156:159], v235, s[72:75], s92 offen offset:1024\n" \
  "s_add_u32 s92, s92, 2048\n" \
  "s_mov_b32 s88, %20\n" \
  "s_mov_b32 s89, %21\n" \
  "s_mov_b32 s90, 7340032\n" \
  "s_mov_b32 s91, 0x20000\n" \
  "v_or_b32 v242, 0, v250\n" \
  "v_xor_b32 v242, v242, v249\n" \
  "v_lshlrev_b32 v242, 4, v242\n" \
  "v_lshl_add_u32 v242, v250, 8, v242\n" \
  "v_or_b32 v243, 4, v250\n" \
  "v_xor_b32 v243, v243, v249\n" \
  "v_lshlrev_b32 v243, 4, v243\n" \
  "v_lshl_add_u32 v243, v250, 8, v243\n" \
  "v_or_b32 v244, 8, v250\n" \
  "v_xor_b32 v244, v244, v249\n" \
  "v_lshlrev_b32 v244, 4, v244\n" \
  "v_lshl_add_u32 v244, v250, 8, v244\n" \
  "v_or_b32 v245, 12, v250\n" \
  "v_xor_b32 v245, v245, v249\n" \
  "v_lshlrev_b32 v245, 4, v245\n" \
  "v_lshl_add_u32 v245, v250, 8, v245\n" \
  "v_lshlrev_b32 v246, 8, v251\n" \
  "v_lshl_add_u32 v246, v250, 6, v246\n" \
  "v_xor_b32 v247, 2, v252\n" \
  "v_lshl_add_u32 v247, v247, 4, v246\n" \
  "v_lshl_add_u32 v246, v252, 4, v246\n" \
  "v_lshlrev_b32 v253, 6, v251\n" \
  "v_lshl_add_u32 v253, v252, 3, v253\n" \
  "v_xor_b32 v255, 0, v254\n" \
  "v_lshl_add_u32 v255, v255, 5, v253\n" \
  "v_lshl_add_u32 v224, v250, 11, v255\n" \
  "v_add_u32 v226, 0x10000, v224\n" \
  "v_lshl_add_u32 v228, v250, 10, v255\n" \
  "v_add_u32 v228, 0x4000, v228\n" \
  "v_add_u32 v230, 0x10000, v228\n" \
  "v_xor_b32 v255, 1, v254\n" \
  "v_lshl_add_u32 v255, v255, 5, v253\n" \
  "v_lshl_add_u32 v225, v250, 11, v255\n" \
  "v_add_u32 v227, 0x10000, v225\n" \
  "v_lshl_add_u32 v229, v250, 10, v255\n" \
  "v_add_u32 v229, 0x4000, v229\n" \
  "v_add_u32 v231, 0x10000, v229\n" \
  "s_waitcnt vmcnt(8)\n" \
  "s_barrier\n" \
  "ds_read_b64_tr_b16 v[0:1], v224 offset:0\n" \
  "ds_read_b64_tr_b16 v[2:3], v224 offset:1024\n" \
  "ds_read_b64_tr_b16 v[4:5], v225 offset:0\n" \
  "ds_read_b64_tr_b16 v[6:7], v225 offset:1024\n" \
  "ds_read_b64_tr_b16 v[8:9], v224 offset:256\n" \
  "ds_read_b64_tr_b16 v[10:11], v224 offset:1280\n" \
  "ds_read_b64_tr_b16 v[12:13], v225 offset:256\n" \
  "ds_read_b64_tr_b16 v[14:15], v225 offset:1280\n" \
  "ds_read_b64_tr_b16 v[16:17], v224 offset:512\n" \
  "ds_read_b64_tr_b16 v[18:19], v224 offset:1536\n" \
  "ds_read_b64_tr_b16 v[20:21], v225 offset:512\n" \
  "ds_read_b64_tr_b16 v[22:23], v225 offset:1536\n" \
  "ds_read_b64_tr_b16 v[24:25], v224 offset:768\n" \
  "ds_read_b64_tr_b16 v[26:27], v224 offset:1792\n" \
  "ds_read_b64_tr_b16 v[28:29], v225 offset:768\n" \
  "ds_read_b64_tr_b16 v[30:31], v225 offset:1792\n" \
  "ds_read_b64_tr_b16 v[32:33], v228 offset:0\n" \
  "ds_read_b64_tr_b16 v[34:35], v228 offset:512\n" \
  "ds_read_b64_tr_b16 v[36:37], v229 offset:0\n" \
  "ds_read_b64_tr_b16 v[38:39], v229 offset:512\n" \
  "ds_read_b64_tr_b16 v[40:41], v228 offset:256\n" \
  "ds_read_b64_tr_b16 v[42:43], v228 offset:768\n" \
  "ds_read_b64_tr_b16 v[44:45], v229 offset:256\n" \
  "ds_read_b64_tr_b16 v[46:47], v229 offset:768\n" \
  "ds_read_b64_tr_b16 v[48:49], v228 offset:8192\n" \
  "ds_read_b64_tr_b16 v[50:51], v228 offset:8704\n" \
  "ds_read_b64_tr_b16 v[52:53], v229 offset:8192\n" \
  "ds_read_b64_tr_b16 v[54:55], v229 offset:8704\n" \
  "ds_read_b64_tr_b16 v[56:57], v228 offset:8448\n" \
  "ds_read_b64_tr_b16 v[58:59], v228 offset:8960\n" \
  "ds_read_b64_tr_b16 v[60:61], v229 offset:8448\n" \
  "ds_read_b64_tr_b16 v[62:63], v229 offset:8960\n" \
  "s_waitcnt vmcnt(0) lgkmcnt(0)\n" \
  "v_mfma_f32_16x16x32_bf16 a[0:3], v[0:3], v[128:131], 0\n" \
  "ds_read_b64_tr_b16 v[64:65], v224 offset:8192\n" \
  "v_mfma_f32_16x16x32_bf16 a[4:7], v[0:3], v[136:139], 0\n" \
  "ds_read_b64_tr_b16 v[66:67], v224 offset:9216\n" \
  "s_add_u32 m0, s95, 32768\n" \
  "s_nop 0\n" \
  "buffer_load_dwordx4 v236, s[76:79], s93 offen lds\n" \
  "v_mfma_f32_16x16x32_bf16 a[8:11], v[0:3], v[144:147], 0\n" \
  "ds_read_b64_tr_b16 v[68:69], v225 offset:8192\n" \
  "v_mfma_f32_16x16x32_bf16 a[12:15], v[0:3], v[152:155], 0\n" \
  "ds_read_b64_tr_b16 v[70:71], v225 offset:9216\n" \
  "buffer_load_dwordx4 v237, s[76:79], s93 offen offset:1024 lds\n" \
  "v_mfma_f32_16x16x32_bf16 a[16:19], v[4:7], v[128:131], 0\n" \
  "ds_read_b64_tr_b16 v[72:73], v224 offset:8448\n" \
  "v_mfma_f32_16x16x32_bf16 a[20:23], v[4:7], v[136:139], 0\n" \
  "ds_read_b64_tr_b16 v[74:75], v224 offset:9472\n" \
  "buffer_load_dwordx4 v238, s[76:79], s93 offen offset:2048 lds\n" \
  "v_mfma_f32_16x16x32_bf16 a[24:27], v[4:7], v[144:147], 0\n" \
  "ds_read_b64_tr_b16 v[76:77], v225 offset:8448\n" \
  "v_mfma_f32_16x16x32_bf16 a[28:31], v[4:7], v[152:155], 0\n" \
  "ds_read_b64_tr_b16 v[78:79], v225 offset:9472\n" \
  "buffer_load_dwordx4 v239, s[76:79], s93 offen offset:3072 lds\n" \
  "v_mfma_f32_16x16x32_bf16 a[32:35], v[8:11], v[128:131], 0\n" \
  "ds_read_b64_tr_b16 v[80:81], v224 offset:8704\n" \
  "v_mfma_f32_16x16x32_bf16 a[36:39], v[8:11], v[136:139], 0\n" \
  "ds_read_b64_tr_b16 v[82:83], v224 offset:9728\n" \
  "s_lshr_b32 s97, s95, 1\n" \
  "s_add_u32 m0, s97, 49152\n" \
  "s_nop 0\n" \
  "buffer_load_dwordx4 v240, s[80:83], s94 offen lds\n" \
  "v_mfma_f32_16x16x32_bf16 a[40:43], v[8:11], v[144:147], 0\n" \
  "ds_read_b64_tr_b16 v[84:85], v225 offset:8704\n" \
  "v_mfma_f32_16x16x32_bf16 a[44:47], v[8:11], v[152:155], 0\n" \
  "ds_read_b64_tr_b16 v[86:87], v225 offset:9728\n" \
  "buffer_load_dwordx4 v241, s[80:83], s94 offen offset:1024 lds\n" \
  "v_mfma_f32_16x16x32_bf16 a[48:51], v[12:15], v[128:131], 0\n" \
  "ds_read_b64_tr_b16 v[88:89], v224 offset:8960\n" \
  "v_mfma_f32_16x16x32_bf16 a[52:55], v[12:15], v[136:139], 0\n" \
  "ds_read_b64_tr_b16 v[90:91], v224 offset:9984\n" \
  "s_add_u32 m0, s97, 57344\n" \
  "s_nop 0\n" \
  "buffer_load_dwordx4 v240, s[84:87], s94 offen lds\n" \
  "v_mfma_f32_16x16x32_bf16 a[56:59], v[12:15], v[144:147], 0\n" \
  "ds_read_b64_tr_b16 v[92:93], v225 offset:8960\n" \
  "v_mfma_f32_16x16x32_bf16 a[60:63], v[12:15], v[152:155], 0\n" \
  "ds_read_b64_tr_b16 v[94:95], v225 offset:9984\n" \
  "buffer_load_dwordx4 v241, s[84:87], s94 offen offset:1024 lds\n" \
  "s_add_u32 s93, s93, 0x80000\n" \
  "s_add_u32 s94, s94, 229376\n" \
  "v_mfma_f32_16x16x32_bf16 a[64:67], v[16:19], v[128:131], 0\n" \
  "ds_read_b64_tr_b16 v[96:97], v228 offset:4096\n" \
  "v_mfma_f32_16x16x32_bf16 a[68:71], v[16:19], v[136:139], 0\n" \
  "ds_read_b64_tr_b16 v[98:99], v228 offset:4608\n" \
  "buffer_load_dwordx4 v[160:163], v232, s[72:75], s92 offen offset:0\n" \
  "v_mfma_f32_16x16x32_bf16 a[72:75], v[16:19], v[144:147], 0\n" \
  "ds_read_b64_tr_b16 v[100:101], v229 offset:4096\n" \
  "v_mfma_f32_16x16x32_bf16 a[76:79], v[16:19], v[152:155], 0\n" \
  "ds_read_b64_tr_b16 v[102:103], v229 offset:4608\n" \
  "buffer_load_dwordx4 v[168:171], v233, s[72:75], s92 offen offset:0\n" \
  "v_mfma_f32_16x16x32_bf16 a[80:83], v[20:23], v[128:131], 0\n" \
  "ds_read_b64_tr_b16 v[104:105], v228 offset:4352\n" \
  "v_mfma_f32_16x16x32_bf16 a[84:87], v[20:23], v[136:139], 0\n" \
  "ds_read_b64_tr_b16 v[106:107], v228 offset:4864\n" \
  "buffer_load_dwordx4 v[176:179], v234, s[72:75], s92 offen offset:0\n" \
  "v_mfma_f32_16x16x32_bf16 a[88:91], v[20:23], v[144:147], 0\n" \
  "ds_read_b64_tr_b16 v[108:109], v229 offset:4352\n" \
  "v_mfma_f32_16x16x32_bf16 a[92:95], v[20:23], v[152:155], 0\n" \
  "ds_read_b64_tr_b16 v[110:111], v229 offset:4864\n" \
  "buffer_load_dwordx4 v[184:187], v235, s[72:75], s92 offen offset:0\n" \
  "v_mfma_f32_16x16x32_bf16 a[96:99], v[24:27], v[128:131], 0\n" \
  "ds_read_b64_tr_b16 v[112:113], v228 offset:12288\n" \
  "v_mfma_f32_16x16x32_bf16 a[100:103], v[24:27], v[136:139], 0\n" \
  "ds_read_b64_tr_b16 v[114:115], v228 offset:12800\n" \
  "buffer_load_dwordx4 v[164:167], v232, s[72:75], s92 offen offset:1024\n" \
  "v_mfma_f32_16x16x32_bf16 a[104:107], v[24:27], v[144:147], 0\n" \
  "ds_read_b64_tr_b16 v[116:117], v229 offset:12288\n" \
  "v_mfma_f32_16x16x32_bf16 a[108:111], v[24:27], v[152:155], 0\n" \
  "ds_read_b64_tr_b16 v[118:119], v229 offset:12800\n" \
  "buffer_load_dwordx4 v[172:175], v233, s[72:75], s92 offen offset:1024\n" \
  "v_mfma_f32_16x16x32_bf16 a[112:115], v[28:31], v[128:131], 0\n" \
  "ds_read_b64_tr_b16 v[120:121], v228 offset:12544\n" \
  "v_mfma_f32_16x16x32_bf16 a[116:119], v[28:31], v[136:139], 0\n" \
  "ds_read_b64_tr_b16 v[122:123], v228 offset:13056\n" \
  "buffer_load_dwordx4 v[180:183], v234, s[72:75], s92 offen offset:1024\n" \
  "v_mfma_f32_16x16x32_bf16 a[120:123], v[28:31], v[144:147], 0\n" \
  "ds_read_b64_tr_b16 v[124:125], v229 offset:12544\n" \
  "v_mfma_f32_16x16x32_bf16 a[124:127], v[28:31], v[152:155], 0\n" \
  "ds_read_b64_tr_b16 v[126:127], v229 offset:13056\n" \
  "buffer_load_dwordx4 v[188:191], v235, s[72:75], s92 offen offset:1024\n" \
  "s_add_u32 s92, s92, 2048\n" \
  "v_mfma_f32_16x16x32_bf16 a[128:131], v[32:35], v[128:131], 0\n" \
  "v_mfma_f32_16x16x32_bf16 a[132:135], v[32:35], v[136:139], 0\n" \
  "s_add_u32 m0, s95, 65536\n" \
  "s_nop 0\n" \
  "buffer_load_dwordx4 v236, s[76:79], s93 offen lds\n" \
  "v_mfma_f32_16x16x32_bf16 a[136:139], v[32:35], v[144:147], 0\n" \
  "v_mfma_f32_16x16x32_bf16 a[140:143], v[32:35], v[152:155], 0\n" \
  "buffer_load_dwordx4 v237, s[76:79], s93 offen offset:1024 lds\n" \
  "v_mfma_f32_16x16x32_bf16 a[144:147], v[36:39], v[128:131], 0\n" \
  "v_mfma_f32_16x16x32_bf16 a[148:151], v[36:39], v[136:139], 0\n" \
  "buffer_load_dwordx4 v238, s[76:79], s93 offen offset:2048 lds\n" \
  "v_mfma_f32_16x16x32_bf16 a[152:155], v[36:39], v[144:147], 0\n" \
  "v_mfma_f32_16x16x32_bf16 a[156:159], v[36:39], v[152:155], 0\n" \
  "buffer_load_dwordx4 v239, s[76:79], s93 offen offset:3072 lds\n" \
  "v_mfma_f32_16x16x32_bf16 a[160:163], v[40:43], v[128:131], 0\n" \
  "v_mfma_f32_16x16x32_bf16 a[164:167], v[40:43], v[136:139], 0\n" \
  "s_lshr_b32 s97, s95, 1\n" \
  "s_add_u32 m0, s97, 81920\n" \
  "s_nop 0\n" \
  "buffer_load_dwordx4 v240, s[80:83], s94 offen lds\n" \
  "v_mfma_f32_16x16x32_bf16 a[168:171], v[40:43], v[144:147], 0\n" \
  "v_mfma_f32_16x16x32_bf16 a[172:175], v[40:43], v[152:155], 0\n" \
  "buffer_load_dwordx4 v241, s[80:83], s94 offen offset:1024 lds\n" \
  "v_mfma_f32_16x16x32_bf16 a[176:179], v[44:47], v[128:131], 0\n" \
  "v_mfma_f32_16x16x32_bf16 a[180:183], v[44:47], v[136:139], 0\n" \
  "s_add_u32 m0, s97, 90112\n" \
  "s_nop 0\n" \
  "buffer_load_dwordx4 v240, s[84:87], s94 offen lds\n" \
  "v_mfma_f32_16x16x32_bf16 a[184:187], v[44:47], v[144:147], 0\n" \
  "v_mfma_f32_16x16x32_bf16 a[188:191], v[44:47], v[152:155], 0\n" \
  "buffer_load_dwordx4 v241, s[84:87], s94 offen offset:1024 lds\n" \
  "s_add_u32 s93, s93, 0x80000\n" \
  "s_add_u32 s94, s94, 229376\n" \
  "v_mfma_f32_16x16x32_bf16 a[192:195], v[48:51], v[128:131], 0\n" \
  "v_mfma_f32_16x16x32_bf16 a[196:199], v[48:51], v[136:139], 0\n" \
  "buffer_load_dwordx4 v[192:195], v232, s[72:75], s92 offen offset:0\n" \
  "v_mfma_f32_16x16x32_bf16 a[200:203], v[48:51], v[144:147], 0\n" \
  "v_mfma_f32_16x16x32_bf16 a[204:207], v[48:51], v[152:155], 0\n" \
  "buffer_load_dwordx4 v[200:203], v233, s[72:75], s92 offen offset:0\n" \
  "v_mfma_f32_16x16x32_bf16 a[208:211], v[52:55], v[128:131], 0\n" \
  "v_mfma_f32_16x16x32_bf16 a[212:215], v[52:55], v[136:139], 0\n" \
  "buffer_load_dwordx4 v[208:211], v234, s[72:75], s92 offen offset:0\n" \
  "v_mfma_f32_16x16x32_bf16 a[216:219], v[52:55], v[144:147], 0\n" \
  "v_mfma_f32_16x16x32_bf16 a[220:223], v[52:55], v[152:155], 0\n" \
  "buffer_load_dwordx4 v[216:219], v235, s[72:75], s92 offen offset:0\n" \
  "v_mfma_f32_16x16x32_bf16 a[224:227], v[56:59], v[128:131], 0\n" \
  "v_mfma_f32_16x16x32_bf16 a[228:231], v[56:59], v[136:139], 0\n" \
  "buffer_load_dwordx4 v[196:199], v232, s[72:75], s92 offen offset:1024\n" \
  "v_mfma_f32_16x16x32_bf16 a[232:235], v[56:59], v[144:147], 0\n" \
  "v_mfma_f32_16x16x32_bf16 a[236:239], v[56:59], v[152:155], 0\n" \
  "buffer_load_dwordx4 v[204:207], v233, s[72:75], s92 offen offset:1024\n" \
  "v_mfma_f32_16x16x32_bf16 a[240:243], v[60:63], v[128:131], 0\n" \
  "v_mfma_f32_16x16x32_bf16 a[244:247], v[60:63], v[136:139], 0\n" \
  "buffer_load_dwordx4 v[212:215], v234, s[72:75], s92 offen offset:1024\n" \
  "v_mfma_f32_16x16x32_bf16 a[248:251], v[60:63], v[144:147], 0\n" \
  "v_mfma_f32_16x16x32_bf16 a[252:255], v[60:63], v[152:155], 0\n" \
  "buffer_load_dwordx4 v[220:223], v235, s[72:75], s92 offen offset:1024\n" \
  "s_add_u32 s92, s92, 2048\n" \
  "s_waitcnt lgkmcnt(0)\n" \
  "v_mfma_f32_16x16x32_bf16 a[0:3], v[64:67], v[132:135], a[0:3]\n" \
  "v_mfma_f32_16x16x32_bf16 a[4:7], v[64:67], v[140:143], a[4:7]\n" \
  "v_mfma_f32_16x16x32_bf16 a[8:11], v[64:67], v[148:151], a[8:11]\n" \
  "v_mfma_f32_16x16x32_bf16 a[12:15], v[64:67], v[156:159], a[12:15]\n" \
  "v_mfma_f32_16x16x32_bf16 a[16:19], v[68:71], v[132:135], a[16:19]\n" \
  "v_mfma_f32_16x16x32_bf16 a[20:23], v[68:71], v[140:143], a[20:23]\n" \
  "v_mfma_f32_16x16x32_bf16 a[24:27], v[68:71], v[148:151], a[24:27]\n" \
  "v_mfma_f32_16x16x32_bf16 a[28:31], v[68:71], v[156:159], a[28:31]\n" \
  "s_waitcnt vmcnt(24)\n" \
  "s_barrier\n" \
  "v_mfma_f32_16x16x32_bf16 a[32:35], v[72:75], v[132:135], a[32:35]\n" \
  "ds_read_b64_tr_b16 v[0:1], v224 offset:32768\n" \
  "v_mfma_f32_16x16x32_bf16 a[36:39], v[72:75], v[140:143], a[36:39]\n" \
  "ds_read_b64_tr_b16 v[2:3], v224 offset:33792\n" \
  "v_mfma_f32_16x16x32_bf16 a[40:43], v[72:75], v[148:151], a[40:43]\n" \
  "ds_read_b64_tr_b16 v[4:5], v225 offset:32768\n" \
  "v_mfma_f32_16x16x32_bf16 a[44:47], v[72:75], v[156:159], a[44:47]\n" \
  "ds_read_b64_tr_b16 v[6:7], v225 offset:33792\n" \
  "v_mfma_f32_16x16x32_bf16 a[48:51], v[76:79], v[132:135], a[48:51]\n" \
  "ds_read_b64_tr_b16 v[8:9], v224 offset:33024\n" \
  "v_mfma_f32_16x16x32_bf16 a[52:55], v[76:79], v[140:143], a[52:55]\n" \
  "ds_read_b64_tr_b16 v[10:11], v224 offset:34048\n" \
  "v_mfma_f32_16x16x32_bf16 a[56:59], v[76:79], v[148:151], a[56:59]\n" \
  "ds_read_b64_tr_b16 v[12:13], v225 offset:33024\n" \
  "v_mfma_f32_16x16x32_bf16 a[60:63], v[76:79], v[156:159], a[60:63]\n" \
  "ds_read_b64_tr_b16 v[14:15], v225 offset:34048\n" \
  "v_mfma_f32_16x16x32_bf16 a[64:67], v[80:83], v[132:135], a[64:67]\n" \
  "s_add_u32 m0, s95, 98304\n" \
  "s_nop 0\n" \
  "buffer_load_dwordx4 v236, s[76:79], s93 offen lds\n" \
  "v_mfma_f32_16x16x32_bf16 a[68:71], v[80:83], v[140:143], a[68:71]\n" \
  "ds_read_b64_tr_b16 v[16:17], v224 offset:33280\n" \
  "v_mfma_f32_16x16x32_bf16 a[72:75], v[80:83], v[148:151], a[72:75]\n" \
  "buffer_load_dwordx4 v237, s[76:79], s93 offen offset:1024 lds\n" \
  "v_mfma_f32_16x16x32_bf16 a[76:79], v[80:83], v[156:159], a[76:79]\n" \
  "ds_read_b64_tr_b16 v[18:19], v224 offset:34304\n" \
  "v_mfma_f32_16x16x32_bf16 a[80:83], v[84:87], v[132:135], a[80:83]\n" \
  "buffer_load_dwordx4 v238, s[76:79], s93 offen offset:2048 lds\n" \
  "v_mfma_f32_16x16x32_bf16 a[84:87], v[84:87], v[140:143], a[84:87]\n" \
  "ds_read_b64_tr_b16 v[20:21], v225 offset:33280\n" \
  "v_mfma_f32_16x16x32_bf16 a[88:91], v[84:87], v[148:151], a[88:91]\n" \
  "buffer_load_dwordx4 v239, s[76:79], s93 offen offset:3072 lds\n" \
  "v_mfma_f32_16x16x32_bf16 a[92:95], v[84:87], v[156:159], a[92:95]\n" \
  "ds_read_b64_tr_b16 v[22:23], v225 offset:34304\n" \
  "v_mfma_f32_16x16x32_bf16 a[96:99], v[88:91], v[132:135], a[96:99]\n" \
  "s_lshr_b32 s97, s95, 1\n" \
  "s_add_u32 m0, s97, 114688\n" \
  "s_nop 0\n" \
  "buffer_load_dwordx4 v240, s[80:83], s94 offen lds\n" \
  "v_mfma_f32_16x16x32_bf16 a[100:103], v[88:91], v[140:143], a[100:103]\n" \
  "ds_read_b64_tr_b16 v[24:25], v224 offset:33536\n" \
  "v_mfma_f32_16x16x32_bf16 a[104:107], v[88:91], v[148:151], a[104:107]\n" \
  "buffer_load_dwordx4 v241, s[80:83], s94 offen offset:1024 lds\n" \
  "v_mfma_f32_16x16x32_bf16 a[108:111], v[88:91], v[156:159], a[108:111]\n" \
  "ds_read_b64_tr_b16 v[26:27], v224 offset:34560\n" \
  "v_mfma_f32_16x16x32_bf16 a[112:115], v[92:95], v[132:135], a[112:115]\n" \
  "s_add_u32 m0, s97, 122880\n" \
  "s_nop 0\n" \
  "buffer_load_dwordx4 v240, s[84:87], s94 offen lds\n" \
  "v_mfma_f32_16x16x32_bf16 a[116:119], v[92:95], v[140:143], a[116:119]\n" \
  "ds_read_b64_tr_b16 v[28:29], v225 offset:33536\n" \
  "v_mfma_f32_16x16x32_bf16 a[120:123], v[92:95], v[148:151], a[120:123]\n" \
  "buffer_load_dwordx4 v241, s[84:87], s94 offen offset:1024 lds\n" \
  "s_add_u32 s93, s93, 0x80000\n" \
  "s_add_u32 s94, s94, 229376\n" \
  "v_mfma_f32_16x16x32_bf16 a[124:127], v[92:95], v[156:159], a[124:127]\n" \
  "ds_read_b64_tr_b16 v[30:31], v225 offset:34560\n" \
  "v_mfma_f32_16x16x32_bf16 a[128:131], v[96:99], v[132:135], a[128:131]\n" \
  "s_add_u32 m0, s95, 0\n" \
  "s_nop 0\n" \
  "buffer_load_dwordx4 v236, s[76:79], s93 offen lds\n" \
  "v_mfma_f32_16x16x32_bf16 a[132:135], v[96:99], v[140:143], a[132:135]\n" \
  "ds_read_b64_tr_b16 v[32:33], v228 offset:32768\n" \
  "v_mfma_f32_16x16x32_bf16 a[136:139], v[96:99], v[148:151], a[136:139]\n" \
  "buffer_load_dwordx4 v237, s[76:79], s93 offen offset:1024 lds\n" \
  "v_mfma_f32_16x16x32_bf16 a[140:143], v[96:99], v[156:159], a[140:143]\n" \
  "ds_read_b64_tr_b16 v[34:35], v228 offset:33280\n" \
  "v_mfma_f32_16x16x32_bf16 a[144:147], v[100:103], v[132:135], a[144:147]\n" \
  "buffer_load_dwordx4 v238, s[76:79], s93 offen offset:2048 lds\n" \
  "v_mfma_f32_16x16x32_bf16 a[148:151], v[100:103], v[140:143], a[148:151]\n" \
  "ds_read_b64_tr_b16 v[36:37], v229 offset:32768\n" \
  "v_mfma_f32_16x16x32_bf16 a[152:155], v[100:103], v[148:151], a[152:155]\n" \
  "buffer_load_dwordx4 v239, s[76:79], s93 offen offset:3072 lds\n" \
  "v_mfma_f32_16x16x32_bf16 a[156:159], v[100:103], v[156:159], a[156:159]\n" \
  "ds_read_b64_tr_b16 v[38:39], v229 offset:33280\n" \
  "v_mfma_f32_16x16x32_bf16 a[160:163], v[104:107], v[132:135], a[160:163]\n" \
  "s_lshr_b32 s97, s95, 1\n" \
  "s_add_u32 m0, s97, 16384\n" \
  "s_nop 0\n" \
  "buffer_load_dwordx4 v240, s[80:83], s94 offen lds\n" \
  "v_mfma_f32_16x16x32_bf16 a[164:167], v[104:107], v[140:143], a[164:167]\n" \
  "ds_read_b64_tr_b16 v[40:41], v228 offset:33024\n" \
  "v_mfma_f32_16x16x32_bf16 a[168:171], v[104:107], v[148:151], a[168:171]\n" \
  "buffer_load_dwordx4 v241, s[80:83], s94 offen offset:1024 lds\n" \
  "v_mfma_f32_16x16x32_bf16 a[172:175], v[104:107], v[156:159], a[172:175]\n" \
  "ds_read_b64_tr_b16 v[42:43], v228 offset:33536\n" \
  "v_mfma_f32_16x16x32_bf16 a[176:179], v[108:111], v[132:135], a[176:179]\n" \
  "s_add_u32 m0, s97, 24576\n" \
  "s_nop 0\n" \
  "buffer_load_dwordx4 v240, s[84:87], s94 offen lds\n" \
  "v_mfma_f32_16x16x32_bf16 a[180:183], v[108:111], v[140:143], a[180:183]\n" \
  "ds_read_b64_tr_b16 v[44:45], v229 offset:33024\n" \
  "v_mfma_f32_16x16x32_bf16 a[184:187], v[108:111], v[148:151], a[184:187]\n" \
  "buffer_load_dwordx4 v241, s[84:87], s94 offen offset:1024 lds\n" \
  "s_add_u32 s93, s93, 0x80000\n" \
  "s_add_u32 s94, s94, 229376\n" \
  "v_mfma_f32_16x16x32_bf16 a[188:191], v[108:111], v[156:159], a[188:191]\n" \
  "ds_read_b64_tr_b16 v[46:47], v229 offset:33536\n" \
  "v_mfma_f32_16x16x32_bf16 a[192:195], v[112:115], v[132:135], a[192:195]\n" \
  "ds_read_b64_tr_b16 v[48:49], v228 offset:40960\n" \
  "v_mfma_f32_16x16x32_bf16 a[196:199], v[112:115], v[140:143], a[196:199]\n" \
  "ds_read_b64_tr_b16 v[50:51], v228 offset:41472\n" \
  "v_mfma_f32_16x16x32_bf16 a[200:203], v[112:115], v[148:151], a[200:203]\n" \
  "ds_read_b64_tr_b16 v[52:53], v229 offset:40960\n" \
  "v_mfma_f32_16x16x32_bf16 a[204:207], v[112:115], v[156:159], a[204:207]\n" \
  "ds_read_b64_tr_b16 v[54:55], v229 offset:41472\n" \
  "v_mfma_f32_16x16x32_bf16 a[208:211], v[116:119], v[132:135], a[208:211]\n" \
  "ds_read_b64_tr_b16 v[56:57], v228 offset:41216\n" \
  "v_mfma_f32_16x16x32_bf16 a[212:215], v[116:119], v[140:143], a[212:215]\n" \
  "ds_read_b64_tr_b16 v[58:59], v228 offset:41728\n" \
  "v_mfma_f32_16x16x32_bf16 a[216:219], v[116:119], v[148:151], a[216:219]\n" \
  "ds_read_b64_tr_b16 v[60:61], v229 offset:41216\n" \
  "v_mfma_f32_16x16x32_bf16 a[220:223], v[116:119], v[156:159], a[220:223]\n" \
  "ds_read_b64_tr_b16 v[62:63], v229 offset:41728\n" \
  "v_mfma_f32_16x16x32_bf16 a[224:227], v[120:123], v[132:135], a[224:227]\n" \
  "v_mfma_f32_16x16x32_bf16 a[228:231], v[120:123], v[140:143], a[228:231]\n" \
  "v_mfma_f32_16x16x32_bf16 a[232:235], v[120:123], v[148:151], a[232:235]\n" \
  "v_mfma_f32_16x16x32_bf16 a[236:239], v[120:123], v[156:159], a[236:239]\n" \
  "v_mfma_f32_16x16x32_bf16 a[240:243], v[124:127], v[132:135], a[240:243]\n" \
  "v_mfma_f32_16x16x32_bf16 a[244:247], v[124:127], v[140:143], a[244:247]\n" \
  "v_mfma_f32_16x16x32_bf16 a[248:251], v[124:127], v[148:151], a[248:251]\n" \
  "v_mfma_f32_16x16x32_bf16 a[252:255], v[124:127], v[156:159], a[252:255]\n" \
  "s_waitcnt vmcnt(32) lgkmcnt(0)\n" \
  "v_mfma_f32_16x16x32_bf16 a[0:3], v[0:3], v[160:163], a[0:3]\n" \
  "ds_read_b64_tr_b16 v[64:65], v224 offset:40960\n" \
  "v_mfma_f32_16x16x32_bf16 a[4:7], v[0:3], v[168:171], a[4:7]\n" \
  "ds_read_b64_tr_b16 v[66:67], v224 offset:41984\n" \
  "v_mfma_f32_16x16x32_bf16 a[8:11], v[0:3], v[176:179], a[8:11]\n" \
  "ds_read_b64_tr_b16 v[68:69], v225 offset:40960\n" \
  "v_mfma_f32_16x16x32_bf16 a[12:15], v[0:3], v[184:187], a[12:15]\n" \
  "ds_read_b64_tr_b16 v[70:71], v225 offset:41984\n" \
  "v_mfma_f32_16x16x32_bf16 a[16:19], v[4:7], v[160:163], a[16:19]\n" \
  "ds_read_b64_tr_b16 v[72:73], v224 offset:41216\n" \
  "v_mfma_f32_16x16x32_bf16 a[20:23], v[4:7], v[168:171], a[20:23]\n" \
  "ds_read_b64_tr_b16 v[74:75], v224 offset:42240\n" \
  "v_mfma_f32_16x16x32_bf16 a[24:27], v[4:7], v[176:179], a[24:27]\n" \
  "ds_read_b64_tr_b16 v[76:77], v225 offset:41216\n" \
  "v_mfma_f32_16x16x32_bf16 a[28:31], v[4:7], v[184:187], a[28:31]\n" \
  "ds_read_b64_tr_b16 v[78:79], v225 offset:42240\n" \
  "v_mfma_f32_16x16x32_bf16 a[32:35], v[8:11], v[160:163], a[32:35]\n" \
  "ds_read_b64_tr_b16 v[80:81], v224 offset:41472\n" \
  "v_mfma_f32_16x16x32_bf16 a[36:39], v[8:11], v[168:171], a[36:39]\n" \
  "ds_read_b64_tr_b16 v[82:83], v224 offset:42496\n" \
  "v_mfma_f32_16x16x32_bf16 a[40:43], v[8:11], v[176:179], a[40:43]\n" \
  "ds_read_b64_tr_b16 v[84:85], v225 offset:41472\n" \
  "v_mfma_f32_16x16x32_bf16 a[44:47], v[8:11], v[184:187], a[44:47]\n" \
  "ds_read_b64_tr_b16 v[86:87], v225 offset:42496\n" \
  "v_mfma_f32_16x16x32_bf16 a[48:51], v[12:15], v[160:163], a[48:51]\n" \
  "ds_read_b64_tr_b16 v[88:89], v224 offset:41728\n" \
  "v_mfma_f32_16x16x32_bf16 a[52:55], v[12:15], v[168:171], a[52:55]\n" \
  "ds_read_b64_tr_b16 v[90:91], v224 offset:42752\n" \
  "v_mfma_f32_16x16x32_bf16 a[56:59], v[12:15], v[176:179], a[56:59]\n" \
  "ds_read_b64_tr_b16 v[92:93], v225 offset:41728\n" \
  "v_mfma_f32_16x16x32_bf16 a[60:63], v[12:15], v[184:187], a[60:63]\n" \
  "ds_read_b64_tr_b16 v[94:95], v225 offset:42752\n" \
  "v_mfma_f32_16x16x32_bf16 a[64:67], v[16:19], v[160:163], a[64:67]\n" \
  "ds_read_b64_tr_b16 v[96:97], v228 offset:36864\n" \
  "v_mfma_f32_16x16x32_bf16 a[68:71], v[16:19], v[168:171], a[68:71]\n" \
  "ds_read_b64_tr_b16 v[98:99], v228 offset:37376\n" \
  "v_mfma_f32_16x16x32_bf16 a[72:75], v[16:19], v[176:179], a[72:75]\n" \
  "ds_read_b64_tr_b16 v[100:101], v229 offset:36864\n" \
  "v_mfma_f32_16x16x32_bf16 a[76:79], v[16:19], v[184:187], a[76:79]\n" \
  "ds_read_b64_tr_b16 v[102:103], v229 offset:37376\n" \
  "v_mfma_f32_16x16x32_bf16 a[80:83], v[20:23], v[160:163], a[80:83]\n" \
  "ds_read_b64_tr_b16 v[104:105], v228 offset:37120\n" \
  "v_mfma_f32_16x16x32_bf16 a[84:87], v[20:23], v[168:171], a[84:87]\n" \
  "ds_read_b64_tr_b16 v[106:107], v228 offset:37632\n" \
  "v_mfma_f32_16x16x32_bf16 a[88:91], v[20:23], v[176:179], a[88:91]\n" \
  "ds_read_b64_tr_b16 v[108:109], v229 offset:37120\n" \
  "v_mfma_f32_16x16x32_bf16 a[92:95], v[20:23], v[184:187], a[92:95]\n" \
  "ds_read_b64_tr_b16 v[110:111], v229 offset:37632\n" \
  "v_mfma_f32_16x16x32_bf16 a[96:99], v[24:27], v[160:163], a[96:99]\n" \
  "ds_read_b64_tr_b16 v[112:113], v228 offset:45056\n" \
  "v_mfma_f32_16x16x32_bf16 a[100:103], v[24:27], v[168:171], a[100:103]\n" \
  "ds_read_b64_tr_b16 v[114:115], v228 offset:45568\n" \
  "v_mfma_f32_16x16x32_bf16 a[104:107], v[24:27], v[176:179], a[104:107]\n" \
  "ds_read_b64_tr_b16 v[116:117], v229 offset:45056\n" \
  "v_mfma_f32_16x16x32_bf16 a[108:111], v[24:27], v[184:187], a[108:111]\n" \
  "ds_read_b64_tr_b16 v[118:119], v229 offset:45568\n" \
  "v_mfma_f32_16x16x32_bf16 a[112:115], v[28:31], v[160:163], a[112:115]\n" \
  "ds_read_b64_tr_b16 v[120:121], v228 offset:45312\n" \
  "v_mfma_f32_16x16x32_bf16 a[116:119], v[28:31], v[168:171], a[116:119]\n" \
  "ds_read_b64_tr_b16 v[122:123], v228 offset:45824\n" \
  "v_mfma_f32_16x16x32_bf16 a[120:123], v[28:31], v[176:179], a[120:123]\n" \
  "ds_read_b64_tr_b16 v[124:125], v229 offset:45312\n" \
  "v_mfma_f32_16x16x32_bf16 a[124:127], v[28:31], v[184:187], a[124:127]\n" \
  "ds_read_b64_tr_b16 v[126:127], v229 offset:45824\n" \
  "v_mfma_f32_16x16x32_bf16 a[128:131], v[32:35], v[160:163], a[128:131]\n" \
  "buffer_load_dwordx4 v[128:131], v232, s[72:75], s92 offen offset:0\n" \
  "v_mfma_f32_16x16x32_bf16 a[132:135], v[32:35], v[168:171], a[132:135]\n" \
  "v_mfma_f32_16x16x32_bf16 a[136:139], v[32:35], v[176:179], a[136:139]\n" \
  "buffer_load_dwordx4 v[136:139], v233, s[72:75], s92 offen offset:0\n" \
  "v_mfma_f32_16x16x32_bf16 a[140:143], v[32:35], v[184:187], a[140:143]\n" \
  "v_mfma_f32_16x16x32_bf16 a[144:147], v[36:39], v[160:163], a[144:147]\n" \
  "buffer_load_dwordx4 v[144:147], v234, s[72:75], s92 offen offset:0\n" \
  "v_mfma_f32_16x16x32_bf16 a[148:151], v[36:39], v[168:171], a[148:151]\n" \
  "v_mfma_f32_16x16x32_bf16 a[152:155], v[36:39], v[176:179], a[152:155]\n" \
  "buffer_load_dwordx4 v[152:155], v235, s[72:75], s92 offen offset:0\n" \
  "v_mfma_f32_16x16x32_bf16 a[156:159], v[36:39], v[184:187], a[156:159]\n" \
  "v_mfma_f32_16x16x32_bf16 a[160:163], v[40:43], v[160:163], a[160:163]\n" \
  "buffer_load_dwordx4 v[132:135], v232, s[72:75], s92 offen offset:1024\n" \
  "v_mfma_f32_16x16x32_bf16 a[164:167], v[40:43], v[168:171], a[164:167]\n" \
  "v_mfma_f32_16x16x32_bf16 a[168:171], v[40:43], v[176:179], a[168:171]\n" \
  "buffer_load_dwordx4 v[140:143], v233, s[72:75], s92 offen offset:1024\n" \
  "v_mfma_f32_16x16x32_bf16 a[172:175], v[40:43], v[184:187], a[172:175]\n" \
  "v_mfma_f32_16x16x32_bf16 a[176:179], v[44:47], v[160:163], a[176:179]\n" \
  "buffer_load_dwordx4 v[148:151], v234, s[72:75], s92 offen offset:1024\n" \
  "v_mfma_f32_16x16x32_bf16 a[180:183], v[44:47], v[168:171], a[180:183]\n" \
  "v_mfma_f32_16x16x32_bf16 a[184:187], v[44:47], v[176:179], a[184:187]\n" \
  "buffer_load_dwordx4 v[156:159], v235, s[72:75], s92 offen offset:1024\n" \
  "s_add_u32 s92, s92, 2048\n" \
  "v_mfma_f32_16x16x32_bf16 a[188:191], v[44:47], v[184:187], a[188:191]\n" \
  "v_mfma_f32_16x16x32_bf16 a[192:195], v[48:51], v[160:163], a[192:195]\n" \
  "v_mfma_f32_16x16x32_bf16 a[196:199], v[48:51], v[168:171], a[196:199]\n" \
  "v_mfma_f32_16x16x32_bf16 a[200:203], v[48:51], v[176:179], a[200:203]\n" \
  "v_mfma_f32_16x16x32_bf16 a[204:207], v[48:51], v[184:187], a[204:207]\n" \
  "v_mfma_f32_16x16x32_bf16 a[208:211], v[52:55], v[160:163], a[208:211]\n" \
  "v_mfma_f32_16x16x32_bf16 a[212:215], v[52:55], v[168:171], a[212:215]\n" \
  "v_mfma_f32_16x16x32_bf16 a[216:219], v[52:55], v[176:179], a[216:219]\n" \
  "v_mfma_f32_16x16x32_bf16 a[220:223], v[52:55], v[184:187], a[220:223]\n" \
  "v_mfma_f32_16x16x32_bf16 a[224:227], v[56:59], v[160:163], a[224:227]\n" \
  "v_mfma_f32_16x16x32_bf16 a[228:231], v[56:59], v[168:171], a[228:231]\n" \
  "v_mfma_f32_16x16x32_bf16 a[232:235], v[56:59], v[176:179], a[232:235]\n" \
  "v_mfma_f32_16x16x32_bf16 a[236:239], v[56:59], v[184:187], a[236:239]\n" \
  "v_mfma_f32_16x16x32_bf16 a[240:243], v[60:63], v[160:163], a[240:243]\n" \
  "v_mfma_f32_16x16x32_bf16 a[244:247], v[60:63], v[168:171], a[244:247]\n" \
  "v_mfma_f32_16x16x32_bf16 a[248:251], v[60:63], v[176:179], a[248:251]\n" \
  "v_mfma_f32_16x16x32_bf16 a[252:255], v[60:63], v[184:187], a[252:255]\n" \
  "s_waitcnt lgkmcnt(0)\n" \
  "v_mfma_f32_16x16x32_bf16 a[0:3], v[64:67], v[164:167], a[0:3]\n" \
  "v_mfma_f32_16x16x32_bf16 a[4:7], v[64:67], v[172:175], a[4:7]\n" \
  "v_mfma_f32_16x16x32_bf16 a[8:11], v[64:67], v[180:183], a[8:11]\n" \
  "v_mfma_f32_16x16x32_bf16 a[12:15], v[64:67], v[188:191], a[12:15]\n" \
  "v_mfma_f32_16x16x32_bf16 a[16:19], v[68:71], v[164:167], a[16:19]\n" \
  "v_mfma_f32_16x16x32_bf16 a[20:23], v[68:71], v[172:175], a[20:23]\n" \
  "v_mfma_f32_16x16x32_bf16 a[24:27], v[68:71], v[180:183], a[24:27]\n" \
  "v_mfma_f32_16x16x32_bf16 a[28:31], v[68:71], v[188:191], a[28:31]\n" \
  "s_waitcnt vmcnt(32)\n" \
  "s_barrier\n" \
  "v_mfma_f32_16x16x32_bf16 a[32:35], v[72:75], v[164:167], a[32:35]\n" \
  "ds_read_b64_tr_b16 v[0:1], v226 offset:0\n" \
  "v_mfma_f32_16x16x32_bf16 a[36:39], v[72:75], v[172:175], a[36:39]\n" \
  "ds_read_b64_tr_b16 v[2:3], v226 offset:1024\n" \
  "v_mfma_f32_16x16x32_bf16 a[40:43], v[72:75], v[180:183], a[40:43]\n" \
  "ds_read_b64_tr_b16 v[4:5], v227 offset:0\n" \
  "v_mfma_f32_16x16x32_bf16 a[44:47], v[72:75], v[188:191], a[44:47]\n" \
  "ds_read_b64_tr_b16 v[6:7], v227 offset:1024\n" \
  "v_mfma_f32_16x16x32_bf16 a[48:51], v[76:79], v[164:167], a[48:51]\n" \
  "ds_read_b64_tr_b16 v[8:9], v226 offset:256\n" \
  "v_mfma_f32_16x16x32_bf16 a[52:55], v[76:79], v[172:175], a[52:55]\n" \
  "ds_read_b64_tr_b16 v[10:11], v226 offset:1280\n" \
  "v_mfma_f32_16x16x32_bf16 a[56:59], v[76:79], v[180:183], a[56:59]\n" \
  "ds_read_b64_tr_b16 v[12:13], v227 offset:256\n" \
  "v_mfma_f32_16x16x32_bf16 a[60:63], v[76:79], v[188:191], a[60:63]\n" \
  "ds_read_b64_tr_b16 v[14:15], v227 offset:1280\n" \
  "v_mfma_f32_16x16x32_bf16 a[64:67], v[80:83], v[164:167], a[64:67]\n" \
  "s_add_u32 m0, s95, 32768\n" \
  "s_nop 0\n" \
  "buffer_load_dwordx4 v236, s[76:79], s93 offen lds\n" \
  "v_mfma_f32_16x16x32_bf16 a[68:71], v[80:83], v[172:175], a[68:71]\n" \
  "ds_read_b64_tr_b16 v[16:17], v226 offset:512\n" \
  "v_mfma_f32_16x16x32_bf16 a[72:75], v[80:83], v[180:183], a[72:75]\n" \
  "ds_read_b64_tr_b16 v[18:19], v226 offset:1536\n" \
  "v_mfma_f32_16x16x32_bf16 a[76:79], v[80:83], v[188:191], a[76:79]\n" \
  "buffer_load_dwordx4 v237, s[76:79], s93 offen offset:1024 lds\n" \
  "v_mfma_f32_16x16x32_bf16 a[80:83], v[84:87], v[164:167], a[80:83]\n" \
  "ds_read_b64_tr_b16 v[20:21], v227 offset:512\n" \
  "v_mfma_f32_16x16x32_bf16 a[84:87], v[84:87], v[172:175], a[84:87]\n" \
  "ds_read_b64_tr_b16 v[22:23], v227 offset:1536\n" \
  "v_mfma_f32_16x16x32_bf16 a[88:91], v[84:87], v[180:183], a[88:91]\n" \
  "buffer_load_dwordx4 v238, s[76:79], s93 offen offset:2048 lds\n" \
  "v_mfma_f32_16x16x32_bf16 a[92:95], v[84:87], v[188:191], a[92:95]\n" \
  "ds_read_b64_tr_b16 v[24:25], v226 offset:768\n" \
  "v_mfma_f32_16x16x32_bf16 a[96:99], v[88:91], v[164:167], a[96:99]\n" \
  "ds_read_b64_tr_b16 v[26:27], v226 offset:1792\n" \
  "v_mfma_f32_16x16x32_bf16 a[100:103], v[88:91], v[172:175], a[100:103]\n" \
  "buffer_load_dwordx4 v239, s[76:79], s93 offen offset:3072 lds\n" \
  "v_mfma_f32_16x16x32_bf16 a[104:107], v[88:91], v[180:183], a[104:107]\n" \
  "ds_read_b64_tr_b16 v[28:29], v227 offset:768\n" \
  "v_mfma_f32_16x16x32_bf16 a[108:111], v[88:91], v[188:191], a[108:111]\n" \
  "ds_read_b64_tr_b16 v[30:31], v227 offset:1792\n" \
  "v_mfma_f32_16x16x32_bf16 a[112:115], v[92:95], v[164:167], a[112:115]\n" \
  "s_lshr_b32 s97, s95, 1\n" \
  "s_add_u32 m0, s97, 49152\n" \
  "s_nop 0\n" \
  "buffer_load_dwordx4 v240, s[80:83], s94 offen lds\n" \
  "v_mfma_f32_16x16x32_bf16 a[116:119], v[92:95], v[172:175], a[116:119]\n" \
  "ds_read_b64_tr_b16 v[32:33], v230 offset:0\n" \
  "v_mfma_f32_16x16x32_bf16 a[120:123], v[92:95], v[180:183], a[120:123]\n" \
  "ds_read_b64_tr_b16 v[34:35], v230 offset:512\n" \
  "v_mfma_f32_16x16x32_bf16 a[124:127], v[92:95], v[188:191], a[124:127]\n" \
  "buffer_load_dwordx4 v241, s[80:83], s94 offen offset:1024 lds\n" \
  "v_mfma_f32_16x16x32_bf16 a[128:131], v[96:99], v[164:167], a[128:131]\n" \
  "ds_read_b64_tr_b16 v[36:37], v231 offset:0\n" \
  "v_mfma_f32_16x16x32_bf16 a[132:135], v[96:99], v[172:175], a[132:135]\n" \
  "ds_read_b64_tr_b16 v[38:39], v231 offset:512\n" \
  "v_mfma_f32_16x16x32_bf16 a[136:139], v[96:99], v[180:183], a[136:139]\n" \
  "s_add_u32 m0, s97, 57344\n" \
  "s_nop 0\n" \
  "buffer_load_dwordx4 v240, s[84:87], s94 offen lds\n" \
  "v_mfma_f32_16x16x32_bf16 a[140:143], v[96:99], v[188:191], a[140:143]\n" \
  "ds_read_b64_tr_b16 v[40:41], v230 offset:256\n" \
  "v_mfma_f32_16x16x32_bf16 a[144:147], v[100:103], v[164:167], a[144:147]\n" \
  "ds_read_b64_tr_b16 v[42:43], v230 offset:768\n" \
  "v_mfma_f32_16x16x32_bf16 a[148:151], v[100:103], v[172:175], a[148:151]\n" \
  "buffer_load_dwordx4 v241, s[84:87], s94 offen offset:1024 lds\n" \
  "s_add_u32 s93, s93, 0x80000\n" \
  "s_add_u32 s94, s94, 229376\n" \
  "v_mfma_f32_16x16x32_bf16 a[152:155], v[100:103], v[180:183], a[152:155]\n" \
  "ds_read_b64_tr_b16 v[44:45], v231 offset:256\n" \
  "v_mfma_f32_16x16x32_bf16 a[156:159], v[100:103], v[188:191], a[156:159]\n" \
  "ds_read_b64_tr_b16 v[46:47], v231 offset:768\n" \
  "v_mfma_f32_16x16x32_bf16 a[160:163], v[104:107], v[164:167], a[160:163]\n" \
  "ds_read_b64_tr_b16 v[48:49], v230 offset:8192\n" \
  "v_mfma_f32_16x16x32_bf16 a[164:167], v[104:107], v[172:175], a[164:167]\n" \
  "ds_read_b64_tr_b16 v[50:51], v230 offset:8704\n" \
  "v_mfma_f32_16x16x32_bf16 a[168:171], v[104:107], v[180:183], a[168:171]\n" \
  "ds_read_b64_tr_b16 v[52:53], v231 offset:8192\n" \
  "v_mfma_f32_16x16x32_bf16 a[172:175], v[104:107], v[188:191], a[172:175]\n" \
  "ds_read_b64_tr_b16 v[54:55], v231 offset:8704\n" \
  "v_mfma_f32_16x16x32_bf16 a[176:179], v[108:111], v[164:167], a[176:179]\n" \
  "ds_read_b64_tr_b16 v[56:57], v230 offset:8448\n" \
  "v_mfma_f32_16x16x32_bf16 a[180:183], v[108:111], v[172:175], a[180:183]\n" \
  "ds_read_b64_tr_b16 v[58:59], v230 offset:8960\n" \
  "v_mfma_f32_16x16x32_bf16 a[184:187], v[108:111], v[180:183], a[184:187]\n" \
  "ds_read_b64_tr_b16 v[60:61], v231 offset:8448\n" \
  "v_mfma_f32_16x16x32_bf16 a[188:191], v[108:111], v[188:191], a[188:191]\n" \
  "ds_read_b64_tr_b16 v[62:63], v231 offset:8960\n" \
  "v_mfma_f32_16x16x32_bf16 a[192:195], v[112:115], v[164:167], a[192:195]\n" \
  "v_mfma_f32_16x16x32_bf16 a[196:199], v[112:115], v[172:175], a[196:199]\n" \
  "v_mfma_f32_16x16x32_bf16 a[200:203], v[112:115], v[180:183], a[200:203]\n" \
  "v_mfma_f32_16x16x32_bf16 a[204:207], v[112:115], v[188:191], a[204:207]\n" \
  "v_mfma_f32_16x16x32_bf16 a[208:211], v[116:119], v[164:167], a[208:211]\n" \
  "v_mfma_f32_16x16x32_bf16 a[212:215], v[116:119], v[172:175], a[212:215]\n" \
  "v_mfma_f32_16x16x32_bf16 a[216:219], v[116:119], v[180:183], a[216:219]\n" \
  "v_mfma_f32_16x16x32_bf16 a[220:223], v[116:119], v[188:191], a[220:223]\n" \
  "v_mfma_f32_16x16x32_bf16 a[224:227], v[120:123], v[164:167], a[224:227]\n" \
  "v_mfma_f32_16x16x32_bf16 a[228:231], v[120:123], v[172:175], a[228:231]\n" \
  "v_mfma_f32_16x16x32_bf16 a[232:235], v[120:123], v[180:183], a[232:235]\n" \
  "v_mfma_f32_16x16x32_bf16 a[236:239], v[120:123], v[188:191], a[236:239]\n" \
  "v_mfma_f32_16x16x32_bf16 a[240:243], v[124:127], v[164:167], a[240:243]\n" \
  "v_mfma_f32_16x16x32_bf16 a[244:247], v[124:127], v[172:175], a[244:247]\n" \
  "v_mfma_f32_16x16x32_bf16 a[248:251], v[124:127], v[180:183], a[248:251]\n" \
  "v_mfma_f32_16x16x32_bf16 a[252:255], v[124:127], v[188:191], a[252:255]\n" \
  "s_waitcnt vmcnt(32) lgkmcnt(0)\n" \
  "v_mfma_f32_16x16x32_bf16 a[0:3], v[0:3], v[192:195], a[0:3]\n" \
  "ds_read_b64_tr_b16 v[64:65], v226 offset:8192\n" \
  "v_mfma_f32_16x16x32_bf16 a[4:7], v[0:3], v[200:203], a[4:7]\n" \
  "ds_read_b64_tr_b16 v[66:67], v226 offset:9216\n" \
  "v_mfma_f32_16x16x32_bf16 a[8:11], v[0:3], v[208:211], a[8:11]\n" \
  "ds_read_b64_tr_b16 v[68:69], v227 offset:8192\n" \
  "v_mfma_f32_16x16x32_bf16 a[12:15], v[0:3], v[216:219], a[12:15]\n" \
  "ds_read_b64_tr_b16 v[70:71], v227 offset:9216\n" \
  "v_mfma_f32_16x16x32_bf16 a[16:19], v[4:7], v[192:195], a[16:19]\n" \
  "ds_read_b64_tr_b16 v[72:73], v226 offset:8448\n" \
  "v_mfma_f32_16x16x32_bf16 a[20:23], v[4:7], v[200:203], a[20:23]\n" \
  "ds_read_b64_tr_b16 v[74:75], v226 offset:9472\n" \
  "v_mfma_f32_16x16x32_bf16 a[24:27], v[4:7], v[208:211], a[24:27]\n" \
  "ds_read_b64_tr_b16 v[76:77], v227 offset:8448\n" \
  "v_mfma_f32_16x16x32_bf16 a[28:31], v[4:7], v[216:219], a[28:31]\n" \
  "ds_read_b64_tr_b16 v[78:79], v227 offset:9472\n" \
  "v_mfma_f32_16x16x32_bf16 a[32:35], v[8:11], v[192:195], a[32:35]\n" \
  "ds_read_b64_tr_b16 v[80:81], v226 offset:8704\n" \
  "v_mfma_f32_16x16x32_bf16 a[36:39], v[8:11], v[200:203], a[36:39]\n" \
  "ds_read_b64_tr_b16 v[82:83], v226 offset:9728\n" \
  "v_mfma_f32_16x16x32_bf16 a[40:43], v[8:11], v[208:211], a[40:43]\n" \
  "ds_read_b64_tr_b16 v[84:85], v227 offset:8704\n" \
  "v_mfma_f32_16x16x32_bf16 a[44:47], v[8:11], v[216:219], a[44:47]\n" \
  "ds_read_b64_tr_b16 v[86:87], v227 offset:9728\n" \
  "v_mfma_f32_16x16x32_bf16 a[48:51], v[12:15], v[192:195], a[48:51]\n" \
  "ds_read_b64_tr_b16 v[88:89], v226 offset:8960\n" \
  "v_mfma_f32_16x16x32_bf16 a[52:55], v[12:15], v[200:203], a[52:55]\n" \
  "ds_read_b64_tr_b16 v[90:91], v226 offset:9984\n" \
  "v_mfma_f32_16x16x32_bf16 a[56:59], v[12:15], v[208:211], a[56:59]\n" \
  "ds_read_b64_tr_b16 v[92:93], v227 offset:8960\n" \
  "v_mfma_f32_16x16x32_bf16 a[60:63], v[12:15], v[216:219], a[60:63]\n" \
  "ds_read_b64_tr_b16 v[94:95], v227 offset:9984\n" \
  "v_mfma_f32_16x16x32_bf16 a[64:67], v[16:19], v[192:195], a[64:67]\n" \
  "ds_read_b64_tr_b16 v[96:97], v230 offset:4096\n" \
  "v_mfma_f32_16x16x32_bf16 a[68:71], v[16:19], v[200:203], a[68:71]\n" \
  "ds_read_b64_tr_b16 v[98:99], v230 offset:4608\n" \
  "v_mfma_f32_16x16x32_bf16 a[72:75], v[16:19], v[208:211], a[72:75]\n" \
  "ds_read_b64_tr_b16 v[100:101], v231 offset:4096\n" \
  "v_mfma_f32_16x16x32_bf16 a[76:79], v[16:19], v[216:219], a[76:79]\n" \
  "ds_read_b64_tr_b16 v[102:103], v231 offset:4608\n" \
  "v_mfma_f32_16x16x32_bf16 a[80:83], v[20:23], v[192:195], a[80:83]\n" \
  "ds_read_b64_tr_b16 v[104:105], v230 offset:4352\n" \
  "v_mfma_f32_16x16x32_bf16 a[84:87], v[20:23], v[200:203], a[84:87]\n" \
  "ds_read_b64_tr_b16 v[106:107], v230 offset:4864\n" \
  "v_mfma_f32_16x16x32_bf16 a[88:91], v[20:23], v[208:211], a[88:91]\n" \
  "ds_read_b64_tr_b16 v[108:109], v231 offset:4352\n" \
  "v_mfma_f32_16x16x32_bf16 a[92:95], v[20:23], v[216:219], a[92:95]\n" \
  "ds_read_b64_tr_b16 v[110:111], v231 offset:4864\n" \
  "v_mfma_f32_16x16x32_bf16 a[96:99], v[24:27], v[192:195], a[96:99]\n" \
  "ds_read_b64_tr_b16 v[112:113], v230 offset:12288\n" \
  "v_mfma_f32_16x16x32_bf16 a[100:103], v[24:27], v[200:203], a[100:103]\n" \
  "ds_read_b64_tr_b16 v[114:115], v230 offset:12800\n" \
  "v_mfma_f32_16x16x32_bf16 a[104:107], v[24:27], v[208:211], a[104:107]\n" \
  "ds_read_b64_tr_b16 v[116:117], v231 offset:12288\n" \
  "v_mfma_f32_16x16x32_bf16 a[108:111], v[24:27], v[216:219], a[108:111]\n" \
  "ds_read_b64_tr_b16 v[118:119], v231 offset:12800\n" \
  "v_mfma_f32_16x16x32_bf16 a[112:115], v[28:31], v[192:195], a[112:115]\n" \
  "ds_read_b64_tr_b16 v[120:121], v230 offset:12544\n" \
  "v_mfma_f32_16x16x32_bf16 a[116:119], v[28:31], v[200:203], a[116:119]\n" \
  "ds_read_b64_tr_b16 v[122:123], v230 offset:13056\n" \
  "v_mfma_f32_16x16x32_bf16 a[120:123], v[28:31], v[208:211], a[120:123]\n" \
  "ds_read_b64_tr_b16 v[124:125], v231 offset:12544\n" \
  "v_mfma_f32_16x16x32_bf16 a[124:127], v[28:31], v[216:219], a[124:127]\n" \
  "ds_read_b64_tr_b16 v[126:127], v231 offset:13056\n" \
  "v_mfma_f32_16x16x32_bf16 a[128:131], v[32:35], v[192:195], a[128:131]\n" \
  "buffer_load_dwordx4 v[160:163], v232, s[72:75], s92 offen offset:0\n" \
  "v_mfma_f32_16x16x32_bf16 a[132:135], v[32:35], v[200:203], a[132:135]\n" \
  "v_mfma_f32_16x16x32_bf16 a[136:139], v[32:35], v[208:211], a[136:139]\n" \
  "buffer_load_dwordx4 v[168:171], v233, s[72:75], s92 offen offset:0\n" \
  "v_mfma_f32_16x16x32_bf16 a[140:143], v[32:35], v[216:219], a[140:143]\n" \
  "v_mfma_f32_16x16x32_bf16 a[144:147], v[36:39], v[192:195], a[144:147]\n" \
  "buffer_load_dwordx4 v[176:179], v234, s[72:75], s92 offen offset:0\n" \
  "v_mfma_f32_16x16x32_bf16 a[148:151], v[36:39], v[200:203], a[148:151]\n" \
  "v_mfma_f32_16x16x32_bf16 a[152:155], v[36:39], v[208:211], a[152:155]\n" \
  "buffer_load_dwordx4 v[184:187], v235, s[72:75], s92 offen offset:0\n" \
  "v_mfma_f32_16x16x32_bf16 a[156:159], v[36:39], v[216:219], a[156:159]\n" \
  "v_mfma_f32_16x16x32_bf16 a[160:163], v[40:43], v[192:195], a[160:163]\n" \
  "buffer_load_dwordx4 v[164:167], v232, s[72:75], s92 offen offset:1024\n" \
  "v_mfma_f32_16x16x32_bf16 a[164:167], v[40:43], v[200:203], a[164:167]\n" \
  "v_mfma_f32_16x16x32_bf16 a[168:171], v[40:43], v[208:211], a[168:171]\n" \
  "buffer_load_dwordx4 v[172:175], v233, s[72:75], s92 offen offset:1024\n" \
  "v_mfma_f32_16x16x32_bf16 a[172:175], v[40:43], v[216:219], a[172:175]\n" \
  "v_mfma_f32_16x16x32_bf16 a[176:179], v[44:47], v[192:195], a[176:179]\n" \
  "buffer_load_dwordx4 v[180:183], v234, s[72:75], s92 offen offset:1024\n" \
  "v_mfma_f32_16x16x32_bf16 a[180:183], v[44:47], v[200:203], a[180:183]\n" \
  "v_mfma_f32_16x16x32_bf16 a[184:187], v[44:47], v[208:211], a[184:187]\n" \
  "buffer_load_dwordx4 v[188:191], v235, s[72:75], s92 offen offset:1024\n" \
  "s_add_u32 s92, s92, 2048\n" \
  "v_mfma_f32_16x16x32_bf16 a[188:191], v[44:47], v[216:219], a[188:191]\n" \
  "v_mfma_f32_16x16x32_bf16 a[192:195], v[48:51], v[192:195], a[192:195]\n" \
  "v_mfma_f32_16x16x32_bf16 a[196:199], v[48:51], v[200:203], a[196:199]\n" \
  "v_mfma_f32_16x16x32_bf16 a[200:203], v[48:51], v[208:211], a[200:203]\n" \
  "v_mfma_f32_16x16x32_bf16 a[204:207], v[48:51], v[216:219], a[204:207]\n" \
  "v_mfma_f32_16x16x32_bf16 a[208:211], v[52:55], v[192:195], a[208:211]\n" \
  "v_mfma_f32_16x16x32_bf16 a[212:215], v[52:55], v[200:203], a[212:215]\n" \
  "v_mfma_f32_16x16x32_bf16 a[216:219], v[52:55], v[208:211], a[216:219]\n" \
  "v_mfma_f32_16x16x32_bf16 a[220:223], v[52:55], v[216:219], a[220:223]\n" \
  "v_mfma_f32_16x16x32_bf16 a[224:227], v[56:59], v[192:195], a[224:227]\n" \
  "v_mfma_f32_16x16x32_bf16 a[228:231], v[56:59], v[200:203], a[228:231]\n" \
  "v_mfma_f32_16x16x32_bf16 a[232:235], v[56:59], v[208:211], a[232:235]\n" \
  "v_mfma_f32_16x16x32_bf16 a[236:239], v[56:59], v[216:219], a[236:239]\n" \
  "v_mfma_f32_16x16x32_bf16 a[240:243], v[60:63], v[192:195], a[240:243]\n" \
  "v_mfma_f32_16x16x32_bf16 a[244:247], v[60:63], v[200:203], a[244:247]\n" \
  "v_mfma_f32_16x16x32_bf16 a[248:251], v[60:63], v[208:211], a[248:251]\n" \
  "v_mfma_f32_16x16x32_bf16 a[252:255], v[60:63], v[216:219], a[252:255]\n" \
  "s_waitcnt lgkmcnt(0)\n" \
  "v_mfma_f32_16x16x32_bf16 a[0:3], v[64:67], v[196:199], a[0:3]\n" \
  "v_mfma_f32_16x16x32_bf16 a[4:7], v[64:67], v[204:207], a[4:7]\n" \
  "v_mfma_f32_16x16x32_bf16 a[8:11], v[64:67], v[212:215], a[8:11]\n" \
  "v_mfma_f32_16x16x32_bf16 a[12:15], v[64:67], v[220:223], a[12:15]\n" \
  "v_mfma_f32_16x16x32_bf16 a[16:19], v[68:71], v[196:199], a[16:19]\n" \
  "v_mfma_f32_16x16x32_bf16 a[20:23], v[68:71], v[204:207], a[20:23]\n" \
  "v_mfma_f32_16x16x32_bf16 a[24:27], v[68:71], v[212:215], a[24:27]\n" \
  "v_mfma_f32_16x16x32_bf16 a[28:31], v[68:71], v[220:223], a[28:31]\n" \
  "s_waitcnt vmcnt(32)\n" \
  "s_barrier\n" \
  "v_mfma_f32_16x16x32_bf16 a[32:35], v[72:75], v[196:199], a[32:35]\n" \
  "ds_read_b64_tr_b16 v[0:1], v226 offset:32768\n" \
  "v_mfma_f32_16x16x32_bf16 a[36:39], v[72:75], v[204:207], a[36:39]\n" \
  "ds_read_b64_tr_b16 v[2:3], v226 offset:33792\n" \
  "v_mfma_f32_16x16x32_bf16 a[40:43], v[72:75], v[212:215], a[40:43]\n" \
  "ds_read_b64_tr_b16 v[4:5], v227 offset:32768\n" \
  "v_mfma_f32_16x16x32_bf16 a[44:47], v[72:75], v[220:223], a[44:47]\n" \
  "ds_read_b64_tr_b16 v[6:7], v227 offset:33792\n" \
  "v_mfma_f32_16x16x32_bf16 a[48:51], v[76:79], v[196:199], a[48:51]\n" \
  "ds_read_b64_tr_b16 v[8:9], v226 offset:33024\n" \
  "v_mfma_f32_16x16x32_bf16 a[52:55], v[76:79], v[204:207], a[52:55]\n" \
  "ds_read_b64_tr_b16 v[10:11], v226 offset:34048\n" \
  "v_mfma_f32_16x16x32_bf16 a[56:59], v[76:79], v[212:215], a[56:59]\n" \
  "ds_read_b64_tr_b16 v[12:13], v227 offset:33024\n" \
  "v_mfma_f32_16x16x32_bf16 a[60:63], v[76:79], v[220:223], a[60:63]\n" \
  "ds_read_b64_tr_b16 v[14:15], v227 offset:34048\n" \
  "v_mfma_f32_16x16x32_bf16 a[64:67], v[80:83], v[196:199], a[64:67]\n" \
  "s_add_u32 m0, s95, 65536\n" \
  "s_nop 0\n" \
  "buffer_load_dwordx4 v236, s[76:79], s93 offen lds\n" \
  "v_mfma_f32_16x16x32_bf16 a[68:71], v[80:83], v[204:207], a[68:71]\n" \
  "ds_read_b64_tr_b16 v[16:17], v226 offset:33280\n" \
  "v_mfma_f32_16x16x32_bf16 a[72:75], v[80:83], v[212:215], a[72:75]\n" \
  "ds_read_b64_tr_b16 v[18:19], v226 offset:34304\n" \
  "v_mfma_f32_16x16x32_bf16 a[76:79], v[80:83], v[220:223], a[76:79]\n" \
  "buffer_load_dwordx4 v237, s[76:79], s93 offen offset:1024 lds\n" \
  "v_mfma_f32_16x16x32_bf16 a[80:83], v[84:87], v[196:199], a[80:83]\n" \
  "ds_read_b64_tr_b16 v[20:21], v227 offset:33280\n" \
  "v_mfma_f32_16x16x32_bf16 a[84:87], v[84:87], v[204:207], a[84:87]\n" \
  "ds_read_b64_tr_b16 v[22:23], v227 offset:34304\n" \
  "v_mfma_f32_16x16x32_bf16 a[88:91], v[84:87], v[212:215], a[88:91]\n" \
  "buffer_load_dwordx4 v238, s[76:79], s93 offen offset:2048 lds\n" \
  "v_mfma_f32_16x16x32_bf16 a[92:95], v[84:87], v[220:223], a[92:95]\n" \
  "ds_read_b64_tr_b16 v[24:25], v226 offset:33536\n" \
  "v_mfma_f32_16x16x32_bf16 a[96:99], v[88:91], v[196:199], a[96:99]\n" \
  "ds_read_b64_tr_b16 v[26:27], v226 offset:34560\n" \
  "v_mfma_f32_16x16x32_bf16 a[100:103], v[88:91], v[204:207], a[100:103]\n" \
  "buffer_load_dwordx4 v239, s[76:79], s93 offen offset:3072 lds\n" \
  "v_mfma_f32_16x16x32_bf16 a[104:107], v[88:91], v[212:215], a[104:107]\n" \
  "ds_read_b64_tr_b16 v[28:29], v227 offset:33536\n" \
  "v_mfma_f32_16x16x32_bf16 a[108:111], v[88:91], v[220:223], a[108:111]\n" \
  "ds_read_b64_tr_b16 v[30:31], v227 offset:34560\n" \
  "v_mfma_f32_16x16x32_bf16 a[112:115], v[92:95], v[196:199], a[112:115]\n" \
  "s_lshr_b32 s97, s95, 1\n" \
  "s_add_u32 m0, s97, 81920\n" \
  "s_nop 0\n" \
  "buffer_load_dwordx4 v240, s[80:83], s94 offen lds\n" \
  "v_mfma_f32_16x16x32_bf16 a[116:119], v[92:95], v[204:207], a[116:119]\n" \
  "ds_read_b64_tr_b16 v[32:33], v230 offset:32768\n" \
  "v_mfma_f32_16x16x32_bf16 a[120:123], v[92:95], v[212:215], a[120:123]\n" \
  "ds_read_b64_tr_b16 v[34:35], v230 offset:33280\n" \
  "v_mfma_f32_16x16x32_bf16 a[124:127], v[92:95], v[220:223], a[124:127]\n" \
  "buffer_load_dwordx4 v241, s[80:83], s94 offen offset:1024 lds\n" \
  "v_mfma_f32_16x16x32_bf16 a[128:131], v[96:99], v[196:199], a[128:131]\n" \
  "ds_read_b64_tr_b16 v[36:37], v231 offset:32768\n" \
  "v_mfma_f32_16x16x32_bf16 a[132:135], v[96:99], v[204:207], a[132:135]\n" \
  "ds_read_b64_tr_b16 v[38:39], v231 offset:33280\n" \
  "v_mfma_f32_16x16x32_bf16 a[136:139], v[96:99], v[212:215], a[136:139]\n" \
  "s_add_u32 m0, s97, 90112\n" \
  "s_nop 0\n" \
  "buffer_load_dwordx4 v240, s[84:87], s94 offen lds\n" \
  "v_mfma_f32_16x16x32_bf16 a[140:143], v[96:99], v[220:223], a[140:143]\n" \
  "ds_read_b64_tr_b16 v[40:41], v230 offset:33024\n" \
  "v_mfma_f32_16x16x32_bf16 a[144:147], v[100:103], v[196:199], a[144:147]\n" \
  "ds_read_b64_tr_b16 v[42:43], v230 offset:33536\n" \
  "v_mfma_f32_16x16x32_bf16 a[148:151], v[100:103], v[204:207], a[148:151]\n" \
  "buffer_load_dwordx4 v241, s[84:87], s94 offen offset:1024 lds\n" \
  "s_add_u32 s93, s93, 0x80000\n" \
  "s_add_u32 s94, s94, 229376\n" \
  "v_mfma_f32_16x16x32_bf16 a[152:155], v[100:103], v[212:215], a[152:155]\n" \
  "ds_read_b64_tr_b16 v[44:45], v231 offset:33024\n" \
  "v_mfma_f32_16x16x32_bf16 a[156:159], v[100:103], v[220:223], a[156:159]\n" \
  "ds_read_b64_tr_b16 v[46:47], v231 offset:33536\n" \
  "v_mfma_f32_16x16x32_bf16 a[160:163], v[104:107], v[196:199], a[160:163]\n" \
  "ds_read_b64_tr_b16 v[48:49], v230 offset:40960\n" \
  "v_mfma_f32_16x16x32_bf16 a[164:167], v[104:107], v[204:207], a[164:167]\n" \
  "ds_read_b64_tr_b16 v[50:51], v230 offset:41472\n" \
  "v_mfma_f32_16x16x32_bf16 a[168:171], v[104:107], v[212:215], a[168:171]\n" \
  "ds_read_b64_tr_b16 v[52:53], v231 offset:40960\n" \
  "v_mfma_f32_16x16x32_bf16 a[172:175], v[104:107], v[220:223], a[172:175]\n" \
  "ds_read_b64_tr_b16 v[54:55], v231 offset:41472\n" \
  "v_mfma_f32_16x16x32_bf16 a[176:179], v[108:111], v[196:199], a[176:179]\n" \
  "ds_read_b64_tr_b16 v[56:57], v230 offset:41216\n" \
  "v_mfma_f32_16x16x32_bf16 a[180:183], v[108:111], v[204:207], a[180:183]\n" \
  "ds_read_b64_tr_b16 v[58:59], v230 offset:41728\n" \
  "v_mfma_f32_16x16x32_bf16 a[184:187], v[108:111], v[212:215], a[184:187]\n" \
  "ds_read_b64_tr_b16 v[60:61], v231 offset:41216\n" \
  "v_mfma_f32_16x16x32_bf16 a[188:191], v[108:111], v[220:223], a[188:191]\n" \
  "ds_read_b64_tr_b16 v[62:63], v231 offset:41728\n" \
  "v_mfma_f32_16x16x32_bf16 a[192:195], v[112:115], v[196:199], a[192:195]\n" \
  "v_mfma_f32_16x16x32_bf16 a[196:199], v[112:115], v[204:207], a[196:199]\n" \
  "v_mfma_f32_16x16x32_bf16 a[200:203], v[112:115], v[212:215], a[200:203]\n" \
  "v_mfma_f32_16x16x32_bf16 a[204:207], v[112:115], v[220:223], a[204:207]\n" \
  "v_mfma_f32_16x16x32_bf16 a[208:211], v[116:119], v[196:199], a[208:211]\n" \
  "v_mfma_f32_16x16x32_bf16 a[212:215], v[116:119], v[204:207], a[212:215]\n" \
  "v_mfma_f32_16x16x32_bf16 a[216:219], v[116:119], v[212:215], a[216:219]\n" \
  "v_mfma_f32_16x16x32_bf16 a[220:223], v[116:119], v[220:223], a[220:223]\n" \
  "v_mfma_f32_16x16x32_bf16 a[224:227], v[120:123], v[196:199], a[224:227]\n" \
  "v_mfma_f32_16x16x32_bf16 a[228:231], v[120:123], v[204:207], a[228:231]\n" \
  "v_mfma_f32_16x16x32_bf16 a[232:235], v[120:123], v[212:215], a[232:235]\n" \
  "v_mfma_f32_16x16x32_bf16 a[236:239], v[120:123], v[220:223], a[236:239]\n" \
  "v_mfma_f32_16x16x32_bf16 a[240:243], v[124:127], v[196:199], a[240:243]\n" \
  "v_mfma_f32_16x16x32_bf16 a[244:247], v[124:127], v[204:207], a[244:247]\n" \
  "v_mfma_f32_16x16x32_bf16 a[248:251], v[124:127], v[212:215], a[248:251]\n" \
  "v_mfma_f32_16x16x32_bf16 a[252:255], v[124:127], v[220:223], a[252:255]\n" \
  "s_waitcnt vmcnt(24) lgkmcnt(0)\n" \
  "v_mfma_f32_16x16x32_bf16 a[0:3], v[0:3], v[128:131], a[0:3]\n" \
  "ds_read_b64_tr_b16 v[64:65], v226 offset:40960\n" \
  "v_mfma_f32_16x16x32_bf16 a[4:7], v[0:3], v[136:139], a[4:7]\n" \
  "ds_read_b64_tr_b16 v[66:67], v226 offset:41984\n" \
  "v_mfma_f32_16x16x32_bf16 a[8:11], v[0:3], v[144:147], a[8:11]\n" \
  "ds_read_b64_tr_b16 v[68:69], v227 offset:40960\n" \
  "v_mfma_f32_16x16x32_bf16 a[12:15], v[0:3], v[152:155], a[12:15]\n" \
  "ds_read_b64_tr_b16 v[70:71], v227 offset:41984\n" \
  "v_mfma_f32_16x16x32_bf16 a[16:19], v[4:7], v[128:131], a[16:19]\n" \
  "ds_read_b64_tr_b16 v[72:73], v226 offset:41216\n" \
  "v_mfma_f32_16x16x32_bf16 a[20:23], v[4:7], v[136:139], a[20:23]\n" \
  "ds_read_b64_tr_b16 v[74:75], v226 offset:42240\n" \
  "v_mfma_f32_16x16x32_bf16 a[24:27], v[4:7], v[144:147], a[24:27]\n" \
  "ds_read_b64_tr_b16 v[76:77], v227 offset:41216\n" \
  "v_mfma_f32_16x16x32_bf16 a[28:31], v[4:7], v[152:155], a[28:31]\n" \
  "ds_read_b64_tr_b16 v[78:79], v227 offset:42240\n" \
  "v_mfma_f32_16x16x32_bf16 a[32:35], v[8:11], v[128:131], a[32:35]\n" \
  "ds_read_b64_tr_b16 v[80:81], v226 offset:41472\n" \
  "v_mfma_f32_16x16x32_bf16 a[36:39], v[8:11], v[136:139], a[36:39]\n" \
  "ds_read_b64_tr_b16 v[82:83], v226 offset:42496\n" \
  "v_mfma_f32_16x16x32_bf16 a[40:43], v[8:11], v[144:147], a[40:43]\n" \
  "ds_read_b64_tr_b16 v[84:85], v227 offset:41472\n" \
  "v_mfma_f32_16x16x32_bf16 a[44:47], v[8:11], v[152:155], a[44:47]\n" \
  "ds_read_b64_tr_b16 v[86:87], v227 offset:42496\n" \
  "v_mfma_f32_16x16x32_bf16 a[48:51], v[12:15], v[128:131], a[48:51]\n" \
  "ds_read_b64_tr_b16 v[88:89], v226 offset:41728\n" \
  "v_mfma_f32_16x16x32_bf16 a[52:55], v[12:15], v[136:139], a[52:55]\n" \
  "ds_read_b64_tr_b16 v[90:91], v226 offset:42752\n" \
  "v_mfma_f32_16x16x32_bf16 a[56:59], v[12:15], v[144:147], a[56:59]\n" \
  "ds_read_b64_tr_b16 v[92:93], v227 offset:41728\n" \
  "v_mfma_f32_16x16x32_bf16 a[60:63], v[12:15], v[152:155], a[60:63]\n" \
  "ds_read_b64_tr_b16 v[94:95], v227 offset:42752\n" \
  "v_mfma_f32_16x16x32_bf16 a[64:67], v[16:19], v[128:131], a[64:67]\n" \
  "ds_read_b64_tr_b16 v[96:97], v230 offset:36864\n" \
  "v_mfma_f32_16x16x32_bf16 a[68:71], v[16:19], v[136:139], a[68:71]\n" \
  "ds_read_b64_tr_b16 v[98:99], v230 offset:37376\n" \
  "v_mfma_f32_16x16x32_bf16 a[72:75], v[16:19], v[144:147], a[72:75]\n" \
  "ds_read_b64_tr_b16 v[100:101], v231 offset:36864\n" \
  "v_mfma_f32_16x16x32_bf16 a[76:79], v[16:19], v[152:155], a[76:79]\n" \
  "ds_read_b64_tr_b16 v[102:103], v231 offset:37376\n" \
  "v_mfma_f32_16x16x32_bf16 a[80:83], v[20:23], v[128:131], a[80:83]\n" \
  "ds_read_b64_tr_b16 v[104:105], v230 offset:37120\n" \
  "v_mfma_f32_16x16x32_bf16 a[84:87], v[20:23], v[136:139], a[84:87]\n" \
  "ds_read_b64_tr_b16 v[106:107], v230 offset:37632\n" \
  "v_mfma_f32_16x16x32_bf16 a[88:91], v[20:23], v[144:147], a[88:91]\n" \
  "ds_read_b64_tr_b16 v[108:109], v231 offset:37120\n" \
  "v_mfma_f32_16x16x32_bf16 a[92:95], v[20:23], v[152:155], a[92:95]\n" \
  "ds_read_b64_tr_b16 v[110:111], v231 offset:37632\n" \
  "v_mfma_f32_16x16x32_bf16 a[96:99], v[24:27], v[128:131], a[96:99]\n" \
  "ds_read_b64_tr_b16 v[112:113], v230 offset:45056\n" \
  "v_mfma_f32_16x16x32_bf16 a[100:103], v[24:27], v[136:139], a[100:103]\n" \
  "ds_read_b64_tr_b16 v[114:115], v230 offset:45568\n" \
  "v_mfma_f32_16x16x32_bf16 a[104:107], v[24:27], v[144:147], a[104:107]\n" \
  "ds_read_b64_tr_b16 v[116:117], v231 offset:45056\n" \
  "v_mfma_f32_16x16x32_bf16 a[108:111], v[24:27], v[152:155], a[108:111]\n" \
  "ds_read_b64_tr_b16 v[118:119], v231 offset:45568\n" \
  "v_mfma_f32_16x16x32_bf16 a[112:115], v[28:31], v[128:131], a[112:115]\n" \
  "ds_read_b64_tr_b16 v[120:121], v230 offset:45312\n" \
  "v_mfma_f32_16x16x32_bf16 a[116:119], v[28:31], v[136:139], a[116:119]\n" \
  "ds_read_b64_tr_b16 v[122:123], v230 offset:45824\n" \
  "v_mfma_f32_16x16x32_bf16 a[120:123], v[28:31], v[144:147], a[120:123]\n" \
  "ds_read_b64_tr_b16 v[124:125], v231 offset:45312\n" \
  "v_mfma_f32_16x16x32_bf16 a[124:127], v[28:31], v[152:155], a[124:127]\n" \
  "ds_read_b64_tr_b16 v[126:127], v231 offset:45824\n" \
  "v_mfma_f32_16x16x32_bf16 a[128:131], v[32:35], v[128:131], a[128:131]\n" \
  "buffer_load_dwordx4 v[192:195], v232, s[72:75], s92 offen offset:0\n" \
  "v_mfma_f32_16x16x32_bf16 a[132:135], v[32:35], v[136:139], a[132:135]\n" \
  "v_mfma_f32_16x16x32_bf16 a[136:139], v[32:35], v[144:147], a[136:139]\n" \
  "buffer_load_dwordx4 v[200:203], v233, s[72:75], s92 offen offset:0\n" \
  "v_mfma_f32_16x16x32_bf16 a[140:143], v[32:35], v[152:155], a[140:143]\n" \
  "v_mfma_f32_16x16x32_bf16 a[144:147], v[36:39], v[128:131], a[144:147]\n" \
  "buffer_load_dwordx4 v[208:211], v234, s[72:75], s92 offen offset:0\n" \
  "v_mfma_f32_16x16x32_bf16 a[148:151], v[36:39], v[136:139], a[148:151]\n" \
  "v_mfma_f32_16x16x32_bf16 a[152:155], v[36:39], v[144:147], a[152:155]\n" \
  "buffer_load_dwordx4 v[216:219], v235, s[72:75], s92 offen offset:0\n" \
  "v_mfma_f32_16x16x32_bf16 a[156:159], v[36:39], v[152:155], a[156:159]\n" \
  "v_mfma_f32_16x16x32_bf16 a[160:163], v[40:43], v[128:131], a[160:163]\n" \
  "buffer_load_dwordx4 v[196:199], v232, s[72:75], s92 offen offset:1024\n" \
  "v_mfma_f32_16x16x32_bf16 a[164:167], v[40:43], v[136:139], a[164:167]\n" \
  "v_mfma_f32_16x16x32_bf16 a[168:171], v[40:43], v[144:147], a[168:171]\n" \
  "buffer_load_dwordx4 v[204:207], v233, s[72:75], s92 offen offset:1024\n" \
  "v_mfma_f32_16x16x32_bf16 a[172:175], v[40:43], v[152:155], a[172:175]\n" \
  "v_mfma_f32_16x16x32_bf16 a[176:179], v[44:47], v[128:131], a[176:179]\n" \
  "buffer_load_dwordx4 v[212:215], v234, s[72:75], s92 offen offset:1024\n" \
  "v_mfma_f32_16x16x32_bf16 a[180:183], v[44:47], v[136:139], a[180:183]\n" \
  "v_mfma_f32_16x16x32_bf16 a[184:187], v[44:47], v[144:147], a[184:187]\n" \
  "buffer_load_dwordx4 v[220:223], v235, s[72:75], s92 offen offset:1024\n" \
  "s_add_u32 s92, s92, 2048\n" \
  "v_mfma_f32_16x16x32_bf16 a[188:191], v[44:47], v[152:155], a[188:191]\n" \
  "v_mfma_f32_16x16x32_bf16 a[192:195], v[48:51], v[128:131], a[192:195]\n" \
  "v_mfma_f32_16x16x32_bf16 a[196:199], v[48:51], v[136:139], a[196:199]\n" \
  "v_mfma_f32_16x16x32_bf16 a[200:203], v[48:51], v[144:147], a[200:203]\n" \
  "v_mfma_f32_16x16x32_bf16 a[204:207], v[48:51], v[152:155], a[204:207]\n" \
  "v_mfma_f32_16x16x32_bf16 a[208:211], v[52:55], v[128:131], a[208:211]\n" \
  "v_mfma_f32_16x16x32_bf16 a[212:215], v[52:55], v[136:139], a[212:215]\n" \
  "v_mfma_f32_16x16x32_bf16 a[216:219], v[52:55], v[144:147], a[216:219]\n" \
  "v_mfma_f32_16x16x32_bf16 a[220:223], v[52:55], v[152:155], a[220:223]\n" \
  "v_mfma_f32_16x16x32_bf16 a[224:227], v[56:59], v[128:131], a[224:227]\n" \
  "v_mfma_f32_16x16x32_bf16 a[228:231], v[56:59], v[136:139], a[228:231]\n" \
  "v_mfma_f32_16x16x32_bf16 a[232:235], v[56:59], v[144:147], a[232:235]\n" \
  "v_mfma_f32_16x16x32_bf16 a[236:239], v[56:59], v[152:155], a[236:239]\n" \
  "v_mfma_f32_16x16x32_bf16 a[240:243], v[60:63], v[128:131], a[240:243]\n" \
  "v_mfma_f32_16x16x32_bf16 a[244:247], v[60:63], v[136:139], a[244:247]\n" \
  "v_mfma_f32_16x16x32_bf16 a[248:251], v[60:63], v[144:147], a[248:251]\n" \
  "v_mfma_f32_16x16x32_bf16 a[252:255], v[60:63], v[152:155], a[252:255]\n" \
  "s_waitcnt lgkmcnt(0)\n" \
  "v_mfma_f32_16x16x32_bf16 a[0:3], v[64:67], v[132:135], a[0:3]\n" \
  "v_mfma_f32_16x16x32_bf16 a[4:7], v[64:67], v[140:143], a[4:7]\n" \
  "v_mfma_f32_16x16x32_bf16 a[8:11], v[64:67], v[148:151], a[8:11]\n" \
  "v_mfma_f32_16x16x32_bf16 a[12:15], v[64:67], v[156:159], a[12:15]\n" \
  "v_mfma_f32_16x16x32_bf16 a[16:19], v[68:71], v[132:135], a[16:19]\n" \
  "v_mfma_f32_16x16x32_bf16 a[20:23], v[68:71], v[140:143], a[20:23]\n" \
  "v_mfma_f32_16x16x32_bf16 a[24:27], v[68:71], v[148:151], a[24:27]\n" \
  "v_mfma_f32_16x16x32_bf16 a[28:31], v[68:71], v[156:159], a[28:31]\n" \
  "s_waitcnt vmcnt(40)\n" \
  "s_barrier\n" \
  "v_mfma_f32_16x16x32_bf16 a[32:35], v[72:75], v[132:135], a[32:35]\n" \
  "ds_read_b64_tr_b16 v[0:1], v224 offset:0\n" \
  "v_mfma_f32_16x16x32_bf16 a[36:39], v[72:75], v[140:143], a[36:39]\n" \
  "ds_read_b64_tr_b16 v[2:3], v224 offset:1024\n" \
  "v_mfma_f32_16x16x32_bf16 a[40:43], v[72:75], v[148:151], a[40:43]\n" \
  "ds_read_b64_tr_b16 v[4:5], v225 offset:0\n" \
  "v_mfma_f32_16x16x32_bf16 a[44:47], v[72:75], v[156:159], a[44:47]\n" \
  "ds_read_b64_tr_b16 v[6:7], v225 offset:1024\n" \
  "v_mfma_f32_16x16x32_bf16 a[48:51], v[76:79], v[132:135], a[48:51]\n" \
  "ds_read_b64_tr_b16 v[8:9], v224 offset:256\n" \
  "v_mfma_f32_16x16x32_bf16 a[52:55], v[76:79], v[140:143], a[52:55]\n" \
  "ds_read_b64_tr_b16 v[10:11], v224 offset:1280\n" \
  "v_mfma_f32_16x16x32_bf16 a[56:59], v[76:79], v[148:151], a[56:59]\n" \
  "ds_read_b64_tr_b16 v[12:13], v225 offset:256\n" \
  "v_mfma_f32_16x16x32_bf16 a[60:63], v[76:79], v[156:159], a[60:63]\n" \
  "ds_read_b64_tr_b16 v[14:15], v225 offset:1280\n" \
  "v_mfma_f32_16x16x32_bf16 a[64:67], v[80:83], v[132:135], a[64:67]\n" \
  "s_add_u32 m0, s95, 98304\n" \
  "s_nop 0\n" \
  "buffer_load_dwordx4 v236, s[76:79], s93 offen lds\n" \
  "v_mfma_f32_16x16x32_bf16 a[68:71], v[80:83], v[140:143], a[68:71]\n" \
  "ds_read_b64_tr_b16 v[16:17], v224 offset:512\n" \
  "v_mfma_f32_16x16x32_bf16 a[72:75], v[80:83], v[148:151], a[72:75]\n" \
  "ds_read_b64_tr_b16 v[18:19], v224 offset:1536\n" \
  "v_mfma_f32_16x16x32_bf16 a[76:79], v[80:83], v[156:159], a[76:79]\n" \
  "buffer_load_dwordx4 v237, s[76:79], s93 offen offset:1024 lds\n" \
  "v_mfma_f32_16x16x32_bf16 a[80:83], v[84:87], v[132:135], a[80:83]\n" \
  "ds_read_b64_tr_b16 v[20:21], v225 offset:512\n" \
  "v_mfma_f32_16x16x32_bf16 a[84:87], v[84:87], v[140:143], a[84:87]\n" \
  "ds_read_b64_tr_b16 v[22:23], v225 offset:1536\n" \
  "v_mfma_f32_16x16x32_bf16 a[88:91], v[84:87], v[148:151], a[88:91]\n" \
  "buffer_load_dwordx4 v238, s[76:79], s93 offen offset:2048 lds\n" \
  "v_mfma_f32_16x16x32_bf16 a[92:95], v[84:87], v[156:159], a[92:95]\n" \
  "ds_read_b64_tr_b16 v[24:25], v224 offset:768\n" \
  "v_mfma_f32_16x16x32_bf16 a[96:99], v[88:91], v[132:135], a[96:99]\n" \
  "ds_read_b64_tr_b16 v[26:27], v224 offset:1792\n" \
  "v_mfma_f32_16x16x32_bf16 a[100:103], v[88:91], v[140:143], a[100:103]\n" \
  "buffer_load_dwordx4 v239, s[76:79], s93 offen offset:3072 lds\n" \
  "v_mfma_f32_16x16x32_bf16 a[104:107], v[88:91], v[148:151], a[104:107]\n" \
  "ds_read_b64_tr_b16 v[28:29], v225 offset:768\n" \
  "v_mfma_f32_16x16x32_bf16 a[108:111], v[88:91], v[156:159], a[108:111]\n" \
  "ds_read_b64_tr_b16 v[30:31], v225 offset:1792\n" \
  "v_mfma_f32_16x16x32_bf16 a[112:115], v[92:95], v[132:135], a[112:115]\n" \
  "s_lshr_b32 s97, s95, 1\n" \
  "s_add_u32 m0, s97, 114688\n" \
  "s_nop 0\n" \
  "buffer_load_dwordx4 v240, s[80:83], s94 offen lds\n" \
  "v_mfma_f32_16x16x32_bf16 a[116:119], v[92:95], v[140:143], a[116:119]\n" \
  "ds_read_b64_tr_b16 v[32:33], v228 offset:0\n" \
  "v_mfma_f32_16x16x32_bf16 a[120:123], v[92:95], v[148:151], a[120:123]\n" \
  "ds_read_b64_tr_b16 v[34:35], v228 offset:512\n" \
  "v_mfma_f32_16x16x32_bf16 a[124:127], v[92:95], v[156:159], a[124:127]\n" \
  "buffer_load_dwordx4 v241, s[80:83], s94 offen offset:1024 lds\n" \
  "v_mfma_f32_16x16x32_bf16 a[128:131], v[96:99], v[132:135], a[128:131]\n" \
  "ds_read_b64_tr_b16 v[36:37], v229 offset:0\n" \
  "v_mfma_f32_16x16x32_bf16 a[132:135], v[96:99], v[140:143], a[132:135]\n" \
  "ds_read_b64_tr_b16 v[38:39], v229 offset:512\n" \
  "v_mfma_f32_16x16x32_bf16 a[136:139], v[96:99], v[148:151], a[136:139]\n" \
  "s_add_u32 m0, s97, 122880\n" \
  "s_nop 0\n" \
  "buffer_load_dwordx4 v240, s[84:87], s94 offen lds\n" \
  "v_mfma_f32_16x16x32_bf16 a[140:143], v[96:99], v[156:159], a[140:143]\n" \
  "ds_read_b64_tr_b16 v[40:41], v228 offset:256\n" \
  "v_mfma_f32_16x16x32_bf16 a[144:147], v[100:103], v[132:135], a[144:147]\n" \
  "ds_read_b64_tr_b16 v[42:43], v228 offset:768\n" \
  "v_mfma_f32_16x16x32_bf16 a[148:151], v[100:103], v[140:143], a[148:151]\n" \
  "buffer_load_dwordx4 v241, s[84:87], s94 offen offset:1024 lds\n" \
  "s_add_u32 s93, s93, 0x80000\n" \
  "s_add_u32 s94, s94, 229376\n" \
  "v_mfma_f32_16x16x32_bf16 a[152:155], v[100:103], v[148:151], a[152:155]\n" \
  "ds_read_b64_tr_b16 v[44:45], v229 offset:256\n" \
  "v_mfma_f32_16x16x32_bf16 a[156:159], v[100:103], v[156:159], a[156:159]\n" \
  "ds_read_b64_tr_b16 v[46:47], v229 offset:768\n" \
  "v_mfma_f32_16x16x32_bf16 a[160:163], v[104:107], v[132:135], a[160:163]\n" \
  "ds_read_b64_tr_b16 v[48:49], v228 offset:8192\n" \
  "v_mfma_f32_16x16x32_bf16 a[164:167], v[104:107], v[140:143], a[164:167]\n" \
  "ds_read_b64_tr_b16 v[50:51], v228 offset:8704\n" \
  "v_mfma_f32_16x16x32_bf16 a[168:171], v[104:107], v[148:151], a[168:171]\n" \
  "ds_read_b64_tr_b16 v[52:53], v229 offset:8192\n" \
  "v_mfma_f32_16x16x32_bf16 a[172:175], v[104:107], v[156:159], a[172:175]\n" \
  "ds_read_b64_tr_b16 v[54:55], v229 offset:8704\n" \
  "v_mfma_f32_16x16x32_bf16 a[176:179], v[108:111], v[132:135], a[176:179]\n" \
  "ds_read_b64_tr_b16 v[56:57], v228 offset:8448\n" \
  "v_mfma_f32_16x16x32_bf16 a[180:183], v[108:111], v[140:143], a[180:183]\n" \
  "ds_read_b64_tr_b16 v[58:59], v228 offset:8960\n" \
  "v_mfma_f32_16x16x32_bf16 a[184:187], v[108:111], v[148:151], a[184:187]\n" \
  "ds_read_b64_tr_b16 v[60:61], v229 offset:8448\n" \
  "v_mfma_f32_16x16x32_bf16 a[188:191], v[108:111], v[156:159], a[188:191]\n" \
  "ds_read_b64_tr_b16 v[62:63], v229 offset:8960\n" \
  "v_mfma_f32_16x16x32_bf16 a[192:195], v[112:115], v[132:135], a[192:195]\n" \
  "v_mfma_f32_16x16x32_bf16 a[196:199], v[112:115], v[140:143], a[196:199]\n" \
  "v_mfma_f32_16x16x32_bf16 a[200:203], v[112:115], v[148:151], a[200:203]\n" \
  "v_mfma_f32_16x16x32_bf16 a[204:207], v[112:115], v[156:159], a[204:207]\n" \
  "v_mfma_f32_16x16x32_bf16 a[208:211], v[116:119], v[132:135], a[208:211]\n" \
  "v_mfma_f32_16x16x32_bf16 a[212:215], v[116:119], v[140:143], a[212:215]\n" \
  "v_mfma_f32_16x16x32_bf16 a[216:219], v[116:119], v[148:151], a[216:219]\n" \
  "v_mfma_f32_16x16x32_bf16 a[220:223], v[116:119], v[156:159], a[220:223]\n" \
  "v_mfma_f32_16x16x32_bf16 a[224:227], v[120:123], v[132:135], a[224:227]\n" \
  "v_mfma_f32_16x16x32_bf16 a[228:231], v[120:123], v[140:143], a[228:231]\n" \
  "v_mfma_f32_16x16x32_bf16 a[232:235], v[120:123], v[148:151], a[232:235]\n" \
  "v_mfma_f32_16x16x32_bf16 a[236:239], v[120:123], v[156:159], a[236:239]\n" \
  "v_mfma_f32_16x16x32_bf16 a[240:243], v[124:127], v[132:135], a[240:243]\n" \
  "v_mfma_f32_16x16x32_bf16 a[244:247], v[124:127], v[140:143], a[244:247]\n" \
  "v_mfma_f32_16x16x32_bf16 a[248:251], v[124:127], v[148:151], a[248:251]\n" \
  "v_mfma_f32_16x16x32_bf16 a[252:255], v[124:127], v[156:159], a[252:255]\n" \
  "s_waitcnt vmcnt(24) lgkmcnt(0)\n" \
  "v_mfma_f32_16x16x32_bf16 a[0:3], v[0:3], v[160:163], a[0:3]\n" \
  "ds_read_b64_tr_b16 v[64:65], v224 offset:8192\n" \
  "v_mfma_f32_16x16x32_bf16 a[4:7], v[0:3], v[168:171], a[4:7]\n" \
  "ds_read_b64_tr_b16 v[66:67], v224 offset:9216\n" \
  "v_mfma_f32_16x16x32_bf16 a[8:11], v[0:3], v[176:179], a[8:11]\n" \
  "ds_read_b64_tr_b16 v[68:69], v225 offset:8192\n" \
  "v_mfma_f32_16x16x32_bf16 a[12:15], v[0:3], v[184:187], a[12:15]\n" \
  "ds_read_b64_tr_b16 v[70:71], v225 offset:9216\n" \
  "v_mfma_f32_16x16x32_bf16 a[16:19], v[4:7], v[160:163], a[16:19]\n" \
  "ds_read_b64_tr_b16 v[72:73], v224 offset:8448\n" \
  "v_mfma_f32_16x16x32_bf16 a[20:23], v[4:7], v[168:171], a[20:23]\n" \
  "ds_read_b64_tr_b16 v[74:75], v224 offset:9472\n" \
  "v_mfma_f32_16x16x32_bf16 a[24:27], v[4:7], v[176:179], a[24:27]\n" \
  "ds_read_b64_tr_b16 v[76:77], v225 offset:8448\n" \
  "v_mfma_f32_16x16x32_bf16 a[28:31], v[4:7], v[184:187], a[28:31]\n" \
  "ds_read_b64_tr_b16 v[78:79], v225 offset:9472\n" \
  "v_mfma_f32_16x16x32_bf16 a[32:35], v[8:11], v[160:163], a[32:35]\n" \
  "ds_read_b64_tr_b16 v[80:81], v224 offset:8704\n" \
  "v_mfma_f32_16x16x32_bf16 a[36:39], v[8:11], v[168:171], a[36:39]\n" \
  "ds_read_b64_tr_b16 v[82:83], v224 offset:9728\n" \
  "v_mfma_f32_16x16x32_bf16 a[40:43], v[8:11], v[176:179], a[40:43]\n" \
  "ds_read_b64_tr_b16 v[84:85], v225 offset:8704\n" \
  "v_mfma_f32_16x16x32_bf16 a[44:47], v[8:11], v[184:187], a[44:47]\n" \
  "ds_read_b64_tr_b16 v[86:87], v225 offset:9728\n" \
  "v_mfma_f32_16x16x32_bf16 a[48:51], v[12:15], v[160:163], a[48:51]\n" \
  "ds_read_b64_tr_b16 v[88:89], v224 offset:8960\n" \
  "v_mfma_f32_16x16x32_bf16 a[52:55], v[12:15], v[168:171], a[52:55]\n" \
  "ds_read_b64_tr_b16 v[90:91], v224 offset:9984\n" \
  "v_mfma_f32_16x16x32_bf16 a[56:59], v[12:15], v[176:179], a[56:59]\n" \
  "ds_read_b64_tr_b16 v[92:93], v225 offset:8960\n" \
  "v_mfma_f32_16x16x32_bf16 a[60:63], v[12:15], v[184:187], a[60:63]\n" \
  "ds_read_b64_tr_b16 v[94:95], v225 offset:9984\n" \
  "v_mfma_f32_16x16x32_bf16 a[64:67], v[16:19], v[160:163], a[64:67]\n" \
  "ds_read_b64_tr_b16 v[96:97], v228 offset:4096\n" \
  "v_mfma_f32_16x16x32_bf16 a[68:71], v[16:19], v[168:171], a[68:71]\n" \
  "ds_read_b64_tr_b16 v[98:99], v228 offset:4608\n" \
  "v_mfma_f32_16x16x32_bf16 a[72:75], v[16:19], v[176:179], a[72:75]\n" \
  "ds_read_b64_tr_b16 v[100:101], v229 offset:4096\n" \
  "v_mfma_f32_16x16x32_bf16 a[76:79], v[16:19], v[184:187], a[76:79]\n" \
  "ds_read_b64_tr_b16 v[102:103], v229 offset:4608\n" \
  "v_mfma_f32_16x16x32_bf16 a[80:83], v[20:23], v[160:163], a[80:83]\n" \
  "ds_read_b64_tr_b16 v[104:105], v228 offset:4352\n" \
  "v_mfma_f32_16x16x32_bf16 a[84:87], v[20:23], v[168:171], a[84:87]\n" \
  "ds_read_b64_tr_b16 v[106:107], v228 offset:4864\n" \
  "v_mfma_f32_16x16x32_bf16 a[88:91], v[20:23], v[176:179], a[88:91]\n" \
  "ds_read_b64_tr_b16 v[108:109], v229 offset:4352\n" \
  "v_mfma_f32_16x16x32_bf16 a[92:95], v[20:23], v[184:187], a[92:95]\n" \
  "ds_read_b64_tr_b16 v[110:111], v229 offset:4864\n" \
  "v_mfma_f32_16x16x32_bf16 a[96:99], v[24:27], v[160:163], a[96:99]\n" \
  "ds_read_b64_tr_b16 v[112:113], v228 offset:12288\n" \
  "v_mfma_f32_16x16x32_bf16 a[100:103], v[24:27], v[168:171], a[100:103]\n" \
  "ds_read_b64_tr_b16 v[114:115], v228 offset:12800\n" \
  "v_mfma_f32_16x16x32_bf16 a[104:107], v[24:27], v[176:179], a[104:107]\n" \
  "ds_read_b64_tr_b16 v[116:117], v229 offset:12288\n" \
  "v_mfma_f32_16x16x32_bf16 a[108:111], v[24:27], v[184:187], a[108:111]\n" \
  "ds_read_b64_tr_b16 v[118:119], v229 offset:12800\n" \
  "v_mfma_f32_16x16x32_bf16 a[112:115], v[28:31], v[160:163], a[112:115]\n" \
  "ds_read_b64_tr_b16 v[120:121], v228 offset:12544\n" \
  "v_mfma_f32_16x16x32_bf16 a[116:119], v[28:31], v[168:171], a[116:119]\n" \
  "ds_read_b64_tr_b16 v[122:123], v228 offset:13056\n" \
  "v_mfma_f32_16x16x32_bf16 a[120:123], v[28:31], v[176:179], a[120:123]\n" \
  "ds_read_b64_tr_b16 v[124:125], v229 offset:12544\n" \
  "v_mfma_f32_16x16x32_bf16 a[124:127], v[28:31], v[184:187], a[124:127]\n" \
  "ds_read_b64_tr_b16 v[126:127], v229 offset:13056\n" \
  "v_mfma_f32_16x16x32_bf16 a[128:131], v[32:35], v[160:163], a[128:131]\n" \
  "buffer_load_dwordx4 v[128:131], v232, s[72:75], s92 offen offset:0\n" \
  "v_mfma_f32_16x16x32_bf16 a[132:135], v[32:35], v[168:171], a[132:135]\n" \
  "v_mfma_f32_16x16x32_bf16 a[136:139], v[32:35], v[176:179], a[136:139]\n" \
  "buffer_load_dwordx4 v[136:139], v233, s[72:75], s92 offen offset:0\n" \
  "v_mfma_f32_16x16x32_bf16 a[140:143], v[32:35], v[184:187], a[140:143]\n" \
  "v_mfma_f32_16x16x32_bf16 a[144:147], v[36:39], v[160:163], a[144:147]\n" \
  "buffer_load_dwordx4 v[144:147], v234, s[72:75], s92 offen offset:0\n" \
  "v_mfma_f32_16x16x32_bf16 a[148:151], v[36:39], v[168:171], a[148:151]\n" \
  "v_mfma_f32_16x16x32_bf16 a[152:155], v[36:39], v[176:179], a[152:155]\n" \
  "buffer_load_dwordx4 v[152:155], v235, s[72:75], s92 offen offset:0\n" \
  "v_mfma_f32_16x16x32_bf16 a[156:159], v[36:39], v[184:187], a[156:159]\n" \
  "v_mfma_f32_16x16x32_bf16 a[160:163], v[40:43], v[160:163], a[160:163]\n" \
  "buffer_load_dwordx4 v[132:135], v232, s[72:75], s92 offen offset:1024\n" \
  "v_mfma_f32_16x16x32_bf16 a[164:167], v[40:43], v[168:171], a[164:167]\n" \
  "v_mfma_f32_16x16x32_bf16 a[168:171], v[40:43], v[176:179], a[168:171]\n" \
  "buffer_load_dwordx4 v[140:143], v233, s[72:75], s92 offen offset:1024\n" \
  "v_mfma_f32_16x16x32_bf16 a[172:175], v[40:43], v[184:187], a[172:175]\n" \
  "v_mfma_f32_16x16x32_bf16 a[176:179], v[44:47], v[160:163], a[176:179]\n" \
  "buffer_load_dwordx4 v[148:151], v234, s[72:75], s92 offen offset:1024\n" \
  "v_mfma_f32_16x16x32_bf16 a[180:183], v[44:47], v[168:171], a[180:183]\n" \
  "v_mfma_f32_16x16x32_bf16 a[184:187], v[44:47], v[176:179], a[184:187]\n" \
  "buffer_load_dwordx4 v[156:159], v235, s[72:75], s92 offen offset:1024\n" \
  "s_add_u32 s92, s92, 2048\n" \
  "v_mfma_f32_16x16x32_bf16 a[188:191], v[44:47], v[184:187], a[188:191]\n" \
  "v_mfma_f32_16x16x32_bf16 a[192:195], v[48:51], v[160:163], a[192:195]\n" \
  "v_mfma_f32_16x16x32_bf16 a[196:199], v[48:51], v[168:171], a[196:199]\n" \
  "v_mfma_f32_16x16x32_bf16 a[200:203], v[48:51], v[176:179], a[200:203]\n" \
  "v_mfma_f32_16x16x32_bf16 a[204:207], v[48:51], v[184:187], a[204:207]\n" \
  "v_mfma_f32_16x16x32_bf16 a[208:211], v[52:55], v[160:163], a[208:211]\n" \
  "v_mfma_f32_16x16x32_bf16 a[212:215], v[52:55], v[168:171], a[212:215]\n" \
  "v_mfma_f32_16x16x32_bf16 a[216:219], v[52:55], v[176:179], a[216:219]\n" \
  "v_mfma_f32_16x16x32_bf16 a[220:223], v[52:55], v[184:187], a[220:223]\n" \
  "v_mfma_f32_16x16x32_bf16 a[224:227], v[56:59], v[160:163], a[224:227]\n" \
  "v_mfma_f32_16x16x32_bf16 a[228:231], v[56:59], v[168:171], a[228:231]\n" \
  "v_mfma_f32_16x16x32_bf16 a[232:235], v[56:59], v[176:179], a[232:235]\n" \
  "v_mfma_f32_16x16x32_bf16 a[236:239], v[56:59], v[184:187], a[236:239]\n" \
  "v_mfma_f32_16x16x32_bf16 a[240:243], v[60:63], v[160:163], a[240:243]\n" \
  "v_mfma_f32_16x16x32_bf16 a[244:247], v[60:63], v[168:171], a[244:247]\n" \
  "v_mfma_f32_16x16x32_bf16 a[248:251], v[60:63], v[176:179], a[248:251]\n" \
  "v_mfma_f32_16x16x32_bf16 a[252:255], v[60:63], v[184:187], a[252:255]\n" \
  "s_waitcnt lgkmcnt(0)\n" \
  "v_mfma_f32_16x16x32_bf16 a[0:3], v[64:67], v[164:167], a[0:3]\n" \
  "v_mfma_f32_16x16x32_bf16 a[4:7], v[64:67], v[172:175], a[4:7]\n" \
  "v_mfma_f32_16x16x32_bf16 a[8:11], v[64:67], v[180:183], a[8:11]\n" \
  "v_mfma_f32_16x16x32_bf16 a[12:15], v[64:67], v[188:191], a[12:15]\n" \
  "v_mfma_f32_16x16x32_bf16 a[16:19], v[68:71], v[164:167], a[16:19]\n" \
  "v_mfma_f32_16x16x32_bf16 a[20:23], v[68:71], v[172:175], a[20:23]\n" \
  "v_mfma_f32_16x16x32_bf16 a[24:27], v[68:71], v[180:183], a[24:27]\n" \
  "v_mfma_f32_16x16x32_bf16 a[28:31], v[68:71], v[188:191], a[28:31]\n" \
  "s_waitcnt vmcnt(40)\n" \
  "s_barrier\n" \
  "v_mfma_f32_16x16x32_bf16 a[32:35], v[72:75], v[164:167], a[32:35]\n" \
  "ds_read_b64_tr_b16 v[0:1], v224 offset:32768\n" \
  "v_mfma_f32_16x16x32_bf16 a[36:39], v[72:75], v[172:175], a[36:39]\n" \
  "ds_read_b64_tr_b16 v[2:3], v224 offset:33792\n" \
  "v_mfma_f32_16x16x32_bf16 a[40:43], v[72:75], v[180:183], a[40:43]\n" \
  "ds_read_b64_tr_b16 v[4:5], v225 offset:32768\n" \
  "v_mfma_f32_16x16x32_bf16 a[44:47], v[72:75], v[188:191], a[44:47]\n" \
  "ds_read_b64_tr_b16 v[6:7], v225 offset:33792\n" \
  "v_mfma_f32_16x16x32_bf16 a[48:51], v[76:79], v[164:167], a[48:51]\n" \
  "ds_read_b64_tr_b16 v[8:9], v224 offset:33024\n" \
  "v_mfma_f32_16x16x32_bf16 a[52:55], v[76:79], v[172:175], a[52:55]\n" \
  "ds_read_b64_tr_b16 v[10:11], v224 offset:34048\n" \
  "v_mfma_f32_16x16x32_bf16 a[56:59], v[76:79], v[180:183], a[56:59]\n" \
  "ds_read_b64_tr_b16 v[12:13], v225 offset:33024\n" \
  "v_mfma_f32_16x16x32_bf16 a[60:63], v[76:79], v[188:191], a[60:63]\n" \
  "ds_read_b64_tr_b16 v[14:15], v225 offset:34048\n" \
  "v_mfma_f32_16x16x32_bf16 a[64:67], v[80:83], v[164:167], a[64:67]\n" \
  "s_add_u32 m0, s95, 0\n" \
  "s_add_u32 s97, s95, 0\n" \
  "buffer_load_dwordx4 v242, s[40:43], s97 offen lds\n" \
  "v_mfma_f32_16x16x32_bf16 a[68:71], v[80:83], v[172:175], a[68:71]\n" \
  "ds_read_b64_tr_b16 v[16:17], v224 offset:33280\n" \
  "v_mfma_f32_16x16x32_bf16 a[72:75], v[80:83], v[180:183], a[72:75]\n" \
  "ds_read_b64_tr_b16 v[18:19], v224 offset:34304\n" \
  "v_mfma_f32_16x16x32_bf16 a[76:79], v[80:83], v[188:191], a[76:79]\n" \
  "buffer_load_dwordx4 v243, s[40:43], s97 offen offset:1024 lds\n" \
  "v_mfma_f32_16x16x32_bf16 a[80:83], v[84:87], v[164:167], a[80:83]\n" \
  "ds_read_b64_tr_b16 v[20:21], v225 offset:33280\n" \
  "v_mfma_f32_16x16x32_bf16 a[84:87], v[84:87], v[172:175], a[84:87]\n" \
  "ds_read_b64_tr_b16 v[22:23], v225 offset:34304\n" \
  "v_mfma_f32_16x16x32_bf16 a[88:91], v[84:87], v[180:183], a[88:91]\n" \
  "buffer_load_dwordx4 v244, s[40:43], s97 offen offset:2048 lds\n" \
  "v_mfma_f32_16x16x32_bf16 a[92:95], v[84:87], v[188:191], a[92:95]\n" \
  "ds_read_b64_tr_b16 v[24:25], v224 offset:33536\n" \
  "v_mfma_f32_16x16x32_bf16 a[96:99], v[88:91], v[164:167], a[96:99]\n" \
  "ds_read_b64_tr_b16 v[26:27], v224 offset:34560\n" \
  "v_mfma_f32_16x16x32_bf16 a[100:103], v[88:91], v[172:175], a[100:103]\n" \
  "buffer_load_dwordx4 v245, s[40:43], s97 offen offset:3072 lds\n" \
  "v_mfma_f32_16x16x32_bf16 a[104:107], v[88:91], v[180:183], a[104:107]\n" \
  "ds_read_b64_tr_b16 v[28:29], v225 offset:33536\n" \
  "v_mfma_f32_16x16x32_bf16 a[108:111], v[88:91], v[188:191], a[108:111]\n" \
  "ds_read_b64_tr_b16 v[30:31], v225 offset:34560\n" \
  "v_mfma_f32_16x16x32_bf16 a[112:115], v[92:95], v[164:167], a[112:115]\n" \
  "s_add_u32 m0, s95, 16384\n" \
  "s_add_u32 s97, s95, 16384\n" \
  "buffer_load_dwordx4 v242, s[40:43], s97 offen lds\n" \
  "v_mfma_f32_16x16x32_bf16 a[116:119], v[92:95], v[172:175], a[116:119]\n" \
  "ds_read_b64_tr_b16 v[32:33], v228 offset:32768\n" \
  "v_mfma_f32_16x16x32_bf16 a[120:123], v[92:95], v[180:183], a[120:123]\n" \
  "ds_read_b64_tr_b16 v[34:35], v228 offset:33280\n" \
  "v_mfma_f32_16x16x32_bf16 a[124:127], v[92:95], v[188:191], a[124:127]\n" \
  "buffer_load_dwordx4 v243, s[40:43], s97 offen offset:1024 lds\n" \
  "v_mfma_f32_16x16x32_bf16 a[128:131], v[96:99], v[164:167], a[128:131]\n" \
  "ds_read_b64_tr_b16 v[36:37], v229 offset:32768\n" \
  "v_mfma_f32_16x16x32_bf16 a[132:135], v[96:99], v[172:175], a[132:135]\n" \
  "ds_read_b64_tr_b16 v[38:39], v229 offset:33280\n" \
  "v_mfma_f32_16x16x32_bf16 a[136:139], v[96:99], v[180:183], a[136:139]\n" \
  "buffer_load_dwordx4 v244, s[40:43], s97 offen offset:2048 lds\n" \
  "v_mfma_f32_16x16x32_bf16 a[140:143], v[96:99], v[188:191], a[140:143]\n" \
  "ds_read_b64_tr_b16 v[40:41], v228 offset:33024\n" \
  "v_mfma_f32_16x16x32_bf16 a[144:147], v[100:103], v[164:167], a[144:147]\n" \
  "ds_read_b64_tr_b16 v[42:43], v228 offset:33536\n" \
  "v_mfma_f32_16x16x32_bf16 a[148:151], v[100:103], v[172:175], a[148:151]\n" \
  "buffer_load_dwordx4 v245, s[40:43], s97 offen offset:3072 lds\n" \
  "v_mfma_f32_16x16x32_bf16 a[152:155], v[100:103], v[180:183], a[152:155]\n" \
  "ds_read_b64_tr_b16 v[44:45], v229 offset:33024\n" \
  "v_mfma_f32_16x16x32_bf16 a[156:159], v[100:103], v[188:191], a[156:159]\n" \
  "ds_read_b64_tr_b16 v[46:47], v229 offset:33536\n" \
  "v_mfma_f32_16x16x32_bf16 a[160:163], v[104:107], v[164:167], a[160:163]\n" \
  "ds_read_b64_tr_b16 v[48:49], v228 offset:40960\n" \
  "v_mfma_f32_16x16x32_bf16 a[164:167], v[104:107], v[172:175], a[164:167]\n" \
  "ds_read_b64_tr_b16 v[50:51], v228 offset:41472\n" \
  "v_mfma_f32_16x16x32_bf16 a[168:171], v[104:107], v[180:183], a[168:171]\n" \
  "ds_read_b64_tr_b16 v[52:53], v229 offset:40960\n" \
  "v_mfma_f32_16x16x32_bf16 a[172:175], v[104:107], v[188:191], a[172:175]\n" \
  "ds_read_b64_tr_b16 v[54:55], v229 offset:41472\n" \
  "v_mfma_f32_16x16x32_bf16 a[176:179], v[108:111], v[164:167], a[176:179]\n" \
  "ds_read_b64_tr_b16 v[56:57], v228 offset:41216\n" \
  "v_mfma_f32_16x16x32_bf16 a[180:183], v[108:111], v[172:175], a[180:183]\n" \
  "ds_read_b64_tr_b16 v[58:59], v228 offset:41728\n" \
  "v_mfma_f32_16x16x32_bf16 a[184:187], v[108:111], v[180:183], a[184:187]\n" \
  "ds_read_b64_tr_b16 v[60:61], v229 offset:41216\n" \
  "v_mfma_f32_16x16x32_bf16 a[188:191], v[108:111], v[188:191], a[188:191]\n" \
  "ds_read_b64_tr_b16 v[62:63], v229 offset:41728\n" \
  "v_mfma_f32_16x16x32_bf16 a[192:195], v[112:115], v[164:167], a[192:195]\n" \
  "v_mfma_f32_16x16x32_bf16 a[196:199], v[112:115], v[172:175], a[196:199]\n" \
  "v_mfma_f32_16x16x32_bf16 a[200:203], v[112:115], v[180:183], a[200:203]\n" \
  "v_mfma_f32_16x16x32_bf16 a[204:207], v[112:115], v[188:191], a[204:207]\n" \
  "v_mfma_f32_16x16x32_bf16 a[208:211], v[116:119], v[164:167], a[208:211]\n" \
  "v_mfma_f32_16x16x32_bf16 a[212:215], v[116:119], v[172:175], a[212:215]\n" \
  "v_mfma_f32_16x16x32_bf16 a[216:219], v[116:119], v[180:183], a[216:219]\n" \
  "v_mfma_f32_16x16x32_bf16 a[220:223], v[116:119], v[188:191], a[220:223]\n" \
  "v_mfma_f32_16x16x32_bf16 a[224:227], v[120:123], v[164:167], a[224:227]\n" \
  "v_mfma_f32_16x16x32_bf16 a[228:231], v[120:123], v[172:175], a[228:231]\n" \
  "v_mfma_f32_16x16x32_bf16 a[232:235], v[120:123], v[180:183], a[232:235]\n" \
  "v_mfma_f32_16x16x32_bf16 a[236:239], v[120:123], v[188:191], a[236:239]\n" \
  "v_mfma_f32_16x16x32_bf16 a[240:243], v[124:127], v[164:167], a[240:243]\n" \
  "v_mfma_f32_16x16x32_bf16 a[244:247], v[124:127], v[172:175], a[244:247]\n" \
  "v_mfma_f32_16x16x32_bf16 a[248:251], v[124:127], v[180:183], a[248:251]\n" \
  "v_mfma_f32_16x16x32_bf16 a[252:255], v[124:127], v[188:191], a[252:255]\n" \
  "s_waitcnt vmcnt(24) lgkmcnt(0)\n" \
  "v_mfma_f32_16x16x32_bf16 a[0:3], v[0:3], v[192:195], a[0:3]\n" \
  "ds_read_b64_tr_b16 v[64:65], v224 offset:40960\n" \
  "v_mfma_f32_16x16x32_bf16 a[4:7], v[0:3], v[200:203], a[4:7]\n" \
  "ds_read_b64_tr_b16 v[66:67], v224 offset:41984\n" \
  "v_mfma_f32_16x16x32_bf16 a[8:11], v[0:3], v[208:211], a[8:11]\n" \
  "ds_read_b64_tr_b16 v[68:69], v225 offset:40960\n" \
  "v_mfma_f32_16x16x32_bf16 a[12:15], v[0:3], v[216:219], a[12:15]\n" \
  "ds_read_b64_tr_b16 v[70:71], v225 offset:41984\n" \
  "v_mfma_f32_16x16x32_bf16 a[16:19], v[4:7], v[192:195], a[16:19]\n" \
  "ds_read_b64_tr_b16 v[72:73], v224 offset:41216\n" \
  "v_mfma_f32_16x16x32_bf16 a[20:23], v[4:7], v[200:203], a[20:23]\n" \
  "ds_read_b64_tr_b16 v[74:75], v224 offset:42240\n" \
  "v_mfma_f32_16x16x32_bf16 a[24:27], v[4:7], v[208:211], a[24:27]\n" \
  "ds_read_b64_tr_b16 v[76:77], v225 offset:41216\n" \
  "v_mfma_f32_16x16x32_bf16 a[28:31], v[4:7], v[216:219], a[28:31]\n" \
  "ds_read_b64_tr_b16 v[78:79], v225 offset:42240\n" \
  "v_mfma_f32_16x16x32_bf16 a[32:35], v[8:11], v[192:195], a[32:35]\n" \
  "ds_read_b64_tr_b16 v[80:81], v224 offset:41472\n" \
  "v_mfma_f32_16x16x32_bf16 a[36:39], v[8:11], v[200:203], a[36:39]\n" \
  "ds_read_b64_tr_b16 v[82:83], v224 offset:42496\n" \
  "v_mfma_f32_16x16x32_bf16 a[40:43], v[8:11], v[208:211], a[40:43]\n" \
  "ds_read_b64_tr_b16 v[84:85], v225 offset:41472\n" \
  "v_mfma_f32_16x16x32_bf16 a[44:47], v[8:11], v[216:219], a[44:47]\n" \
  "ds_read_b64_tr_b16 v[86:87], v225 offset:42496\n" \
  "v_mfma_f32_16x16x32_bf16 a[48:51], v[12:15], v[192:195], a[48:51]\n" \
  "ds_read_b64_tr_b16 v[88:89], v224 offset:41728\n" \
  "v_mfma_f32_16x16x32_bf16 a[52:55], v[12:15], v[200:203], a[52:55]\n" \
  "ds_read_b64_tr_b16 v[90:91], v224 offset:42752\n" \
  "v_mfma_f32_16x16x32_bf16 a[56:59], v[12:15], v[208:211], a[56:59]\n" \
  "ds_read_b64_tr_b16 v[92:93], v225 offset:41728\n" \
  "v_mfma_f32_16x16x32_bf16 a[60:63], v[12:15], v[216:219], a[60:63]\n" \
  "ds_read_b64_tr_b16 v[94:95], v225 offset:42752\n" \
  "v_mfma_f32_16x16x32_bf16 a[64:67], v[16:19], v[192:195], a[64:67]\n" \
  "ds_read_b64_tr_b16 v[96:97], v228 offset:36864\n" \
  "v_mfma_f32_16x16x32_bf16 a[68:71], v[16:19], v[200:203], a[68:71]\n" \
  "ds_read_b64_tr_b16 v[98:99], v228 offset:37376\n" \
  "v_mfma_f32_16x16x32_bf16 a[72:75], v[16:19], v[208:211], a[72:75]\n" \
  "ds_read_b64_tr_b16 v[100:101], v229 offset:36864\n" \
  "v_mfma_f32_16x16x32_bf16 a[76:79], v[16:19], v[216:219], a[76:79]\n" \
  "ds_read_b64_tr_b16 v[102:103], v229 offset:37376\n" \
  "v_mfma_f32_16x16x32_bf16 a[80:83], v[20:23], v[192:195], a[80:83]\n" \
  "ds_read_b64_tr_b16 v[104:105], v228 offset:37120\n" \
  "v_mfma_f32_16x16x32_bf16 a[84:87], v[20:23], v[200:203], a[84:87]\n" \
  "ds_read_b64_tr_b16 v[106:107], v228 offset:37632\n" \
  "v_mfma_f32_16x16x32_bf16 a[88:91], v[20:23], v[208:211], a[88:91]\n" \
  "ds_read_b64_tr_b16 v[108:109], v229 offset:37120\n" \
  "v_mfma_f32_16x16x32_bf16 a[92:95], v[20:23], v[216:219], a[92:95]\n" \
  "ds_read_b64_tr_b16 v[110:111], v229 offset:37632\n" \
  "v_mfma_f32_16x16x32_bf16 a[96:99], v[24:27], v[192:195], a[96:99]\n" \
  "ds_read_b64_tr_b16 v[112:113], v228 offset:45056\n" \
  "v_mfma_f32_16x16x32_bf16 a[100:103], v[24:27], v[200:203], a[100:103]\n" \
  "ds_read_b64_tr_b16 v[114:115], v228 offset:45568\n" \
  "v_mfma_f32_16x16x32_bf16 a[104:107], v[24:27], v[208:211], a[104:107]\n" \
  "ds_read_b64_tr_b16 v[116:117], v229 offset:45056\n" \
  "v_mfma_f32_16x16x32_bf16 a[108:111], v[24:27], v[216:219], a[108:111]\n" \
  "ds_read_b64_tr_b16 v[118:119], v229 offset:45568\n" \
  "v_mfma_f32_16x16x32_bf16 a[112:115], v[28:31], v[192:195], a[112:115]\n" \
  "ds_read_b64_tr_b16 v[120:121], v228 offset:45312\n" \
  "v_mfma_f32_16x16x32_bf16 a[116:119], v[28:31], v[200:203], a[116:119]\n" \
  "ds_read_b64_tr_b16 v[122:123], v228 offset:45824\n" \
  "v_mfma_f32_16x16x32_bf16 a[120:123], v[28:31], v[208:211], a[120:123]\n" \
  "ds_read_b64_tr_b16 v[124:125], v229 offset:45312\n" \
  "v_mfma_f32_16x16x32_bf16 a[124:127], v[28:31], v[216:219], a[124:127]\n" \
  "ds_read_b64_tr_b16 v[126:127], v229 offset:45824\n" \
  "v_mfma_f32_16x16x32_bf16 a[128:131], v[32:35], v[192:195], a[128:131]\n" \
  "buffer_load_dwordx4 v[160:163], v232, s[72:75], s92 offen offset:0\n" \
  "v_mfma_f32_16x16x32_bf16 a[132:135], v[32:35], v[200:203], a[132:135]\n" \
  "v_mfma_f32_16x16x32_bf16 a[136:139], v[32:35], v[208:211], a[136:139]\n" \
  "buffer_load_dwordx4 v[168:171], v233, s[72:75], s92 offen offset:0\n" \
  "v_mfma_f32_16x16x32_bf16 a[140:143], v[32:35], v[216:219], a[140:143]\n" \
  "v_mfma_f32_16x16x32_bf16 a[144:147], v[36:39], v[192:195], a[144:147]\n" \
  "buffer_load_dwordx4 v[176:179], v234, s[72:75], s92 offen offset:0\n" \
  "v_mfma_f32_16x16x32_bf16 a[148:151], v[36:39], v[200:203], a[148:151]\n" \
  "v_mfma_f32_16x16x32_bf16 a[152:155], v[36:39], v[208:211], a[152:155]\n" \
  "buffer_load_dwordx4 v[184:187], v235, s[72:75], s92 offen offset:0\n" \
  "v_mfma_f32_16x16x32_bf16 a[156:159], v[36:39], v[216:219], a[156:159]\n" \
  "v_mfma_f32_16x16x32_bf16 a[160:163], v[40:43], v[192:195], a[160:163]\n" \
  "buffer_load_dwordx4 v[164:167], v232, s[72:75], s92 offen offset:1024\n" \
  "v_mfma_f32_16x16x32_bf16 a[164:167], v[40:43], v[200:203], a[164:167]\n" \
  "v_mfma_f32_16x16x32_bf16 a[168:171], v[40:43], v[208:211], a[168:171]\n" \
  "buffer_load_dwordx4 v[172:175], v233, s[72:75], s92 offen offset:1024\n" \
  "v_mfma_f32_16x16x32_bf16 a[172:175], v[40:43], v[216:219], a[172:175]\n" \
  "v_mfma_f32_16x16x32_bf16 a[176:179], v[44:47], v[192:195], a[176:179]\n" \
  "buffer_load_dwordx4 v[180:183], v234, s[72:75], s92 offen offset:1024\n" \
  "v_mfma_f32_16x16x32_bf16 a[180:183], v[44:47], v[200:203], a[180:183]\n" \
  "v_mfma_f32_16x16x32_bf16 a[184:187], v[44:47], v[208:211], a[184:187]\n" \
  "buffer_load_dwordx4 v[188:191], v235, s[72:75], s92 offen offset:1024\n" \
  "s_add_u32 s92, s92, 2048\n" \
  "v_mfma_f32_16x16x32_bf16 a[188:191], v[44:47], v[216:219], a[188:191]\n" \
  "v_mfma_f32_16x16x32_bf16 a[192:195], v[48:51], v[192:195], a[192:195]\n" \
  "v_mfma_f32_16x16x32_bf16 a[196:199], v[48:51], v[200:203], a[196:199]\n" \
  "v_mfma_f32_16x16x32_bf16 a[200:203], v[48:51], v[208:211], a[200:203]\n" \
  "v_mfma_f32_16x16x32_bf16 a[204:207], v[48:51], v[216:219], a[204:207]\n" \
  "v_mfma_f32_16x16x32_bf16 a[208:211], v[52:55], v[192:195], a[208:211]\n" \
  "v_mfma_f32_16x16x32_bf16 a[212:215], v[52:55], v[200:203], a[212:215]\n" \
  "v_mfma_f32_16x16x32_bf16 a[216:219], v[52:55], v[208:211], a[216:219]\n" \
  "v_mfma_f32_16x16x32_bf16 a[220:223], v[52:55], v[216:219], a[220:223]\n" \
  "v_mfma_f32_16x16x32_bf16 a[224:227], v[56:59], v[192:195], a[224:227]\n" \
  "v_mfma_f32_16x16x32_bf16 a[228:231], v[56:59], v[200:203], a[228:231]\n" \
  "v_mfma_f32_16x16x32_bf16 a[232:235], v[56:59], v[208:211], a[232:235]\n" \
  "v_mfma_f32_16x16x32_bf16 a[236:239], v[56:59], v[216:219], a[236:239]\n" \
  "v_mfma_f32_16x16x32_bf16 a[240:243], v[60:63], v[192:195], a[240:243]\n" \
  "v_mfma_f32_16x16x32_bf16 a[244:247], v[60:63], v[200:203], a[244:247]\n" \
  "v_mfma_f32_16x16x32_bf16 a[248:251], v[60:63], v[208:211], a[248:251]\n" \
  "v_mfma_f32_16x16x32_bf16 a[252:255], v[60:63], v[216:219], a[252:255]\n" \
  "s_waitcnt lgkmcnt(0)\n" \
  "v_mfma_f32_16x16x32_bf16 a[0:3], v[64:67], v[196:199], a[0:3]\n" \
  "v_mfma_f32_16x16x32_bf16 a[4:7], v[64:67], v[204:207], a[4:7]\n" \
  "v_mfma_f32_16x16x32_bf16 a[8:11], v[64:67], v[212:215], a[8:11]\n" \
  "v_mfma_f32_16x16x32_bf16 a[12:15], v[64:67], v[220:223], a[12:15]\n" \
  "v_mfma_f32_16x16x32_bf16 a[16:19], v[68:71], v[196:199], a[16:19]\n" \
  "v_mfma_f32_16x16x32_bf16 a[20:23], v[68:71], v[204:207], a[20:23]\n" \
  "v_mfma_f32_16x16x32_bf16 a[24:27], v[68:71], v[212:215], a[24:27]\n" \
  "v_mfma_f32_16x16x32_bf16 a[28:31], v[68:71], v[220:223], a[28:31]\n" \
  "s_waitcnt vmcnt(40)\n" \
  "s_barrier\n" \
  "v_mfma_f32_16x16x32_bf16 a[32:35], v[72:75], v[196:199], a[32:35]\n" \
  "ds_read_b64_tr_b16 v[0:1], v226 offset:0\n" \
  "v_mfma_f32_16x16x32_bf16 a[36:39], v[72:75], v[204:207], a[36:39]\n" \
  "ds_read_b64_tr_b16 v[2:3], v226 offset:1024\n" \
  "v_mfma_f32_16x16x32_bf16 a[40:43], v[72:75], v[212:215], a[40:43]\n" \
  "ds_read_b64_tr_b16 v[4:5], v227 offset:0\n" \
  "v_mfma_f32_16x16x32_bf16 a[44:47], v[72:75], v[220:223], a[44:47]\n" \
  "ds_read_b64_tr_b16 v[6:7], v227 offset:1024\n" \
  "v_mfma_f32_16x16x32_bf16 a[48:51], v[76:79], v[196:199], a[48:51]\n" \
  "ds_read_b64_tr_b16 v[8:9], v226 offset:256\n" \
  "v_mfma_f32_16x16x32_bf16 a[52:55], v[76:79], v[204:207], a[52:55]\n" \
  "ds_read_b64_tr_b16 v[10:11], v226 offset:1280\n" \
  "v_mfma_f32_16x16x32_bf16 a[56:59], v[76:79], v[212:215], a[56:59]\n" \
  "ds_read_b64_tr_b16 v[12:13], v227 offset:256\n" \
  "v_mfma_f32_16x16x32_bf16 a[60:63], v[76:79], v[220:223], a[60:63]\n" \
  "ds_read_b64_tr_b16 v[14:15], v227 offset:1280\n" \
  "v_mfma_f32_16x16x32_bf16 a[64:67], v[80:83], v[196:199], a[64:67]\n" \
  "s_add_u32 m0, s95, 32768\n" \
  "s_add_u32 s97, s95, 32768\n" \
  "buffer_load_dwordx4 v242, s[40:43], s97 offen lds\n" \
  "v_mfma_f32_16x16x32_bf16 a[68:71], v[80:83], v[204:207], a[68:71]\n" \
  "ds_read_b64_tr_b16 v[16:17], v226 offset:512\n" \
  "v_mfma_f32_16x16x32_bf16 a[72:75], v[80:83], v[212:215], a[72:75]\n" \
  "ds_read_b64_tr_b16 v[18:19], v226 offset:1536\n" \
  "v_mfma_f32_16x16x32_bf16 a[76:79], v[80:83], v[220:223], a[76:79]\n" \
  "buffer_load_dwordx4 v243, s[40:43], s97 offen offset:1024 lds\n" \
  "v_mfma_f32_16x16x32_bf16 a[80:83], v[84:87], v[196:199], a[80:83]\n" \
  "ds_read_b64_tr_b16 v[20:21], v227 offset:512\n" \
  "v_mfma_f32_16x16x32_bf16 a[84:87], v[84:87], v[204:207], a[84:87]\n" \
  "ds_read_b64_tr_b16 v[22:23], v227 offset:1536\n" \
  "v_mfma_f32_16x16x32_bf16 a[88:91], v[84:87], v[212:215], a[88:91]\n" \
  "buffer_load_dwordx4 v244, s[40:43], s97 offen offset:2048 lds\n" \
  "v_mfma_f32_16x16x32_bf16 a[92:95], v[84:87], v[220:223], a[92:95]\n" \
  "ds_read_b64_tr_b16 v[24:25], v226 offset:768\n" \
  "v_mfma_f32_16x16x32_bf16 a[96:99], v[88:91], v[196:199], a[96:99]\n" \
  "ds_read_b64_tr_b16 v[26:27], v226 offset:1792\n" \
  "v_mfma_f32_16x16x32_bf16 a[100:103], v[88:91], v[204:207], a[100:103]\n" \
  "buffer_load_dwordx4 v245, s[40:43], s97 offen offset:3072 lds\n" \
  "v_mfma_f32_16x16x32_bf16 a[104:107], v[88:91], v[212:215], a[104:107]\n" \
  "ds_read_b64_tr_b16 v[28:29], v227 offset:768\n" \
  "v_mfma_f32_16x16x32_bf16 a[108:111], v[88:91], v[220:223], a[108:111]\n" \
  "ds_read_b64_tr_b16 v[30:31], v227 offset:1792\n" \
  "v_mfma_f32_16x16x32_bf16 a[112:115], v[92:95], v[196:199], a[112:115]\n" \
  "ds_read_b64_tr_b16 v[32:33], v230 offset:0\n" \
  "v_mfma_f32_16x16x32_bf16 a[116:119], v[92:95], v[204:207], a[116:119]\n" \
  "ds_read_b64_tr_b16 v[34:35], v230 offset:512\n" \
  "v_mfma_f32_16x16x32_bf16 a[120:123], v[92:95], v[212:215], a[120:123]\n" \
  "ds_read_b64_tr_b16 v[36:37], v231 offset:0\n" \
  "v_mfma_f32_16x16x32_bf16 a[124:127], v[92:95], v[220:223], a[124:127]\n" \
  "ds_read_b64_tr_b16 v[38:39], v231 offset:512\n" \
  "v_mfma_f32_16x16x32_bf16 a[128:131], v[96:99], v[196:199], a[128:131]\n" \
  "ds_read_b64_tr_b16 v[40:41], v230 offset:256\n" \
  "v_mfma_f32_16x16x32_bf16 a[132:135], v[96:99], v[204:207], a[132:135]\n" \
  "ds_read_b64_tr_b16 v[42:43], v230 offset:768\n" \
  "v_mfma_f32_16x16x32_bf16 a[136:139], v[96:99], v[212:215], a[136:139]\n" \
  "ds_read_b64_tr_b16 v[44:45], v231 offset:256\n" \
  "v_mfma_f32_16x16x32_bf16 a[140:143], v[96:99], v[220:223], a[140:143]\n" \
  "ds_read_b64_tr_b16 v[46:47], v231 offset:768\n" \
  "v_mfma_f32_16x16x32_bf16 a[144:147], v[100:103], v[196:199], a[144:147]\n" \
  "ds_read_b64_tr_b16 v[48:49], v230 offset:8192\n" \
  "v_mfma_f32_16x16x32_bf16 a[148:151], v[100:103], v[204:207], a[148:151]\n" \
  "ds_read_b64_tr_b16 v[50:51], v230 offset:8704\n" \
  "v_mfma_f32_16x16x32_bf16 a[152:155], v[100:103], v[212:215], a[152:155]\n" \
  "ds_read_b64_tr_b16 v[52:53], v231 offset:8192\n" \
  "v_mfma_f32_16x16x32_bf16 a[156:159], v[100:103], v[220:223], a[156:159]\n" \
  "ds_read_b64_tr_b16 v[54:55], v231 offset:8704\n" \
  "v_mfma_f32_16x16x32_bf16 a[160:163], v[104:107], v[196:199], a[160:163]\n" \
  "ds_read_b64_tr_b16 v[56:57], v230 offset:8448\n" \
  "v_mfma_f32_16x16x32_bf16 a[164:167], v[104:107], v[204:207], a[164:167]\n" \
  "ds_read_b64_tr_b16 v[58:59], v230 offset:8960\n" \
  "v_mfma_f32_16x16x32_bf16 a[168:171], v[104:107], v[212:215], a[168:171]\n" \
  "ds_read_b64_tr_b16 v[60:61], v231 offset:8448\n" \
  "v_mfma_f32_16x16x32_bf16 a[172:175], v[104:107], v[220:223], a[172:175]\n" \
  "ds_read_b64_tr_b16 v[62:63], v231 offset:8960\n" \
  "v_mfma_f32_16x16x32_bf16 a[176:179], v[108:111], v[196:199], a[176:179]\n" \
  "v_mfma_f32_16x16x32_bf16 a[180:183], v[108:111], v[204:207], a[180:183]\n" \
  "v_mfma_f32_16x16x32_bf16 a[184:187], v[108:111], v[212:215], a[184:187]\n" \
  "v_mfma_f32_16x16x32_bf16 a[188:191], v[108:111], v[220:223], a[188:191]\n" \
  "v_mfma_f32_16x16x32_bf16 a[192:195], v[112:115], v[196:199], a[192:195]\n" \
  "v_mfma_f32_16x16x32_bf16 a[196:199], v[112:115], v[204:207], a[196:199]\n" \
  "v_mfma_f32_16x16x32_bf16 a[200:203], v[112:115], v[212:215], a[200:203]\n" \
  "v_mfma_f32_16x16x32_bf16 a[204:207], v[112:115], v[220:223], a[204:207]\n" \
  "v_mfma_f32_16x16x32_bf16 a[208:211], v[116:119], v[196:199], a[208:211]\n" \
  "v_mfma_f32_16x16x32_bf16 a[212:215], v[116:119], v[204:207], a[212:215]\n" \
  "v_mfma_f32_16x16x32_bf16 a[216:219], v[116:119], v[212:215], a[216:219]\n" \
  "v_mfma_f32_16x16x32_bf16 a[220:223], v[116:119], v[220:223], a[220:223]\n" \
  "v_mfma_f32_16x16x32_bf16 a[224:227], v[120:123], v[196:199], a[224:227]\n" \
  "v_mfma_f32_16x16x32_bf16 a[228:231], v[120:123], v[204:207], a[228:231]\n" \
  "v_mfma_f32_16x16x32_bf16 a[232:235], v[120:123], v[212:215], a[232:235]\n" \
  "v_mfma_f32_16x16x32_bf16 a[236:239], v[120:123], v[220:223], a[236:239]\n" \
  "v_mfma_f32_16x16x32_bf16 a[240:243], v[124:127], v[196:199], a[240:243]\n" \
  "v_mfma_f32_16x16x32_bf16 a[244:247], v[124:127], v[204:207], a[244:247]\n" \
  "v_mfma_f32_16x16x32_bf16 a[248:251], v[124:127], v[212:215], a[248:251]\n" \
  "v_mfma_f32_16x16x32_bf16 a[252:255], v[124:127], v[220:223], a[252:255]\n" \
  "s_waitcnt vmcnt(20) lgkmcnt(0)\n" \
  "v_mfma_f32_16x16x32_bf16 a[0:3], v[0:3], v[128:131], a[0:3]\n" \
  "ds_read_b64_tr_b16 v[64:65], v226 offset:8192\n" \
  "v_mfma_f32_16x16x32_bf16 a[4:7], v[0:3], v[136:139], a[4:7]\n" \
  "ds_read_b64_tr_b16 v[66:67], v226 offset:9216\n" \
  "v_mfma_f32_16x16x32_bf16 a[8:11], v[0:3], v[144:147], a[8:11]\n" \
  "ds_read_b64_tr_b16 v[68:69], v227 offset:8192\n" \
  "v_mfma_f32_16x16x32_bf16 a[12:15], v[0:3], v[152:155], a[12:15]\n" \
  "ds_read_b64_tr_b16 v[70:71], v227 offset:9216\n" \
  "v_mfma_f32_16x16x32_bf16 a[16:19], v[4:7], v[128:131], a[16:19]\n" \
  "ds_read_b64_tr_b16 v[72:73], v226 offset:8448\n" \
  "v_mfma_f32_16x16x32_bf16 a[20:23], v[4:7], v[136:139], a[20:23]\n" \
  "ds_read_b64_tr_b16 v[74:75], v226 offset:9472\n" \
  "v_mfma_f32_16x16x32_bf16 a[24:27], v[4:7], v[144:147], a[24:27]\n" \
  "ds_read_b64_tr_b16 v[76:77], v227 offset:8448\n" \
  "v_mfma_f32_16x16x32_bf16 a[28:31], v[4:7], v[152:155], a[28:31]\n" \
  "ds_read_b64_tr_b16 v[78:79], v227 offset:9472\n" \
  "v_mfma_f32_16x16x32_bf16 a[32:35], v[8:11], v[128:131], a[32:35]\n" \
  "ds_read_b64_tr_b16 v[80:81], v226 offset:8704\n" \
  "v_mfma_f32_16x16x32_bf16 a[36:39], v[8:11], v[136:139], a[36:39]\n" \
  "ds_read_b64_tr_b16 v[82:83], v226 offset:9728\n" \
  "v_mfma_f32_16x16x32_bf16 a[40:43], v[8:11], v[144:147], a[40:43]\n" \
  "ds_read_b64_tr_b16 v[84:85], v227 offset:8704\n" \
  "v_mfma_f32_16x16x32_bf16 a[44:47], v[8:11], v[152:155], a[44:47]\n" \
  "ds_read_b64_tr_b16 v[86:87], v227 offset:9728\n" \
  "v_mfma_f32_16x16x32_bf16 a[48:51], v[12:15], v[128:131], a[48:51]\n" \
  "ds_read_b64_tr_b16 v[88:89], v226 offset:8960\n" \
  "v_mfma_f32_16x16x32_bf16 a[52:55], v[12:15], v[136:139], a[52:55]\n" \
  "ds_read_b64_tr_b16 v[90:91], v226 offset:9984\n" \
  "v_mfma_f32_16x16x32_bf16 a[56:59], v[12:15], v[144:147], a[56:59]\n" \
  "ds_read_b64_tr_b16 v[92:93], v227 offset:8960\n" \
  "v_mfma_f32_16x16x32_bf16 a[60:63], v[12:15], v[152:155], a[60:63]\n" \
  "ds_read_b64_tr_b16 v[94:95], v227 offset:9984\n" \
  "v_mfma_f32_16x16x32_bf16 a[64:67], v[16:19], v[128:131], a[64:67]\n" \
  "ds_read_b64_tr_b16 v[96:97], v230 offset:4096\n" \
  "v_mfma_f32_16x16x32_bf16 a[68:71], v[16:19], v[136:139], a[68:71]\n" \
  "ds_read_b64_tr_b16 v[98:99], v230 offset:4608\n" \
  "v_mfma_f32_16x16x32_bf16 a[72:75], v[16:19], v[144:147], a[72:75]\n" \
  "ds_read_b64_tr_b16 v[100:101], v231 offset:4096\n" \
  "v_mfma_f32_16x16x32_bf16 a[76:79], v[16:19], v[152:155], a[76:79]\n" \
  "ds_read_b64_tr_b16 v[102:103], v231 offset:4608\n" \
  "v_mfma_f32_16x16x32_bf16 a[80:83], v[20:23], v[128:131], a[80:83]\n" \
  "ds_read_b64_tr_b16 v[104:105], v230 offset:4352\n" \
  "v_mfma_f32_16x16x32_bf16 a[84:87], v[20:23], v[136:139], a[84:87]\n" \
  "ds_read_b64_tr_b16 v[106:107], v230 offset:4864\n" \
  "v_mfma_f32_16x16x32_bf16 a[88:91], v[20:23], v[144:147], a[88:91]\n" \
  "ds_read_b64_tr_b16 v[108:109], v231 offset:4352\n" \
  "v_mfma_f32_16x16x32_bf16 a[92:95], v[20:23], v[152:155], a[92:95]\n" \
  "ds_read_b64_tr_b16 v[110:111], v231 offset:4864\n" \
  "v_mfma_f32_16x16x32_bf16 a[96:99], v[24:27], v[128:131], a[96:99]\n" \
  "ds_read_b64_tr_b16 v[112:113], v230 offset:12288\n" \
  "v_mfma_f32_16x16x32_bf16 a[100:103], v[24:27], v[136:139], a[100:103]\n" \
  "ds_read_b64_tr_b16 v[114:115], v230 offset:12800\n" \
  "v_mfma_f32_16x16x32_bf16 a[104:107], v[24:27], v[144:147], a[104:107]\n" \
  "ds_read_b64_tr_b16 v[116:117], v231 offset:12288\n" \
  "v_mfma_f32_16x16x32_bf16 a[108:111], v[24:27], v[152:155], a[108:111]\n" \
  "ds_read_b64_tr_b16 v[118:119], v231 offset:12800\n" \
  "v_mfma_f32_16x16x32_bf16 a[112:115], v[28:31], v[128:131], a[112:115]\n" \
  "ds_read_b64_tr_b16 v[120:121], v230 offset:12544\n" \
  "v_mfma_f32_16x16x32_bf16 a[116:119], v[28:31], v[136:139], a[116:119]\n" \
  "ds_read_b64_tr_b16 v[122:123], v230 offset:13056\n" \
  "v_mfma_f32_16x16x32_bf16 a[120:123], v[28:31], v[144:147], a[120:123]\n" \
  "ds_read_b64_tr_b16 v[124:125], v231 offset:12544\n" \
  "v_mfma_f32_16x16x32_bf16 a[124:127], v[28:31], v[152:155], a[124:127]\n" \
  "ds_read_b64_tr_b16 v[126:127], v231 offset:13056\n" \
  "v_mfma_f32_16x16x32_bf16 a[128:131], v[32:35], v[128:131], a[128:131]\n" \
  "v_mfma_f32_16x16x32_bf16 a[132:135], v[32:35], v[136:139], a[132:135]\n" \
  "v_mfma_f32_16x16x32_bf16 a[136:139], v[32:35], v[144:147], a[136:139]\n" \
  "v_mfma_f32_16x16x32_bf16 a[140:143], v[32:35], v[152:155], a[140:143]\n" \
  "v_mfma_f32_16x16x32_bf16 a[144:147], v[36:39], v[128:131], a[144:147]\n" \
  "v_mfma_f32_16x16x32_bf16 a[148:151], v[36:39], v[136:139], a[148:151]\n" \
  "v_mfma_f32_16x16x32_bf16 a[152:155], v[36:39], v[144:147], a[152:155]\n" \
  "v_mfma_f32_16x16x32_bf16 a[156:159], v[36:39], v[152:155], a[156:159]\n" \
  "v_mfma_f32_16x16x32_bf16 a[160:163], v[40:43], v[128:131], a[160:163]\n" \
  "v_mfma_f32_16x16x32_bf16 a[164:167], v[40:43], v[136:139], a[164:167]\n" \
  "v_mfma_f32_16x16x32_bf16 a[168:171], v[40:43], v[144:147], a[168:171]\n" \
  "v_mfma_f32_16x16x32_bf16 a[172:175], v[40:43], v[152:155], a[172:175]\n" \
  "v_mfma_f32_16x16x32_bf16 a[176:179], v[44:47], v[128:131], a[176:179]\n" \
  "v_mfma_f32_16x16x32_bf16 a[180:183], v[44:47], v[136:139], a[180:183]\n" \
  "v_mfma_f32_16x16x32_bf16 a[184:187], v[44:47], v[144:147], a[184:187]\n" \
  "v_mfma_f32_16x16x32_bf16 a[188:191], v[44:47], v[152:155], a[188:191]\n" \
  "v_mfma_f32_16x16x32_bf16 a[192:195], v[48:51], v[128:131], a[192:195]\n" \
  "v_mfma_f32_16x16x32_bf16 a[196:199], v[48:51], v[136:139], a[196:199]\n" \
  "v_mfma_f32_16x16x32_bf16 a[200:203], v[48:51], v[144:147], a[200:203]\n" \
  "v_mfma_f32_16x16x32_bf16 a[204:207], v[48:51], v[152:155], a[204:207]\n" \
  "v_mfma_f32_16x16x32_bf16 a[208:211], v[52:55], v[128:131], a[208:211]\n" \
  "v_mfma_f32_16x16x32_bf16 a[212:215], v[52:55], v[136:139], a[212:215]\n" \
  "v_mfma_f32_16x16x32_bf16 a[216:219], v[52:55], v[144:147], a[216:219]\n" \
  "v_mfma_f32_16x16x32_bf16 a[220:223], v[52:55], v[152:155], a[220:223]\n" \
  "v_mfma_f32_16x16x32_bf16 a[224:227], v[56:59], v[128:131], a[224:227]\n" \
  "v_mfma_f32_16x16x32_bf16 a[228:231], v[56:59], v[136:139], a[228:231]\n" \
  "v_mfma_f32_16x16x32_bf16 a[232:235], v[56:59], v[144:147], a[232:235]\n" \
  "v_mfma_f32_16x16x32_bf16 a[236:239], v[56:59], v[152:155], a[236:239]\n" \
  "v_mfma_f32_16x16x32_bf16 a[240:243], v[60:63], v[128:131], a[240:243]\n" \
  "v_mfma_f32_16x16x32_bf16 a[244:247], v[60:63], v[136:139], a[244:247]\n" \
  "v_mfma_f32_16x16x32_bf16 a[248:251], v[60:63], v[144:147], a[248:251]\n" \
  "v_mfma_f32_16x16x32_bf16 a[252:255], v[60:63], v[152:155], a[252:255]\n" \
  "s_waitcnt lgkmcnt(0)\n" \
  "v_mfma_f32_16x16x32_bf16 a[0:3], v[64:67], v[132:135], a[0:3]\n" \
  "v_mfma_f32_16x16x32_bf16 a[4:7], v[64:67], v[140:143], a[4:7]\n" \
  "v_mfma_f32_16x16x32_bf16 a[8:11], v[64:67], v[148:151], a[8:11]\n" \
  "v_mfma_f32_16x16x32_bf16 a[12:15], v[64:67], v[156:159], a[12:15]\n" \
  "v_mfma_f32_16x16x32_bf16 a[16:19], v[68:71], v[132:135], a[16:19]\n" \
  "v_mfma_f32_16x16x32_bf16 a[20:23], v[68:71], v[140:143], a[20:23]\n" \
  "v_mfma_f32_16x16x32_bf16 a[24:27], v[68:71], v[148:151], a[24:27]\n" \
  "v_mfma_f32_16x16x32_bf16 a[28:31], v[68:71], v[156:159], a[28:31]\n" \
  "s_waitcnt vmcnt(28)\n" \
  "s_barrier\n" \
  "v_mfma_f32_16x16x32_bf16 a[32:35], v[72:75], v[132:135], a[32:35]\n" \
  "ds_read_b64_tr_b16 v[0:1], v226 offset:32768\n" \
  "v_mfma_f32_16x16x32_bf16 a[36:39], v[72:75], v[140:143], a[36:39]\n" \
  "ds_read_b64_tr_b16 v[2:3], v226 offset:33792\n" \
  "v_mfma_f32_16x16x32_bf16 a[40:43], v[72:75], v[148:151], a[40:43]\n" \
  "ds_read_b64_tr_b16 v[4:5], v227 offset:32768\n" \
  "v_mfma_f32_16x16x32_bf16 a[44:47], v[72:75], v[156:159], a[44:47]\n" \
  "ds_read_b64_tr_b16 v[6:7], v227 offset:33792\n" \
  "v_mfma_f32_16x16x32_bf16 a[48:51], v[76:79], v[132:135], a[48:51]\n" \
  "ds_read_b64_tr_b16 v[8:9], v226 offset:33024\n" \
  "v_mfma_f32_16x16x32_bf16 a[52:55], v[76:79], v[140:143], a[52:55]\n" \
  "ds_read_b64_tr_b16 v[10:11], v226 offset:34048\n" \
  "v_mfma_f32_16x16x32_bf16 a[56:59], v[76:79], v[148:151], a[56:59]\n" \
  "ds_read_b64_tr_b16 v[12:13], v227 offset:33024\n" \
  "v_mfma_f32_16x16x32_bf16 a[60:63], v[76:79], v[156:159], a[60:63]\n" \
  "ds_read_b64_tr_b16 v[14:15], v227 offset:34048\n" \
  "v_mfma_f32_16x16x32_bf16 a[64:67], v[80:83], v[132:135], a[64:67]\n" \
  "s_add_u32 m0, s95, 65536\n" \
  "s_add_u32 s97, s95, 0\n" \
  "buffer_load_dwordx4 v246, s[44:47], s97 offen lds\n" \
  "v_mfma_f32_16x16x32_bf16 a[68:71], v[80:83], v[140:143], a[68:71]\n" \
  "ds_read_b64_tr_b16 v[16:17], v226 offset:33280\n" \
  "v_mfma_f32_16x16x32_bf16 a[72:75], v[80:83], v[148:151], a[72:75]\n" \
  "ds_read_b64_tr_b16 v[18:19], v226 offset:34304\n" \
  "v_mfma_f32_16x16x32_bf16 a[76:79], v[80:83], v[156:159], a[76:79]\n" \
  "buffer_load_dwordx4 v247, s[44:47], s97 offen offset:1024 lds\n" \
  "v_mfma_f32_16x16x32_bf16 a[80:83], v[84:87], v[132:135], a[80:83]\n" \
  "ds_read_b64_tr_b16 v[20:21], v227 offset:33280\n" \
  "v_mfma_f32_16x16x32_bf16 a[84:87], v[84:87], v[140:143], a[84:87]\n" \
  "ds_read_b64_tr_b16 v[22:23], v227 offset:34304\n" \
  "v_mfma_f32_16x16x32_bf16 a[88:91], v[84:87], v[148:151], a[88:91]\n" \
  "buffer_load_dwordx4 v246, s[44:47], s97 offen offset:2048 lds\n" \
  "v_mfma_f32_16x16x32_bf16 a[92:95], v[84:87], v[156:159], a[92:95]\n" \
  "ds_read_b64_tr_b16 v[24:25], v226 offset:33536\n" \
  "v_mfma_f32_16x16x32_bf16 a[96:99], v[88:91], v[132:135], a[96:99]\n" \
  "ds_read_b64_tr_b16 v[26:27], v226 offset:34560\n" \
  "v_mfma_f32_16x16x32_bf16 a[100:103], v[88:91], v[140:143], a[100:103]\n" \
  "buffer_load_dwordx4 v247, s[44:47], s97 offen offset:3072 lds\n" \
  "v_mfma_f32_16x16x32_bf16 a[104:107], v[88:91], v[148:151], a[104:107]\n" \
  "ds_read_b64_tr_b16 v[28:29], v227 offset:33536\n" \
  "v_mfma_f32_16x16x32_bf16 a[108:111], v[88:91], v[156:159], a[108:111]\n" \
  "ds_read_b64_tr_b16 v[30:31], v227 offset:34560\n" \
  "v_mfma_f32_16x16x32_bf16 a[112:115], v[92:95], v[132:135], a[112:115]\n" \
  "s_add_u32 m0, s95, 81920\n" \
  "s_add_u32 s97, s95, 16384\n" \
  "buffer_load_dwordx4 v246, s[44:47], s97 offen lds\n" \
  "v_mfma_f32_16x16x32_bf16 a[116:119], v[92:95], v[140:143], a[116:119]\n" \
  "ds_read_b64_tr_b16 v[32:33], v230 offset:32768\n" \
  "v_mfma_f32_16x16x32_bf16 a[120:123], v[92:95], v[148:151], a[120:123]\n" \
  "ds_read_b64_tr_b16 v[34:35], v230 offset:33280\n" \
  "v_mfma_f32_16x16x32_bf16 a[124:127], v[92:95], v[156:159], a[124:127]\n" \
  "buffer_load_dwordx4 v247, s[44:47], s97 offen offset:1024 lds\n" \
  "v_mfma_f32_16x16x32_bf16 a[128:131], v[96:99], v[132:135], a[128:131]\n" \
  "ds_read_b64_tr_b16 v[36:37], v231 offset:32768\n" \
  "v_mfma_f32_16x16x32_bf16 a[132:135], v[96:99], v[140:143], a[132:135]\n" \
  "ds_read_b64_tr_b16 v[38:39], v231 offset:33280\n" \
  "v_mfma_f32_16x16x32_bf16 a[136:139], v[96:99], v[148:151], a[136:139]\n" \
  "buffer_load_dwordx4 v246, s[44:47], s97 offen offset:2048 lds\n" \
  "v_mfma_f32_16x16x32_bf16 a[140:143], v[96:99], v[156:159], a[140:143]\n" \
  "ds_read_b64_tr_b16 v[40:41], v230 offset:33024\n" \
  "v_mfma_f32_16x16x32_bf16 a[144:147], v[100:103], v[132:135], a[144:147]\n" \
  "ds_read_b64_tr_b16 v[42:43], v230 offset:33536\n" \
  "v_mfma_f32_16x16x32_bf16 a[148:151], v[100:103], v[140:143], a[148:151]\n" \
  "buffer_load_dwordx4 v247, s[44:47], s97 offen offset:3072 lds\n" \
  "v_mfma_f32_16x16x32_bf16 a[152:155], v[100:103], v[148:151], a[152:155]\n" \
  "ds_read_b64_tr_b16 v[44:45], v231 offset:33024\n" \
  "v_mfma_f32_16x16x32_bf16 a[156:159], v[100:103], v[156:159], a[156:159]\n" \
  "ds_read_b64_tr_b16 v[46:47], v231 offset:33536\n" \
  "v_mfma_f32_16x16x32_bf16 a[160:163], v[104:107], v[132:135], a[160:163]\n" \
  "ds_read_b64_tr_b16 v[48:49], v230 offset:40960\n" \
  "v_mfma_f32_16x16x32_bf16 a[164:167], v[104:107], v[140:143], a[164:167]\n" \
  "ds_read_b64_tr_b16 v[50:51], v230 offset:41472\n" \
  "v_mfma_f32_16x16x32_bf16 a[168:171], v[104:107], v[148:151], a[168:171]\n" \
  "ds_read_b64_tr_b16 v[52:53], v231 offset:40960\n" \
  "v_mfma_f32_16x16x32_bf16 a[172:175], v[104:107], v[156:159], a[172:175]\n" \
  "ds_read_b64_tr_b16 v[54:55], v231 offset:41472\n" \
  "v_mfma_f32_16x16x32_bf16 a[176:179], v[108:111], v[132:135], a[176:179]\n" \
  "ds_read_b64_tr_b16 v[56:57], v230 offset:41216\n" \
  "v_mfma_f32_16x16x32_bf16 a[180:183], v[108:111], v[140:143], a[180:183]\n" \
  "ds_read_b64_tr_b16 v[58:59], v230 offset:41728\n" \
  "v_mfma_f32_16x16x32_bf16 a[184:187], v[108:111], v[148:151], a[184:187]\n" \
  "ds_read_b64_tr_b16 v[60:61], v231 offset:41216\n" \
  "v_mfma_f32_16x16x32_bf16 a[188:191], v[108:111], v[156:159], a[188:191]\n" \
  "ds_read_b64_tr_b16 v[62:63], v231 offset:41728\n" \
  "v_mfma_f32_16x16x32_bf16 a[192:195], v[112:115], v[132:135], a[192:195]\n" \
  "v_mfma_f32_16x16x32_bf16 a[196:199], v[112:115], v[140:143], a[196:199]\n" \
  "v_mfma_f32_16x16x32_bf16 a[200:203], v[112:115], v[148:151], a[200:203]\n" \
  "v_mfma_f32_16x16x32_bf16 a[204:207], v[112:115], v[156:159], a[204:207]\n" \
  "v_mfma_f32_16x16x32_bf16 a[208:211], v[116:119], v[132:135], a[208:211]\n" \
  "v_mfma_f32_16x16x32_bf16 a[212:215], v[116:119], v[140:143], a[212:215]\n" \
  "v_mfma_f32_16x16x32_bf16 a[216:219], v[116:119], v[148:151], a[216:219]\n" \
  "v_mfma_f32_16x16x32_bf16 a[220:223], v[116:119], v[156:159], a[220:223]\n" \
  "v_mfma_f32_16x16x32_bf16 a[224:227], v[120:123], v[132:135], a[224:227]\n" \
  "v_mfma_f32_16x16x32_bf16 a[228:231], v[120:123], v[140:143], a[228:231]\n" \
  "v_mfma_f32_16x16x32_bf16 a[232:235], v[120:123], v[148:151], a[232:235]\n" \
  "v_mfma_f32_16x16x32_bf16 a[236:239], v[120:123], v[156:159], a[236:239]\n" \
  "v_mfma_f32_16x16x32_bf16 a[240:243], v[124:127], v[132:135], a[240:243]\n" \
  "v_mfma_f32_16x16x32_bf16 a[244:247], v[124:127], v[140:143], a[244:247]\n" \
  "v_mfma_f32_16x16x32_bf16 a[248:251], v[124:127], v[148:151], a[248:251]\n" \
  "v_mfma_f32_16x16x32_bf16 a[252:255], v[124:127], v[156:159], a[252:255]\n" \
  "s_waitcnt vmcnt(12) lgkmcnt(0)\n" \
  "v_mfma_f32_16x16x32_bf16 a[0:3], v[0:3], v[160:163], a[0:3]\n" \
  "ds_read_b64_tr_b16 v[64:65], v226 offset:40960\n" \
  "v_mfma_f32_16x16x32_bf16 a[4:7], v[0:3], v[168:171], a[4:7]\n" \
  "ds_read_b64_tr_b16 v[66:67], v226 offset:41984\n" \
  "v_mfma_f32_16x16x32_bf16 a[8:11], v[0:3], v[176:179], a[8:11]\n" \
  "ds_read_b64_tr_b16 v[68:69], v227 offset:40960\n" \
  "v_mfma_f32_16x16x32_bf16 a[12:15], v[0:3], v[184:187], a[12:15]\n" \
  "ds_read_b64_tr_b16 v[70:71], v227 offset:41984\n" \
  "v_mfma_f32_16x16x32_bf16 a[16:19], v[4:7], v[160:163], a[16:19]\n" \
  "ds_read_b64_tr_b16 v[72:73], v226 offset:41216\n" \
  "v_mfma_f32_16x16x32_bf16 a[20:23], v[4:7], v[168:171], a[20:23]\n" \
  "ds_read_b64_tr_b16 v[74:75], v226 offset:42240\n" \
  "v_mfma_f32_16x16x32_bf16 a[24:27], v[4:7], v[176:179], a[24:27]\n" \
  "ds_read_b64_tr_b16 v[76:77], v227 offset:41216\n" \
  "v_mfma_f32_16x16x32_bf16 a[28:31], v[4:7], v[184:187], a[28:31]\n" \
  "ds_read_b64_tr_b16 v[78:79], v227 offset:42240\n" \
  "v_mfma_f32_16x16x32_bf16 a[32:35], v[8:11], v[160:163], a[32:35]\n" \
  "ds_read_b64_tr_b16 v[80:81], v226 offset:41472\n" \
  "v_mfma_f32_16x16x32_bf16 a[36:39], v[8:11], v[168:171], a[36:39]\n" \
  "ds_read_b64_tr_b16 v[82:83], v226 offset:42496\n" \
  "v_mfma_f32_16x16x32_bf16 a[40:43], v[8:11], v[176:179], a[40:43]\n" \
  "ds_read_b64_tr_b16 v[84:85], v227 offset:41472\n" \
  "v_mfma_f32_16x16x32_bf16 a[44:47], v[8:11], v[184:187], a[44:47]\n" \
  "ds_read_b64_tr_b16 v[86:87], v227 offset:42496\n" \
  "v_mfma_f32_16x16x32_bf16 a[48:51], v[12:15], v[160:163], a[48:51]\n" \
  "ds_read_b64_tr_b16 v[88:89], v226 offset:41728\n" \
  "v_mfma_f32_16x16x32_bf16 a[52:55], v[12:15], v[168:171], a[52:55]\n" \
  "ds_read_b64_tr_b16 v[90:91], v226 offset:42752\n" \
  "v_mfma_f32_16x16x32_bf16 a[56:59], v[12:15], v[176:179], a[56:59]\n" \
  "ds_read_b64_tr_b16 v[92:93], v227 offset:41728\n" \
  "v_mfma_f32_16x16x32_bf16 a[60:63], v[12:15], v[184:187], a[60:63]\n" \
  "ds_read_b64_tr_b16 v[94:95], v227 offset:42752\n" \
  "v_mfma_f32_16x16x32_bf16 a[64:67], v[16:19], v[160:163], a[64:67]\n" \
  "ds_read_b64_tr_b16 v[96:97], v230 offset:36864\n" \
  "v_mfma_f32_16x16x32_bf16 a[68:71], v[16:19], v[168:171], a[68:71]\n" \
  "ds_read_b64_tr_b16 v[98:99], v230 offset:37376\n" \
  "v_mfma_f32_16x16x32_bf16 a[72:75], v[16:19], v[176:179], a[72:75]\n" \
  "ds_read_b64_tr_b16 v[100:101], v231 offset:36864\n" \
  "v_mfma_f32_16x16x32_bf16 a[76:79], v[16:19], v[184:187], a[76:79]\n" \
  "ds_read_b64_tr_b16 v[102:103], v231 offset:37376\n" \
  "v_mfma_f32_16x16x32_bf16 a[80:83], v[20:23], v[160:163], a[80:83]\n" \
  "ds_read_b64_tr_b16 v[104:105], v230 offset:37120\n" \
  "v_mfma_f32_16x16x32_bf16 a[84:87], v[20:23], v[168:171], a[84:87]\n" \
  "ds_read_b64_tr_b16 v[106:107], v230 offset:37632\n" \
  "v_mfma_f32_16x16x32_bf16 a[88:91], v[20:23], v[176:179], a[88:91]\n" \
  "ds_read_b64_tr_b16 v[108:109], v231 offset:37120\n" \
  "v_mfma_f32_16x16x32_bf16 a[92:95], v[20:23], v[184:187], a[92:95]\n" \
  "ds_read_b64_tr_b16 v[110:111], v231 offset:37632\n" \
  "v_mfma_f32_16x16x32_bf16 a[96:99], v[24:27], v[160:163], a[96:99]\n" \
  "ds_read_b64_tr_b16 v[112:113], v230 offset:45056\n" \
  "v_mfma_f32_16x16x32_bf16 a[100:103], v[24:27], v[168:171], a[100:103]\n" \
  "ds_read_b64_tr_b16 v[114:115], v230 offset:45568\n" \
  "v_mfma_f32_16x16x32_bf16 a[104:107], v[24:27], v[176:179], a[104:107]\n" \
  "ds_read_b64_tr_b16 v[116:117], v231 offset:45056\n" \
  "v_mfma_f32_16x16x32_bf16 a[108:111], v[24:27], v[184:187], a[108:111]\n" \
  "ds_read_b64_tr_b16 v[118:119], v231 offset:45568\n" \
  "v_mfma_f32_16x16x32_bf16 a[112:115], v[28:31], v[160:163], a[112:115]\n" \
  "ds_read_b64_tr_b16 v[120:121], v230 offset:45312\n" \
  "v_mfma_f32_16x16x32_bf16 a[116:119], v[28:31], v[168:171], a[116:119]\n" \
  "ds_read_b64_tr_b16 v[122:123], v230 offset:45824\n" \
  "v_mfma_f32_16x16x32_bf16 a[120:123], v[28:31], v[176:179], a[120:123]\n" \
  "ds_read_b64_tr_b16 v[124:125], v231 offset:45312\n" \
  "v_mfma_f32_16x16x32_bf16 a[124:127], v[28:31], v[184:187], a[124:127]\n" \
  "ds_read_b64_tr_b16 v[126:127], v231 offset:45824\n" \
  "v_mfma_f32_16x16x32_bf16 a[128:131], v[32:35], v[160:163], a[128:131]\n" \
  "v_mfma_f32_16x16x32_bf16 a[132:135], v[32:35], v[168:171], a[132:135]\n" \
  "v_mfma_f32_16x16x32_bf16 a[136:139], v[32:35], v[176:179], a[136:139]\n" \
  "v_mfma_f32_16x16x32_bf16 a[140:143], v[32:35], v[184:187], a[140:143]\n" \
  "v_mfma_f32_16x16x32_bf16 a[144:147], v[36:39], v[160:163], a[144:147]\n" \
  "v_mfma_f32_16x16x32_bf16 a[148:151], v[36:39], v[168:171], a[148:151]\n" \
  "v_mfma_f32_16x16x32_bf16 a[152:155], v[36:39], v[176:179], a[152:155]\n" \
  "v_mfma_f32_16x16x32_bf16 a[156:159], v[36:39], v[184:187], a[156:159]\n" \
  "v_mfma_f32_16x16x32_bf16 a[160:163], v[40:43], v[160:163], a[160:163]\n" \
  "v_mfma_f32_16x16x32_bf16 a[164:167], v[40:43], v[168:171], a[164:167]\n" \
  "v_mfma_f32_16x16x32_bf16 a[168:171], v[40:43], v[176:179], a[168:171]\n" \
  "v_mfma_f32_16x16x32_bf16 a[172:175], v[40:43], v[184:187], a[172:175]\n" \
  "v_mfma_f32_16x16x32_bf16 a[176:179], v[44:47], v[160:163], a[176:179]\n" \
  "v_mfma_f32_16x16x32_bf16 a[180:183], v[44:47], v[168:171], a[180:183]\n" \
  "v_mfma_f32_16x16x32_bf16 a[184:187], v[44:47], v[176:179], a[184:187]\n" \
  "v_mfma_f32_16x16x32_bf16 a[188:191], v[44:47], v[184:187], a[188:191]\n" \
  "v_mfma_f32_16x16x32_bf16 a[192:195], v[48:51], v[160:163], a[192:195]\n" \
  "v_mfma_f32_16x16x32_bf16 a[196:199], v[48:51], v[168:171], a[196:199]\n" \
  "v_mfma_f32_16x16x32_bf16 a[200:203], v[48:51], v[176:179], a[200:203]\n" \
  "v_mfma_f32_16x16x32_bf16 a[204:207], v[48:51], v[184:187], a[204:207]\n" \
  "v_mfma_f32_16x16x32_bf16 a[208:211], v[52:55], v[160:163], a[208:211]\n" \
  "v_mfma_f32_16x16x32_bf16 a[212:215], v[52:55], v[168:171], a[212:215]\n" \
  "v_mfma_f32_16x16x32_bf16 a[216:219], v[52:55], v[176:179], a[216:219]\n" \
  "v_mfma_f32_16x16x32_bf16 a[220:223], v[52:55], v[184:187], a[220:223]\n" \
  "v_mfma_f32_16x16x32_bf16 a[224:227], v[56:59], v[160:163], a[224:227]\n" \
  "v_mfma_f32_16x16x32_bf16 a[228:231], v[56:59], v[168:171], a[228:231]\n" \
  "v_mfma_f32_16x16x32_bf16 a[232:235], v[56:59], v[176:179], a[232:235]\n" \
  "v_mfma_f32_16x16x32_bf16 a[236:239], v[56:59], v[184:187], a[236:239]\n" \
  "v_mfma_f32_16x16x32_bf16 a[240:243], v[60:63], v[160:163], a[240:243]\n" \
  "v_mfma_f32_16x16x32_bf16 a[244:247], v[60:63], v[168:171], a[244:247]\n" \
  "v_mfma_f32_16x16x32_bf16 a[248:251], v[60:63], v[176:179], a[248:251]\n" \
  "v_mfma_f32_16x16x32_bf16 a[252:255], v[60:63], v[184:187], a[252:255]\n" \
  "s_waitcnt lgkmcnt(0)\n" \
  "v_mfma_f32_16x16x32_bf16 a[0:3], v[64:67], v[164:167], a[0:3]\n" \
  "v_mfma_f32_16x16x32_bf16 a[4:7], v[64:67], v[172:175], a[4:7]\n" \
  "v_mfma_f32_16x16x32_bf16 a[8:11], v[64:67], v[180:183], a[8:11]\n" \
  "v_mfma_f32_16x16x32_bf16 a[12:15], v[64:67], v[188:191], a[12:15]\n" \
  "v_mfma_f32_16x16x32_bf16 a[16:19], v[68:71], v[164:167], a[16:19]\n" \
  "v_mfma_f32_16x16x32_bf16 a[20:23], v[68:71], v[172:175], a[20:23]\n" \
  "v_mfma_f32_16x16x32_bf16 a[24:27], v[68:71], v[180:183], a[24:27]\n" \
  "v_mfma_f32_16x16x32_bf16 a[28:31], v[68:71], v[188:191], a[28:31]\n" \
  "s_barrier\n" \
  "v_mfma_f32_16x16x32_bf16 a[32:35], v[72:75], v[164:167], a[32:35]\n" \
  "v_mfma_f32_16x16x32_bf16 a[36:39], v[72:75], v[172:175], a[36:39]\n" \
  "v_mfma_f32_16x16x32_bf16 a[40:43], v[72:75], v[180:183], a[40:43]\n" \
  "v_mfma_f32_16x16x32_bf16 a[44:47], v[72:75], v[188:191], a[44:47]\n" \
  "v_mfma_f32_16x16x32_bf16 a[48:51], v[76:79], v[164:167], a[48:51]\n" \
  "v_mfma_f32_16x16x32_bf16 a[52:55], v[76:79], v[172:175], a[52:55]\n" \
  "v_mfma_f32_16x16x32_bf16 a[56:59], v[76:79], v[180:183], a[56:59]\n" \
  "v_mfma_f32_16x16x32_bf16 a[60:63], v[76:79], v[188:191], a[60:63]\n" \
  "v_mfma_f32_16x16x32_bf16 a[64:67], v[80:83], v[164:167], a[64:67]\n" \
  "v_mfma_f32_16x16x32_bf16 a[68:71], v[80:83], v[172:175], a[68:71]\n" \
  "v_mfma_f32_16x16x32_bf16 a[72:75], v[80:83], v[180:183], a[72:75]\n" \
  "v_mfma_f32_16x16x32_bf16 a[76:79], v[80:83], v[188:191], a[76:79]\n" \
  "v_mfma_f32_16x16x32_bf16 a[80:83], v[84:87], v[164:167], a[80:83]\n" \
  "v_mfma_f32_16x16x32_bf16 a[84:87], v[84:87], v[172:175], a[84:87]\n" \
  "v_mfma_f32_16x16x32_bf16 a[88:91], v[84:87], v[180:183], a[88:91]\n" \
  "v_mfma_f32_16x16x32_bf16 a[92:95], v[84:87], v[188:191], a[92:95]\n" \
  "v_mfma_f32_16x16x32_bf16 a[96:99], v[88:91], v[164:167], a[96:99]\n" \
  "v_mfma_f32_16x16x32_bf16 a[100:103], v[88:91], v[172:175], a[100:103]\n" \
  "v_mfma_f32_16x16x32_bf16 a[104:107], v[88:91], v[180:183], a[104:107]\n" \
  "v_mfma_f32_16x16x32_bf16 a[108:111], v[88:91], v[188:191], a[108:111]\n" \
  "v_mfma_f32_16x16x32_bf16 a[112:115], v[92:95], v[164:167], a[112:115]\n" \
  "v_mfma_f32_16x16x32_bf16 a[116:119], v[92:95], v[172:175], a[116:119]\n" \
  "v_mfma_f32_16x16x32_bf16 a[120:123], v[92:95], v[180:183], a[120:123]\n" \
  "v_mfma_f32_16x16x32_bf16 a[124:127], v[92:95], v[188:191], a[124:127]\n" \
  "v_mfma_f32_16x16x32_bf16 a[128:131], v[96:99], v[164:167], a[128:131]\n" \
  "v_mfma_f32_16x16x32_bf16 a[132:135], v[96:99], v[172:175], a[132:135]\n" \
  "v_mfma_f32_16x16x32_bf16 a[136:139], v[96:99], v[180:183], a[136:139]\n" \
  "v_mfma_f32_16x16x32_bf16 a[140:143], v[96:99], v[188:191], a[140:143]\n" \
  "v_mfma_f32_16x16x32_bf16 a[144:147], v[100:103], v[164:167], a[144:147]\n" \
  "v_mfma_f32_16x16x32_bf16 a[148:151], v[100:103], v[172:175], a[148:151]\n" \
  "v_mfma_f32_16x16x32_bf16 a[152:155], v[100:103], v[180:183], a[152:155]\n" \
  "v_mfma_f32_16x16x32_bf16 a[156:159], v[100:103], v[188:191], a[156:159]\n" \
  "v_mfma_f32_16x16x32_bf16 a[160:163], v[104:107], v[164:167], a[160:163]\n" \
  "v_mfma_f32_16x16x32_bf16 a[164:167], v[104:107], v[172:175], a[164:167]\n" \
  "v_mfma_f32_16x16x32_bf16 a[168:171], v[104:107], v[180:183], a[168:171]\n" \
  "v_mfma_f32_16x16x32_bf16 a[172:175], v[104:107], v[188:191], a[172:175]\n" \
  "v_mfma_f32_16x16x32_bf16 a[176:179], v[108:111], v[164:167], a[176:179]\n" \
  "v_mfma_f32_16x16x32_bf16 a[180:183], v[108:111], v[172:175], a[180:183]\n" \
  "v_mfma_f32_16x16x32_bf16 a[184:187], v[108:111], v[180:183], a[184:187]\n" \
  "v_mfma_f32_16x16x32_bf16 a[188:191], v[108:111], v[188:191], a[188:191]\n" \
  "v_mfma_f32_16x16x32_bf16 a[192:195], v[112:115], v[164:167], a[192:195]\n" \
  "v_mfma_f32_16x16x32_bf16 a[196:199], v[112:115], v[172:175], a[196:199]\n" \
  "v_mfma_f32_16x16x32_bf16 a[200:203], v[112:115], v[180:183], a[200:203]\n" \
  "v_mfma_f32_16x16x32_bf16 a[204:207], v[112:115], v[188:191], a[204:207]\n" \
  "v_mfma_f32_16x16x32_bf16 a[208:211], v[116:119], v[164:167], a[208:211]\n" \
  "v_mfma_f32_16x16x32_bf16 a[212:215], v[116:119], v[172:175], a[212:215]\n" \
  "v_mfma_f32_16x16x32_bf16 a[216:219], v[116:119], v[180:183], a[216:219]\n" \
  "v_mfma_f32_16x16x32_bf16 a[220:223], v[116:119], v[188:191], a[220:223]\n" \
  "v_mfma_f32_16x16x32_bf16 a[224:227], v[120:123], v[164:167], a[224:227]\n" \
  "v_mfma_f32_16x16x32_bf16 a[228:231], v[120:123], v[172:175], a[228:231]\n" \
  "v_mfma_f32_16x16x32_bf16 a[232:235], v[120:123], v[180:183], a[232:235]\n" \
  "v_mfma_f32_16x16x32_bf16 a[236:239], v[120:123], v[188:191], a[236:239]\n" \
  "v_mfma_f32_16x16x32_bf16 a[240:243], v[124:127], v[164:167], a[240:243]\n" \
  "v_mfma_f32_16x16x32_bf16 a[244:247], v[124:127], v[172:175], a[244:247]\n" \
  "v_mfma_f32_16x16x32_bf16 a[248:251], v[124:127], v[180:183], a[248:251]\n" \
  "v_mfma_f32_16x16x32_bf16 a[252:255], v[124:127], v[188:191], a[252:255]\n" \
  "s_nop 7\n" \
  "s_nop 7\n" \
  "s_cmp_eq_u32 %24, 0\n" \
  "s_cbranch_scc1 NOH%=\n" \
  "v_lshlrev_b32 v251, 4, v249\n" \
  "v_lshrrev_b32 v253, 1, v250\n" \
  "v_lshl_add_u32 v251, v253, 8, v251\n" \
  "v_and_b32 v253, 1, v250\n" \
  "v_lshl_add_u32 v251, v253, 3, v251\n" \
  "s_mov_b32 s97, %23\n" \
  "s_mov_b32 s98, 0x3dd2d3e8\n" \
  "s_mov_b32 s99, 0x3dd2d3e8\n" \
  "v_mov_b32 v64, 0x40135761\n" \
  "v_mov_b32 v65, 0x40135761\n" \
  "v_mov_b32 v66, 1.0\n" \
  "v_mov_b32 v67, 1.0\n" \
  "v_accvgpr_read_b32 v0, a128\n" \
  "v_accvgpr_read_b32 v1, a129\n" \
  "v_accvgpr_read_b32 v2, a130\n" \
  "v_accvgpr_read_b32 v3, a131\n" \
  "v_accvgpr_read_b32 v4, a192\n" \
  "v_accvgpr_read_b32 v5, a193\n" \
  "v_accvgpr_read_b32 v6, a194\n" \
  "v_accvgpr_read_b32 v7, a195\n" \
  "v_pk_mul_f32 v[8:9], v[0:1], v[0:1]\n" \
  "v_pk_mul_f32 v[10:11], v[2:3], v[2:3]\n" \
  "v_pk_fma_f32 v[8:9], v[8:9], s[98:99], v[64:65]\n" \
  "v_pk_fma_f32 v[10:11], v[10:11], s[98:99], v[64:65]\n" \
  "v_pk_mul_f32 v[8:9], v[8:9], v[0:1]\n" \
  "v_pk_mul_f32 v[10:11], v[10:11], v[2:3]\n" \
  "v_exp_f32 v8, v8\n" \
  "v_exp_f32 v9, v9\n" \
  "v_exp_f32 v10, v10\n" \
  "v_exp_f32 v11, v11\n" \
  "v_pk_mul_f32 v[4:5], v[4:5], v[0:1]\n" \
  "v_pk_mul_f32 v[6:7], v[6:7], v[2:3]\n" \
  "v_pk_add_f32 v[8:9], v[8:9], v[66:67]\n" \
  "v_pk_add_f32 v[10:11], v[10:11], v[66:67]\n" \
  "v_rcp_f32 v8, v8\n" \
  "v_rcp_f32 v9, v9\n" \
  "v_rcp_f32 v10, v10\n" \
  "v_rcp_f32 v11, v11\n" \
  "s_nop 0\n" \
  "v_pk_fma_f32 v[8:9], v[4:5], v[8:9], v[4:5] neg_lo:[1,0,0] neg_hi:[1,0,0]\n" \
  "v_pk_fma_f32 v[10:11], v[6:7], v[10:11], v[6:7] neg_lo:[1,0,0] neg_hi:[1,0,0]\n" \
  "v_cvt_pk_bf16_f32 v12, v8, v9\n" \
  "v_cvt_pk_bf16_f32 v13, v10, v11\n" \
  "buffer_store_dwordx2 v[12:13], v251, s[88:91], s97 offen offset:0\n" \
  "v_accvgpr_read_b32 v16, a144\n" \
  "v_accvgpr_read_b32 v17, a145\n" \
  "v_accvgpr_read_b32 v18, a146\n" \
  "v_accvgpr_read_b32 v19, a147\n" \
  "v_accvgpr_read_b32 v20, a208\n" \
  "v_accvgpr_read_b32 v21, a209\n" \
  "v_accvgpr_read_b32 v22, a210\n" \
  "v_accvgpr_read_b32 v23, a211\n" \
  "v_pk_mul_f32 v[24:25], v[16:17], v[16:17]\n" \
  "v_pk_mul_f32 v[26:27], v[18:19], v[18:19]\n" \
  "v_pk_fma_f32 v[24:25], v[24:25], s[98:99], v[64:65]\n" \
  "v_pk_fma_f32 v[26:27], v[26:27], s[98:99], v[64:65]\n" \
  "v_pk_mul_f32 v[24:25], v[24:25], v[16:17]\n" \
  "v_pk_mul_f32 v[26:27], v[26:27], v[18:19]\n" \
  "v_exp_f32 v24, v24\n" \
  "v_exp_f32 v25, v25\n" \
  "v_exp_f32 v26, v26\n" \
  "v_exp_f32 v27, v27\n" \
  "v_pk_mul_f32 v[20:21], v[20:21], v[16:17]\n" \
  "v_pk_mul_f32 v[22:23], v[22:23], v[18:19]\n" \
  "v_pk_add_f32 v[24:25], v[24:25], v[66:67]\n" \
  "v_pk_add_f32 v[26:27], v[26:27], v[66:67]\n" \
  "v_rcp_f32 v24, v24\n" \
  "v_rcp_f32 v25, v25\n" \
  "v_rcp_f32 v26, v26\n" \
  "v_rcp_f32 v27, v27\n" \
  "s_nop 0\n" \
  "v_pk_fma_f32 v[24:25], v[20:21], v[24:25], v[20:21] neg_lo:[1,0,0] neg_hi:[1,0,0]\n" \
  "v_pk_fma_f32 v[26:27], v[22:23], v[26:27], v[22:23] neg_lo:[1,0,0] neg_hi:[1,0,0]\n" \
  "v_cvt_pk_bf16_f32 v28, v24, v25\n" \
  "v_cvt_pk_bf16_f32 v29, v26, v27\n" \
  "buffer_store_dwordx2 v[28:29], v251, s[88:91], s97 offen offset:512\n" \
  "v_accvgpr_read_b32 v32, a160\n" \
  "v_accvgpr_read_b32 v33, a161\n" \
  "v_accvgpr_read_b32 v34, a162\n" \
  "v_accvgpr_read_b32 v35, a163\n" \
  "v_accvgpr_read_b32 v36, a224\n" \
  "v_accvgpr_read_b32 v37, a225\n" \
  "v_accvgpr_read_b32 v38, a226\n" \
  "v_accvgpr_read_b32 v39, a227\n" \
  "v_pk_mul_f32 v[40:41], v[32:33], v[32:33]\n" \
  "v_pk_mul_f32 v[42:43], v[34:35], v[34:35]\n" \
  "v_pk_fma_f32 v[40:41], v[40:41], s[98:99], v[64:65]\n" \
  "v_pk_fma_f32 v[42:43], v[42:43], s[98:99], v[64:65]\n" \
  "v_pk_mul_f32 v[40:41], v[40:41], v[32:33]\n" \
  "v_pk_mul_f32 v[42:43], v[42:43], v[34:35]\n" \
  "v_exp_f32 v40, v40\n" \
  "v_exp_f32 v41, v41\n" \
  "v_exp_f32 v42, v42\n" \
  "v_exp_f32 v43, v43\n" \
  "v_pk_mul_f32 v[36:37], v[36:37], v[32:33]\n" \
  "v_pk_mul_f32 v[38:39], v[38:39], v[34:35]\n" \
  "v_pk_add_f32 v[40:41], v[40:41], v[66:67]\n" \
  "v_pk_add_f32 v[42:43], v[42:43], v[66:67]\n" \
  "v_rcp_f32 v40, v40\n" \
  "v_rcp_f32 v41, v41\n" \
  "v_rcp_f32 v42, v42\n" \
  "v_rcp_f32 v43, v43\n" \
  "s_nop 0\n" \
  "v_pk_fma_f32 v[40:41], v[36:37], v[40:41], v[36:37] neg_lo:[1,0,0] neg_hi:[1,0,0]\n" \
  "v_pk_fma_f32 v[42:43], v[38:39], v[42:43], v[38:39] neg_lo:[1,0,0] neg_hi:[1,0,0]\n" \
  "v_cvt_pk_bf16_f32 v44, v40, v41\n" \
  "v_cvt_pk_bf16_f32 v45, v42, v43\n" \
  "buffer_store_dwordx2 v[44:45], v251, s[88:91], s97 offen offset:1024\n" \
  "v_accvgpr_read_b32 v48, a176\n" \
  "v_accvgpr_read_b32 v49, a177\n" \
  "v_accvgpr_read_b32 v50, a178\n" \
  "v_accvgpr_read_b32 v51, a179\n" \
  "v_accvgpr_read_b32 v52, a240\n" \
  "v_accvgpr_read_b32 v53, a241\n" \
  "v_accvgpr_read_b32 v54, a242\n" \
  "v_accvgpr_read_b32 v55, a243\n" \
  "v_pk_mul_f32 v[56:57], v[48:49], v[48:49]\n" \
  "v_pk_mul_f32 v[58:59], v[50:51], v[50:51]\n" \
  "v_pk_fma_f32 v[56:57], v[56:57], s[98:99], v[64:65]\n" \
  "v_pk_fma_f32 v[58:59], v[58:59], s[98:99], v[64:65]\n" \
  "v_pk_mul_f32 v[56:57], v[56:57], v[48:49]\n" \
  "v_pk_mul_f32 v[58:59], v[58:59], v[50:51]\n" \
  "v_exp_f32 v56, v56\n" \
  "v_exp_f32 v57, v57\n" \
  "v_exp_f32 v58, v58\n" \
  "v_exp_f32 v59, v59\n" \
  "v_pk_mul_f32 v[52:53], v[52:53], v[48:49]\n" \
  "v_pk_mul_f32 v[54:55], v[54:55], v[50:51]\n" \
  "v_pk_add_f32 v[56:57], v[56:57], v[66:67]\n" \
  "v_pk_add_f32 v[58:59], v[58:59], v[66:67]\n" \
  "v_rcp_f32 v56, v56\n" \
  "v_rcp_f32 v57, v57\n" \
  "v_rcp_f32 v58, v58\n" \
  "v_rcp_f32 v59, v59\n" \
  "s_nop 0\n" \
  "v_pk_fma_f32 v[56:57], v[52:53], v[56:57], v[52:53] neg_lo:[1,0,0] neg_hi:[1,0,0]\n" \
  "v_pk_fma_f32 v[58:59], v[54:55], v[58:59], v[54:55] neg_lo:[1,0,0] neg_hi:[1,0,0]\n" \
  "v_cvt_pk_bf16_f32 v60, v56, v57\n" \
  "v_cvt_pk_bf16_f32 v61, v58, v59\n" \
  "buffer_store_dwordx2 v[60:61], v251, s[88:91], s97 offen offset:1536\n" \
  "s_add_u32 s97, s97, 57344\n" \
  "v_accvgpr_read_b32 v0, a132\n" \
  "v_accvgpr_read_b32 v1, a133\n" \
  "v_accvgpr_read_b32 v2, a134\n" \
  "v_accvgpr_read_b32 v3, a135\n" \
  "v_accvgpr_read_b32 v4, a196\n" \
  "v_accvgpr_read_b32 v5, a197\n" \
  "v_accvgpr_read_b32 v6, a198\n" \
  "v_accvgpr_read_b32 v7, a199\n" \
  "v_pk_mul_f32 v[8:9], v[0:1], v[0:1]\n" \
  "v_pk_mul_f32 v[10:11], v[2:3], v[2:3]\n" \
  "v_pk_fma_f32 v[8:9], v[8:9], s[98:99], v[64:65]\n" \
  "v_pk_fma_f32 v[10:11], v[10:11], s[98:99], v[64:65]\n" \
  "v_pk_mul_f32 v[8:9], v[8:9], v[0:1]\n" \
  "v_pk_mul_f32 v[10:11], v[10:11], v[2:3]\n" \
  "v_exp_f32 v8, v8\n" \
  "v_exp_f32 v9, v9\n" \
  "v_exp_f32 v10, v10\n" \
  "v_exp_f32 v11, v11\n" \
  "v_pk_mul_f32 v[4:5], v[4:5], v[0:1]\n" \
  "v_pk_mul_f32 v[6:7], v[6:7], v[2:3]\n" \
  "v_pk_add_f32 v[8:9], v[8:9], v[66:67]\n" \
  "v_pk_add_f32 v[10:11], v[10:11], v[66:67]\n" \
  "v_rcp_f32 v8, v8\n" \
  "v_rcp_f32 v9, v9\n" \
  "v_rcp_f32 v10, v10\n" \
  "v_rcp_f32 v11, v11\n" \
  "s_nop 0\n" \
  "v_pk_fma_f32 v[8:9], v[4:5], v[8:9], v[4:5] neg_lo:[1,0,0] neg_hi:[1,0,0]\n" \
  "v_pk_fma_f32 v[10:11], v[6:7], v[10:11], v[6:7] neg_lo:[1,0,0] neg_hi:[1,0,0]\n" \
  "v_cvt_pk_bf16_f32 v12, v8, v9\n" \
  "v_cvt_pk_bf16_f32 v13, v10, v11\n" \
  "buffer_store_dwordx2 v[12:13], v251, s[88:91], s97 offen offset:0\n" \
  "v_accvgpr_read_b32 v16, a148\n" \
  "v_accvgpr_read_b32 v17, a149\n" \
  "v_accvgpr_read_b32 v18, a150\n" \
  "v_accvgpr_read_b32 v19, a151\n" \
  "v_accvgpr_read_b32 v20, a212\n" \
  "v_accvgpr_read_b32 v21, a213\n" \
  "v_accvgpr_read_b32 v22, a214\n" \
  "v_accvgpr_read_b32 v23, a215\n" \
  "v_pk_mul_f32 v[24:25], v[16:17], v[16:17]\n" \
  "v_pk_mul_f32 v[26:27], v[18:19], v[18:19]\n" \
  "v_pk_fma_f32 v[24:25], v[24:25], s[98:99], v[64:65]\n" \
  "v_pk_fma_f32 v[26:27], v[26:27], s[98:99], v[64:65]\n" \
  "v_pk_mul_f32 v[24:25], v[24:25], v[16:17]\n" \
  "v_pk_mul_f32 v[26:27], v[26:27], v[18:19]\n" \
  "v_exp_f32 v24, v24\n" \
  "v_exp_f32 v25, v25\n" \
  "v_exp_f32 v26, v26\n" \
  "v_exp_f32 v27, v27\n" \
  "v_pk_mul_f32 v[20:21], v[20:21], v[16:17]\n" \
  "v_pk_mul_f32 v[22:23], v[22:23], v[18:19]\n" \
  "v_pk_add_f32 v[24:25], v[24:25], v[66:67]\n" \
  "v_pk_add_f32 v[26:27], v[26:27], v[66:67]\n" \
  "v_rcp_f32 v24, v24\n" \
  "v_rcp_f32 v25, v25\n" \
  "v_rcp_f32 v26, v26\n" \
  "v_rcp_f32 v27, v27\n" \
  "s_nop 0\n" \
  "v_pk_fma_f32 v[24:25], v[20:21], v[24:25], v[20:21] neg_lo:[1,0,0] neg_hi:[1,0,0]\n" \
  "v_pk_fma_f32 v[26:27], v[22:23], v[26:27], v[22:23] neg_lo:[1,0,0] neg_hi:[1,0,0]\n" \
  "v_cvt_pk_bf16_f32 v28, v24, v25\n" \
  "v_cvt_pk_bf16_f32 v29, v26, v27\n" \
  "buffer_store_dwordx2 v[28:29], v251, s[88:91], s97 offen offset:512\n" \
  "v_accvgpr_read_b32 v32, a164\n" \
  "v_accvgpr_read_b32 v33, a165\n" \
  "v_accvgpr_read_b32 v34, a166\n" \
  "v_accvgpr_read_b32 v35, a167\n" \
  "v_accvgpr_read_b32 v36, a228\n" \
  "v_accvgpr_read_b32 v37, a229\n" \
  "v_accvgpr_read_b32 v38, a230\n" \
  "v_accvgpr_read_b32 v39, a231\n" \
  "v_pk_mul_f32 v[40:41], v[32:33], v[32:33]\n" \
  "v_pk_mul_f32 v[42:43], v[34:35], v[34:35]\n" \
  "v_pk_fma_f32 v[40:41], v[40:41], s[98:99], v[64:65]\n" \
  "v_pk_fma_f32 v[42:43], v[42:43], s[98:99], v[64:65]\n" \
  "v_pk_mul_f32 v[40:41], v[40:41], v[32:33]\n" \
  "v_pk_mul_f32 v[42:43], v[42:43], v[34:35]\n" \
  "v_exp_f32 v40, v40\n" \
  "v_exp_f32 v41, v41\n" \
  "v_exp_f32 v42, v42\n" \
  "v_exp_f32 v43, v43\n" \
  "v_pk_mul_f32 v[36:37], v[36:37], v[32:33]\n" \
  "v_pk_mul_f32 v[38:39], v[38:39], v[34:35]\n" \
  "v_pk_add_f32 v[40:41], v[40:41], v[66:67]\n" \
  "v_pk_add_f32 v[42:43], v[42:43], v[66:67]\n" \
  "v_rcp_f32 v40, v40\n" \
  "v_rcp_f32 v41, v41\n" \
  "v_rcp_f32 v42, v42\n" \
  "v_rcp_f32 v43, v43\n" \
  "s_nop 0\n" \
  "v_pk_fma_f32 v[40:41], v[36:37], v[40:41], v[36:37] neg_lo:[1,0,0] neg_hi:[1,0,0]\n" \
  "v_pk_fma_f32 v[42:43], v[38:39], v[42:43], v[38:39] neg_lo:[1,0,0] neg_hi:[1,0,0]\n" \
  "v_cvt_pk_bf16_f32 v44, v40, v41\n" \
  "v_cvt_pk_bf16_f32 v45, v42, v43\n" \
  "buffer_store_dwordx2 v[44:45], v251, s[88:91], s97 offen offset:1024\n" \
  "v_accvgpr_read_b32 v48, a180\n" \
  "v_accvgpr_read_b32 v49, a181\n" \
  "v_accvgpr_read_b32 v50, a182\n" \
  "v_accvgpr_read_b32 v51, a183\n" \
  "v_accvgpr_read_b32 v52, a244\n" \
  "v_accvgpr_read_b32 v53, a245\n" \
  "v_accvgpr_read_b32 v54, a246\n" \
  "v_accvgpr_read_b32 v55, a247\n" \
  "v_pk_mul_f32 v[56:57], v[48:49], v[48:49]\n" \
  "v_pk_mul_f32 v[58:59], v[50:51], v[50:51]\n" \
  "v_pk_fma_f32 v[56:57], v[56:57], s[98:99], v[64:65]\n" \
  "v_pk_fma_f32 v[58:59], v[58:59], s[98:99], v[64:65]\n" \
  "v_pk_mul_f32 v[56:57], v[56:57], v[48:49]\n" \
  "v_pk_mul_f32 v[58:59], v[58:59], v[50:51]\n" \
  "v_exp_f32 v56, v56\n" \
  "v_exp_f32 v57, v57\n" \
  "v_exp_f32 v58, v58\n" \
  "v_exp_f32 v59, v59\n" \
  "v_pk_mul_f32 v[52:53], v[52:53], v[48:49]\n" \
  "v_pk_mul_f32 v[54:55], v[54:55], v[50:51]\n" \
  "v_pk_add_f32 v[56:57], v[56:57], v[66:67]\n" \
  "v_pk_add_f32 v[58:59], v[58:59], v[66:67]\n" \
  "v_rcp_f32 v56, v56\n" \
  "v_rcp_f32 v57, v57\n" \
  "v_rcp_f32 v58, v58\n" \
  "v_rcp_f32 v59, v59\n" \
  "s_nop 0\n" \
  "v_pk_fma_f32 v[56:57], v[52:53], v[56:57], v[52:53] neg_lo:[1,0,0] neg_hi:[1,0,0]\n" \
  "v_pk_fma_f32 v[58:59], v[54:55], v[58:59], v[54:55] neg_lo:[1,0,0] neg_hi:[1,0,0]\n" \
  "v_cvt_pk_bf16_f32 v60, v56, v57\n" \
  "v_cvt_pk_bf16_f32 v61, v58, v59\n" \
  "buffer_store_dwordx2 v[60:61], v251, s[88:91], s97 offen offset:1536\n" \
  "s_add_u32 s97, s97, 57344\n" \
  "v_accvgpr_read_b32 v0, a136\n" \
  "v_accvgpr_read_b32 v1, a137\n" \
  "v_accvgpr_read_b32 v2, a138\n" \
  "v_accvgpr_read_b32 v3, a139\n" \
  "v_accvgpr_read_b32 v4, a200\n" \
  "v_accvgpr_read_b32 v5, a201\n" \
  "v_accvgpr_read_b32 v6, a202\n" \
  "v_accvgpr_read_b32 v7, a203\n" \
  "v_pk_mul_f32 v[8:9], v[0:1], v[0:1]\n" \
  "v_pk_mul_f32 v[10:11], v[2:3], v[2:3]\n" \
  "v_pk_fma_f32 v[8:9], v[8:9], s[98:99], v[64:65]\n" \
  "v_pk_fma_f32 v[10:11], v[10:11], s[98:99], v[64:65]\n" \
  "v_pk_mul_f32 v[8:9], v[8:9], v[0:1]\n" \
  "v_pk_mul_f32 v[10:11], v[10:11], v[2:3]\n" \
  "v_exp_f32 v8, v8\n" \
  "v_exp_f32 v9, v9\n" \
  "v_exp_f32 v10, v10\n" \
  "v_exp_f32 v11, v11\n" \
  "v_pk_mul_f32 v[4:5], v[4:5], v[0:1]\n" \
  "v_pk_mul_f32 v[6:7], v[6:7], v[2:3]\n" \
  "v_pk_add_f32 v[8:9], v[8:9], v[66:67]\n" \
  "v_pk_add_f32 v[10:11], v[10:11], v[66:67]\n" \
  "v_rcp_f32 v8, v8\n" \
  "v_rcp_f32 v9, v9\n" \
  "v_rcp_f32 v10, v10\n" \
  "v_rcp_f32 v11, v11\n" \
  "s_nop 0\n" \
  "v_pk_fma_f32 v[8:9], v[4:5], v[8:9], v[4:5] neg_lo:[1,0,0] neg_hi:[1,0,0]\n" \
  "v_pk_fma_f32 v[10:11], v[6:7], v[10:11], v[6:7] neg_lo:[1,0,0] neg_hi:[1,0,0]\n" \
  "v_cvt_pk_bf16_f32 v12, v8, v9\n" \
  "v_cvt_pk_bf16_f32 v13, v10, v11\n" \
  "buffer_store_dwordx2 v[12:13], v251, s[88:91], s97 offen offset:0\n" \
  "v_accvgpr_read_b32 v16, a152\n" \
  "v_accvgpr_read_b32 v17, a153\n" \
  "v_accvgpr_read_b32 v18, a154\n" \
  "v_accvgpr_read_b32 v19, a155\n" \
  "v_accvgpr_read_b32 v20, a216\n" \
  "v_accvgpr_read_b32 v21, a217\n" \
  "v_accvgpr_read_b32 v22, a218\n" \
  "v_accvgpr_read_b32 v23, a219\n" \
  "v_pk_mul_f32 v[24:25], v[16:17], v[16:17]\n" \
  "v_pk_mul_f32 v[26:27], v[18:19], v[18:19]\n" \
  "v_pk_fma_f32 v[24:25], v[24:25], s[98:99], v[64:65]\n" \
  "v_pk_fma_f32 v[26:27], v[26:27], s[98:99], v[64:65]\n" \
  "v_pk_mul_f32 v[24:25], v[24:25], v[16:17]\n" \
  "v_pk_mul_f32 v[26:27], v[26:27], v[18:19]\n" \
  "v_exp_f32 v24, v24\n" \
  "v_exp_f32 v25, v25\n" \
  "v_exp_f32 v26, v26\n" \
  "v_exp_f32 v27, v27\n" \
  "v_pk_mul_f32 v[20:21], v[20:21], v[16:17]\n" \
  "v_pk_mul_f32 v[22:23], v[22:23], v[18:19]\n" \
  "v_pk_add_f32 v[24:25], v[24:25], v[66:67]\n" \
  "v_pk_add_f32 v[26:27], v[26:27], v[66:67]\n" \
  "v_rcp_f32 v24, v24\n" \
  "v_rcp_f32 v25, v25\n" \
  "v_rcp_f32 v26, v26\n" \
  "v_rcp_f32 v27, v27\n" \
  "s_nop 0\n" \
  "v_pk_fma_f32 v[24:25], v[20:21], v[24:25], v[20:21] neg_lo:[1,0,0] neg_hi:[1,0,0]\n" \
  "v_pk_fma_f32 v[26:27], v[22:23], v[26:27], v[22:23] neg_lo:[1,0,0] neg_hi:[1,0,0]\n" \
  "v_cvt_pk_bf16_f32 v28, v24, v25\n" \
  "v_cvt_pk_bf16_f32 v29, v26, v27\n" \
  "buffer_store_dwordx2 v[28:29], v251, s[88:91], s97 offen offset:512\n" \
  "v_accvgpr_read_b32 v32, a168\n" \
  "v_accvgpr_read_b32 v33, a169\n" \
  "v_accvgpr_read_b32 v34, a170\n" \
  "v_accvgpr_read_b32 v35, a171\n" \
  "v_accvgpr_read_b32 v36, a232\n" \
  "v_accvgpr_read_b32 v37, a233\n" \
  "v_accvgpr_read_b32 v38, a234\n" \
  "v_accvgpr_read_b32 v39, a235\n" \
  "v_pk_mul_f32 v[40:41], v[32:33], v[32:33]\n" \
  "v_pk_mul_f32 v[42:43], v[34:35], v[34:35]\n" \
  "v_pk_fma_f32 v[40:41], v[40:41], s[98:99], v[64:65]\n" \
  "v_pk_fma_f32 v[42:43], v[42:43], s[98:99], v[64:65]\n" \
  "v_pk_mul_f32 v[40:41], v[40:41], v[32:33]\n" \
  "v_pk_mul_f32 v[42:43], v[42:43], v[34:35]\n" \
  "v_exp_f32 v40, v40\n" \
  "v_exp_f32 v41, v41\n" \
  "v_exp_f32 v42, v42\n" \
  "v_exp_f32 v43, v43\n" \
  "v_pk_mul_f32 v[36:37], v[36:37], v[32:33]\n" \
  "v_pk_mul_f32 v[38:39], v[38:39], v[34:35]\n" \
  "v_pk_add_f32 v[40:41], v[40:41], v[66:67]\n" \
  "v_pk_add_f32 v[42:43], v[42:43], v[66:67]\n" \
  "v_rcp_f32 v40, v40\n" \
  "v_rcp_f32 v41, v41\n" \
  "v_rcp_f32 v42, v42\n" \
  "v_rcp_f32 v43, v43\n" \
  "s_nop 0\n" \
  "v_pk_fma_f32 v[40:41], v[36:37], v[40:41], v[36:37] neg_lo:[1,0,0] neg_hi:[1,0,0]\n" \
  "v_pk_fma_f32 v[42:43], v[38:39], v[42:43], v[38:39] neg_lo:[1,0,0] neg_hi:[1,0,0]\n" \
  "v_cvt_pk_bf16_f32 v44, v40, v41\n" \
  "v_cvt_pk_bf16_f32 v45, v42, v43\n" \
  "buffer_store_dwordx2 v[44:45], v251, s[88:91], s97 offen offset:1024\n" \
  "v_accvgpr_read_b32 v48, a184\n" \
  "v_accvgpr_read_b32 v49, a185\n" \
  "v_accvgpr_read_b32 v50, a186\n" \
  "v_accvgpr_read_b32 v51, a187\n" \
  "v_accvgpr_read_b32 v52, a248\n" \
  "v_accvgpr_read_b32 v53, a249\n" \
  "v_accvgpr_read_b32 v54, a250\n" \
  "v_accvgpr_read_b32 v55, a251\n" \
  "v_pk_mul_f32 v[56:57], v[48:49], v[48:49]\n" \
  "v_pk_mul_f32 v[58:59], v[50:51], v[50:51]\n" \
  "v_pk_fma_f32 v[56:57], v[56:57], s[98:99], v[64:65]\n" \
  "v_pk_fma_f32 v[58:59], v[58:59], s[98:99], v[64:65]\n" \
  "v_pk_mul_f32 v[56:57], v[56:57], v[48:49]\n" \
  "v_pk_mul_f32 v[58:59], v[58:59], v[50:51]\n" \
  "v_exp_f32 v56, v56\n" \
  "v_exp_f32 v57, v57\n" \
  "v_exp_f32 v58, v58\n" \
  "v_exp_f32 v59, v59\n" \
  "v_pk_mul_f32 v[52:53], v[52:53], v[48:49]\n" \
  "v_pk_mul_f32 v[54:55], v[54:55], v[50:51]\n" \
  "v_pk_add_f32 v[56:57], v[56:57], v[66:67]\n" \
  "v_pk_add_f32 v[58:59], v[58:59], v[66:67]\n" \
  "v_rcp_f32 v56, v56\n" \
  "v_rcp_f32 v57, v57\n" \
  "v_rcp_f32 v58, v58\n" \
  "v_rcp_f32 v59, v59\n" \
  "s_nop 0\n" \
  "v_pk_fma_f32 v[56:57], v[52:53], v[56:57], v[52:53] neg_lo:[1,0,0] neg_hi:[1,0,0]\n" \
  "v_pk_fma_f32 v[58:59], v[54:55], v[58:59], v[54:55] neg_lo:[1,0,0] neg_hi:[1,0,0]\n" \
  "v_cvt_pk_bf16_f32 v60, v56, v57\n" \
  "v_cvt_pk_bf16_f32 v61, v58, v59\n" \
  "buffer_store_dwordx2 v[60:61], v251, s[88:91], s97 offen offset:1536\n" \
  "s_add_u32 s97, s97, 57344\n" \
  "v_accvgpr_read_b32 v0, a140\n" \
  "v_accvgpr_read_b32 v1, a141\n" \
  "v_accvgpr_read_b32 v2, a142\n" \
  "v_accvgpr_read_b32 v3, a143\n" \
  "v_accvgpr_read_b32 v4, a204\n" \
  "v_accvgpr_read_b32 v5, a205\n" \
  "v_accvgpr_read_b32 v6, a206\n" \
  "v_accvgpr_read_b32 v7, a207\n" \
  "v_pk_mul_f32 v[8:9], v[0:1], v[0:1]\n" \
  "v_pk_mul_f32 v[10:11], v[2:3], v[2:3]\n" \
  "v_pk_fma_f32 v[8:9], v[8:9], s[98:99], v[64:65]\n" \
  "v_pk_fma_f32 v[10:11], v[10:11], s[98:99], v[64:65]\n" \
  "v_pk_mul_f32 v[8:9], v[8:9], v[0:1]\n" \
  "v_pk_mul_f32 v[10:11], v[10:11], v[2:3]\n" \
  "v_exp_f32 v8, v8\n" \
  "v_exp_f32 v9, v9\n" \
  "v_exp_f32 v10, v10\n" \
  "v_exp_f32 v11, v11\n" \
  "v_pk_mul_f32 v[4:5], v[4:5], v[0:1]\n" \
  "v_pk_mul_f32 v[6:7], v[6:7], v[2:3]\n" \
  "v_pk_add_f32 v[8:9], v[8:9], v[66:67]\n" \
  "v_pk_add_f32 v[10:11], v[10:11], v[66:67]\n" \
  "v_rcp_f32 v8, v8\n" \
  "v_rcp_f32 v9, v9\n" \
  "v_rcp_f32 v10, v10\n" \
  "v_rcp_f32 v11, v11\n" \
  "s_nop 0\n" \
  "v_pk_fma_f32 v[8:9], v[4:5], v[8:9], v[4:5] neg_lo:[1,0,0] neg_hi:[1,0,0]\n" \
  "v_pk_fma_f32 v[10:11], v[6:7], v[10:11], v[6:7] neg_lo:[1,0,0] neg_hi:[1,0,0]\n" \
  "v_cvt_pk_bf16_f32 v12, v8, v9\n" \
  "v_cvt_pk_bf16_f32 v13, v10, v11\n" \
  "buffer_store_dwordx2 v[12:13], v251, s[88:91], s97 offen offset:0\n" \
  "v_accvgpr_read_b32 v16, a156\n" \
  "v_accvgpr_read_b32 v17, a157\n" \
  "v_accvgpr_read_b32 v18, a158\n" \
  "v_accvgpr_read_b32 v19, a159\n" \
  "v_accvgpr_read_b32 v20, a220\n" \
  "v_accvgpr_read_b32 v21, a221\n" \
  "v_accvgpr_read_b32 v22, a222\n" \
  "v_accvgpr_read_b32 v23, a223\n" \
  "v_pk_mul_f32 v[24:25], v[16:17], v[16:17]\n" \
  "v_pk_mul_f32 v[26:27], v[18:19], v[18:19]\n" \
  "v_pk_fma_f32 v[24:25], v[24:25], s[98:99], v[64:65]\n" \
  "v_pk_fma_f32 v[26:27], v[26:27], s[98:99], v[64:65]\n" \
  "v_pk_mul_f32 v[24:25], v[24:25], v[16:17]\n" \
  "v_pk_mul_f32 v[26:27], v[26:27], v[18:19]\n" \
  "v_exp_f32 v24, v24\n" \
  "v_exp_f32 v25, v25\n" \
  "v_exp_f32 v26, v26\n" \
  "v_exp_f32 v27, v27\n" \
  "v_pk_mul_f32 v[20:21], v[20:21], v[16:17]\n" \
  "v_pk_mul_f32 v[22:23], v[22:23], v[18:19]\n" \
  "v_pk_add_f32 v[24:25], v[24:25], v[66:67]\n" \
  "v_pk_add_f32 v[26:27], v[26:27], v[66:67]\n" \
  "v_rcp_f32 v24, v24\n" \
  "v_rcp_f32 v25, v25\n" \
  "v_rcp_f32 v26, v26\n" \
  "v_rcp_f32 v27, v27\n" \
  "s_nop 0\n" \
  "v_pk_fma_f32 v[24:25], v[20:21], v[24:25], v[20:21] neg_lo:[1,0,0] neg_hi:[1,0,0]\n" \
  "v_pk_fma_f32 v[26:27], v[22:23], v[26:27], v[22:23] neg_lo:[1,0,0] neg_hi:[1,0,0]\n" \
  "v_cvt_pk_bf16_f32 v28, v24, v25\n" \
  "v_cvt_pk_bf16_f32 v29, v26, v27\n" \
  "buffer_store_dwordx2 v[28:29], v251, s[88:91], s97 offen offset:512\n" \
  "v_accvgpr_read_b32 v32, a172\n" \
  "v_accvgpr_read_b32 v33, a173\n" \
  "v_accvgpr_read_b32 v34, a174\n" \
  "v_accvgpr_read_b32 v35, a175\n" \
  "v_accvgpr_read_b32 v36, a236\n" \
  "v_accvgpr_read_b32 v37, a237\n" \
  "v_accvgpr_read_b32 v38, a238\n" \
  "v_accvgpr_read_b32 v39, a239\n" \
  "v_pk_mul_f32 v[40:41], v[32:33], v[32:33]\n" \
  "v_pk_mul_f32 v[42:43], v[34:35], v[34:35]\n" \
  "v_pk_fma_f32 v[40:41], v[40:41], s[98:99], v[64:65]\n" \
  "v_pk_fma_f32 v[42:43], v[42:43], s[98:99], v[64:65]\n" \
  "v_pk_mul_f32 v[40:41], v[40:41], v[32:33]\n" \
  "v_pk_mul_f32 v[42:43], v[42:43], v[34:35]\n" \
  "v_exp_f32 v40, v40\n" \
  "v_exp_f32 v41, v41\n" \
  "v_exp_f32 v42, v42\n" \
  "v_exp_f32 v43, v43\n" \
  "v_pk_mul_f32 v[36:37], v[36:37], v[32:33]\n" \
  "v_pk_mul_f32 v[38:39], v[38:39], v[34:35]\n" \
  "v_pk_add_f32 v[40:41], v[40:41], v[66:67]\n" \
  "v_pk_add_f32 v[42:43], v[42:43], v[66:67]\n" \
  "v_rcp_f32 v40, v40\n" \
  "v_rcp_f32 v41, v41\n" \
  "v_rcp_f32 v42, v42\n" \
  "v_rcp_f32 v43, v43\n" \
  "s_nop 0\n" \
  "v_pk_fma_f32 v[40:41], v[36:37], v[40:41], v[36:37] neg_lo:[1,0,0] neg_hi:[1,0,0]\n" \
  "v_pk_fma_f32 v[42:43], v[38:39], v[42:43], v[38:39] neg_lo:[1,0,0] neg_hi:[1,0,0]\n" \
  "v_cvt_pk_bf16_f32 v44, v40, v41\n" \
  "v_cvt_pk_bf16_f32 v45, v42, v43\n" \
  "buffer_store_dwordx2 v[44:45], v251, s[88:91], s97 offen offset:1024\n" \
  "v_accvgpr_read_b32 v48, a188\n" \
  "v_accvgpr_read_b32 v49, a189\n" \
  "v_accvgpr_read_b32 v50, a190\n" \
  "v_accvgpr_read_b32 v51, a191\n" \
  "v_accvgpr_read_b32 v52, a252\n" \
  "v_accvgpr_read_b32 v53, a253\n" \
  "v_accvgpr_read_b32 v54, a254\n" \
  "v_accvgpr_read_b32 v55, a255\n" \
  "v_pk_mul_f32 v[56:57], v[48:49], v[48:49]\n" \
  "v_pk_mul_f32 v[58:59], v[50:51], v[50:51]\n" \
  "v_pk_fma_f32 v[56:57], v[56:57], s[98:99], v[64:65]\n" \
  "v_pk_fma_f32 v[58:59], v[58:59], s[98:99], v[64:65]\n" \
  "v_pk_mul_f32 v[56:57], v[56:57], v[48:49]\n" \
  "v_pk_mul_f32 v[58:59], v[58:59], v[50:51]\n" \
  "v_exp_f32 v56, v56\n" \
  "v_exp_f32 v57, v57\n" \
  "v_exp_f32 v58, v58\n" \
  "v_exp_f32 v59, v59\n" \
  "v_pk_mul_f32 v[52:53], v[52:53], v[48:49]\n" \
  "v_pk_mul_f32 v[54:55], v[54:55], v[50:51]\n" \
  "v_pk_add_f32 v[56:57], v[56:57], v[66:67]\n" \
  "v_pk_add_f32 v[58:59], v[58:59], v[66:67]\n" \
  "v_rcp_f32 v56, v56\n" \
  "v_rcp_f32 v57, v57\n" \
  "v_rcp_f32 v58, v58\n" \
  "v_rcp_f32 v59, v59\n" \
  "s_nop 0\n" \
  "v_pk_fma_f32 v[56:57], v[52:53], v[56:57], v[52:53] neg_lo:[1,0,0] neg_hi:[1,0,0]\n" \
  "v_pk_fma_f32 v[58:59], v[54:55], v[58:59], v[54:55] neg_lo:[1,0,0] neg_hi:[1,0,0]\n" \
  "v_cvt_pk_bf16_f32 v60, v56, v57\n" \
  "v_cvt_pk_bf16_f32 v61, v58, v59\n" \
  "buffer_store_dwordx2 v[60:61], v251, s[88:91], s97 offen offset:1536\n" \
  "NOH%=:\n" \
  "v_lshrrev_b32 v251, 1, v250\n" \
  "v_lshl_add_u32 v251, v251, 4, v249\n" \
  "v_lshlrev_b32 v251, 4, v251\n" \
  "v_and_b32 v252, 1, v250\n" \
  "v_lshl_add_u32 v251, v252, 3, v251\n" \
  "v_add_u32 v251, s96, v251\n" \
  "s_mov_b32 s98, 0x3e0293ee\n" \
  "s_mov_b32 s99, 0x3e0293ee\n" \
  "v_accvgpr_read_b32 v64, a0\n" \
  "v_accvgpr_read_b32 v65, a1\n" \
  "v_accvgpr_read_b32 v66, a2\n" \
  "v_accvgpr_read_b32 v67, a3\n" \
  "v_pk_mul_f32 v[64:65], v[64:65], s[98:99]\n" \
  "v_pk_mul_f32 v[66:67], v[66:67], s[98:99]\n" \
  "v_cvt_pk_bf16_f32 v68, v64, v65\n" \
  "v_cvt_pk_bf16_f32 v69, v66, v67\n" \
  "ds_write_b64 v251, v[68:69] offset:0\n" \
  "v_accvgpr_read_b32 v72, a16\n" \
  "v_accvgpr_read_b32 v73, a17\n" \
  "v_accvgpr_read_b32 v74, a18\n" \
  "v_accvgpr_read_b32 v75, a19\n" \
  "v_pk_mul_f32 v[72:73], v[72:73], s[98:99]\n" \
  "v_pk_mul_f32 v[74:75], v[74:75], s[98:99]\n" \
  "v_cvt_pk_bf16_f32 v76, v72, v73\n" \
  "v_cvt_pk_bf16_f32 v77, v74, v75\n" \
  "ds_write_b64 v251, v[76:77] offset:512\n" \
  "v_accvgpr_read_b32 v80, a32\n" \
  "v_accvgpr_read_b32 v81, a33\n" \
  "v_accvgpr_read_b32 v82, a34\n" \
  "v_accvgpr_read_b32 v83, a35\n" \
  "v_pk_mul_f32 v[80:81], v[80:81], s[98:99]\n" \
  "v_pk_mul_f32 v[82:83], v[82:83], s[98:99]\n" \
  "v_cvt_pk_bf16_f32 v84, v80, v81\n" \
  "v_cvt_pk_bf16_f32 v85, v82, v83\n" \
  "ds_write_b64 v251, v[84:85] offset:1024\n" \
  "v_accvgpr_read_b32 v88, a48\n" \
  "v_accvgpr_read_b32 v89, a49\n" \
  "v_accvgpr_read_b32 v90, a50\n" \
  "v_accvgpr_read_b32 v91, a51\n" \
  "v_pk_mul_f32 v[88:89], v[88:89], s[98:99]\n" \
  "v_pk_mul_f32 v[90:91], v[90:91], s[98:99]\n" \
  "v_cvt_pk_bf16_f32 v92, v88, v89\n" \
  "v_cvt_pk_bf16_f32 v93, v90, v91\n" \
  "ds_write_b64 v251, v[92:93] offset:1536\n" \
  "v_accvgpr_read_b32 v96, a64\n" \
  "v_accvgpr_read_b32 v97, a65\n" \
  "v_accvgpr_read_b32 v98, a66\n" \
  "v_accvgpr_read_b32 v99, a67\n" \
  "v_pk_mul_f32 v[96:97], v[96:97], s[98:99]\n" \
  "v_pk_mul_f32 v[98:99], v[98:99], s[98:99]\n" \
  "v_cvt_pk_bf16_f32 v100, v96, v97\n" \
  "v_cvt_pk_bf16_f32 v101, v98, v99\n" \
  "ds_write_b64 v251, v[100:101] offset:2048\n" \
  "v_accvgpr_read_b32 v104, a80\n" \
  "v_accvgpr_read_b32 v105, a81\n" \
  "v_accvgpr_read_b32 v106, a82\n" \
  "v_accvgpr_read_b32 v107, a83\n" \
  "v_pk_mul_f32 v[104:105], v[104:105], s[98:99]\n" \
  "v_pk_mul_f32 v[106:107], v[106:107], s[98:99]\n" \
  "v_cvt_pk_bf16_f32 v108, v104, v105\n" \
  "v_cvt_pk_bf16_f32 v109, v106, v107\n" \
  "ds_write_b64 v251, v[108:109] offset:2560\n" \
  "v_accvgpr_read_b32 v112, a96\n" \
  "v_accvgpr_read_b32 v113, a97\n" \
  "v_accvgpr_read_b32 v114, a98\n" \
  "v_accvgpr_read_b32 v115, a99\n" \
  "v_pk_mul_f32 v[112:113], v[112:113], s[98:99]\n" \
  "v_pk_mul_f32 v[114:115], v[114:115], s[98:99]\n" \
  "v_cvt_pk_bf16_f32 v116, v112, v113\n" \
  "v_cvt_pk_bf16_f32 v117, v114, v115\n" \
  "ds_write_b64 v251, v[116:117] offset:3072\n" \
  "v_accvgpr_read_b32 v120, a112\n" \
  "v_accvgpr_read_b32 v121, a113\n" \
  "v_accvgpr_read_b32 v122, a114\n" \
  "v_accvgpr_read_b32 v123, a115\n" \
  "v_pk_mul_f32 v[120:121], v[120:121], s[98:99]\n" \
  "v_pk_mul_f32 v[122:123], v[122:123], s[98:99]\n" \
  "v_cvt_pk_bf16_f32 v124, v120, v121\n" \
  "v_cvt_pk_bf16_f32 v125, v122, v123\n" \
  "ds_write_b64 v251, v[124:125] offset:3584\n" \
  "v_accvgpr_read_b32 v64, a4\n" \
  "v_accvgpr_read_b32 v65, a5\n" \
  "v_accvgpr_read_b32 v66, a6\n" \
  "v_accvgpr_read_b32 v67, a7\n" \
  "v_pk_mul_f32 v[64:65], v[64:65], s[98:99]\n" \
  "v_pk_mul_f32 v[66:67], v[66:67], s[98:99]\n" \
  "v_cvt_pk_bf16_f32 v68, v64, v65\n" \
  "v_cvt_pk_bf16_f32 v69, v66, v67\n" \
  "ds_write_b64 v251, v[68:69] offset:4096\n" \
  "v_accvgpr_read_b32 v72, a20\n" \
  "v_accvgpr_read_b32 v73, a21\n" \
  "v_accvgpr_read_b32 v74, a22\n" \
  "v_accvgpr_read_b32 v75, a23\n" \
  "v_pk_mul_f32 v[72:73], v[72:73], s[98:99]\n" \
  "v_pk_mul_f32 v[74:75], v[74:75], s[98:99]\n" \
  "v_cvt_pk_bf16_f32 v76, v72, v73\n" \
  "v_cvt_pk_bf16_f32 v77, v74, v75\n" \
  "ds_write_b64 v251, v[76:77] offset:4608\n" \
  "v_accvgpr_read_b32 v80, a36\n" \
  "v_accvgpr_read_b32 v81, a37\n" \
  "v_accvgpr_read_b32 v82, a38\n" \
  "v_accvgpr_read_b32 v83, a39\n" \
  "v_pk_mul_f32 v[80:81], v[80:81], s[98:99]\n" \
  "v_pk_mul_f32 v[82:83], v[82:83], s[98:99]\n" \
  "v_cvt_pk_bf16_f32 v84, v80, v81\n" \
  "v_cvt_pk_bf16_f32 v85, v82, v83\n" \
  "ds_write_b64 v251, v[84:85] offset:5120\n" \
  "v_accvgpr_read_b32 v88, a52\n" \
  "v_accvgpr_read_b32 v89, a53\n" \
  "v_accvgpr_read_b32 v90, a54\n" \
  "v_accvgpr_read_b32 v91, a55\n" \
  "v_pk_mul_f32 v[88:89], v[88:89], s[98:99]\n" \
  "v_pk_mul_f32 v[90:91], v[90:91], s[98:99]\n" \
  "v_cvt_pk_bf16_f32 v92, v88, v89\n" \
  "v_cvt_pk_bf16_f32 v93, v90, v91\n" \
  "ds_write_b64 v251, v[92:93] offset:5632\n" \
  "v_accvgpr_read_b32 v96, a68\n" \
  "v_accvgpr_read_b32 v97, a69\n" \
  "v_accvgpr_read_b32 v98, a70\n" \
  "v_accvgpr_read_b32 v99, a71\n" \
  "v_pk_mul_f32 v[96:97], v[96:97], s[98:99]\n" \
  "v_pk_mul_f32 v[98:99], v[98:99], s[98:99]\n" \
  "v_cvt_pk_bf16_f32 v100, v96, v97\n" \
  "v_cvt_pk_bf16_f32 v101, v98, v99\n" \
  "ds_write_b64 v251, v[100:101] offset:6144\n" \
  "v_accvgpr_read_b32 v104, a84\n" \
  "v_accvgpr_read_b32 v105, a85\n" \
  "v_accvgpr_read_b32 v106, a86\n" \
  "v_accvgpr_read_b32 v107, a87\n" \
  "v_pk_mul_f32 v[104:105], v[104:105], s[98:99]\n" \
  "v_pk_mul_f32 v[106:107], v[106:107], s[98:99]\n" \
  "v_cvt_pk_bf16_f32 v108, v104, v105\n" \
  "v_cvt_pk_bf16_f32 v109, v106, v107\n" \
  "ds_write_b64 v251, v[108:109] offset:6656\n" \
  "v_accvgpr_read_b32 v112, a100\n" \
  "v_accvgpr_read_b32 v113, a101\n" \
  "v_accvgpr_read_b32 v114, a102\n" \
  "v_accvgpr_read_b32 v115, a103\n" \
  "v_pk_mul_f32 v[112:113], v[112:113], s[98:99]\n" \
  "v_pk_mul_f32 v[114:115], v[114:115], s[98:99]\n" \
  "v_cvt_pk_bf16_f32 v116, v112, v113\n" \
  "v_cvt_pk_bf16_f32 v117, v114, v115\n" \
  "ds_write_b64 v251, v[116:117] offset:7168\n" \
  "v_accvgpr_read_b32 v120, a116\n" \
  "v_accvgpr_read_b32 v121, a117\n" \
  "v_accvgpr_read_b32 v122, a118\n" \
  "v_accvgpr_read_b32 v123, a119\n" \
  "v_pk_mul_f32 v[120:121], v[120:121], s[98:99]\n" \
  "v_pk_mul_f32 v[122:123], v[122:123], s[98:99]\n" \
  "v_cvt_pk_bf16_f32 v124, v120, v121\n" \
  "v_cvt_pk_bf16_f32 v125, v122, v123\n" \
  "ds_write_b64 v251, v[124:125] offset:7680\n" \
  "v_accvgpr_read_b32 v64, a8\n" \
  "v_accvgpr_read_b32 v65, a9\n" \
  "v_accvgpr_read_b32 v66, a10\n" \
  "v_accvgpr_read_b32 v67, a11\n" \
  "v_pk_mul_f32 v[64:65], v[64:65], s[98:99]\n" \
  "v_pk_mul_f32 v[66:67], v[66:67], s[98:99]\n" \
  "v_cvt_pk_bf16_f32 v68, v64, v65\n" \
  "v_cvt_pk_bf16_f32 v69, v66, v67\n" \
  "ds_write_b64 v251, v[68:69] offset:8192\n" \
  "v_accvgpr_read_b32 v72, a24\n" \
  "v_accvgpr_read_b32 v73, a25\n" \
  "v_accvgpr_read_b32 v74, a26\n" \
  "v_accvgpr_read_b32 v75, a27\n" \
  "v_pk_mul_f32 v[72:73], v[72:73], s[98:99]\n" \
  "v_pk_mul_f32 v[74:75], v[74:75], s[98:99]\n" \
  "v_cvt_pk_bf16_f32 v76, v72, v73\n" \
  "v_cvt_pk_bf16_f32 v77, v74, v75\n" \
  "ds_write_b64 v251, v[76:77] offset:8704\n" \
  "v_accvgpr_read_b32 v80, a40\n" \
  "v_accvgpr_read_b32 v81, a41\n" \
  "v_accvgpr_read_b32 v82, a42\n" \
  "v_accvgpr_read_b32 v83, a43\n" \
  "v_pk_mul_f32 v[80:81], v[80:81], s[98:99]\n" \
  "v_pk_mul_f32 v[82:83], v[82:83], s[98:99]\n" \
  "v_cvt_pk_bf16_f32 v84, v80, v81\n" \
  "v_cvt_pk_bf16_f32 v85, v82, v83\n" \
  "ds_write_b64 v251, v[84:85] offset:9216\n" \
  "v_accvgpr_read_b32 v88, a56\n" \
  "v_accvgpr_read_b32 v89, a57\n" \
  "v_accvgpr_read_b32 v90, a58\n" \
  "v_accvgpr_read_b32 v91, a59\n" \
  "v_pk_mul_f32 v[88:89], v[88:89], s[98:99]\n" \
  "v_pk_mul_f32 v[90:91], v[90:91], s[98:99]\n" \
  "v_cvt_pk_bf16_f32 v92, v88, v89\n" \
  "v_cvt_pk_bf16_f32 v93, v90, v91\n" \
  "ds_write_b64 v251, v[92:93] offset:9728\n" \
  "v_accvgpr_read_b32 v96, a72\n" \
  "v_accvgpr_read_b32 v97, a73\n" \
  "v_accvgpr_read_b32 v98, a74\n" \
  "v_accvgpr_read_b32 v99, a75\n" \
  "v_pk_mul_f32 v[96:97], v[96:97], s[98:99]\n" \
  "v_pk_mul_f32 v[98:99], v[98:99], s[98:99]\n" \
  "v_cvt_pk_bf16_f32 v100, v96, v97\n" \
  "v_cvt_pk_bf16_f32 v101, v98, v99\n" \
  "ds_write_b64 v251, v[100:101] offset:10240\n" \
  "v_accvgpr_read_b32 v104, a88\n" \
  "v_accvgpr_read_b32 v105, a89\n" \
  "v_accvgpr_read_b32 v106, a90\n" \
  "v_accvgpr_read_b32 v107, a91\n" \
  "v_pk_mul_f32 v[104:105], v[104:105], s[98:99]\n" \
  "v_pk_mul_f32 v[106:107], v[106:107], s[98:99]\n" \
  "v_cvt_pk_bf16_f32 v108, v104, v105\n" \
  "v_cvt_pk_bf16_f32 v109, v106, v107\n" \
  "ds_write_b64 v251, v[108:109] offset:10752\n" \
  "v_accvgpr_read_b32 v112, a104\n" \
  "v_accvgpr_read_b32 v113, a105\n" \
  "v_accvgpr_read_b32 v114, a106\n" \
  "v_accvgpr_read_b32 v115, a107\n" \
  "v_pk_mul_f32 v[112:113], v[112:113], s[98:99]\n" \
  "v_pk_mul_f32 v[114:115], v[114:115], s[98:99]\n" \
  "v_cvt_pk_bf16_f32 v116, v112, v113\n" \
  "v_cvt_pk_bf16_f32 v117, v114, v115\n" \
  "ds_write_b64 v251, v[116:117] offset:11264\n" \
  "v_accvgpr_read_b32 v120, a120\n" \
  "v_accvgpr_read_b32 v121, a121\n" \
  "v_accvgpr_read_b32 v122, a122\n" \
  "v_accvgpr_read_b32 v123, a123\n" \
  "v_pk_mul_f32 v[120:121], v[120:121], s[98:99]\n" \
  "v_pk_mul_f32 v[122:123], v[122:123], s[98:99]\n" \
  "v_cvt_pk_bf16_f32 v124, v120, v121\n" \
  "v_cvt_pk_bf16_f32 v125, v122, v123\n" \
  "ds_write_b64 v251, v[124:125] offset:11776\n" \
  "v_accvgpr_read_b32 v64, a12\n" \
  "v_accvgpr_read_b32 v65, a13\n" \
  "v_accvgpr_read_b32 v66, a14\n" \
  "v_accvgpr_read_b32 v67, a15\n" \
  "v_pk_mul_f32 v[64:65], v[64:65], s[98:99]\n" \
  "v_pk_mul_f32 v[66:67], v[66:67], s[98:99]\n" \
  "v_cvt_pk_bf16_f32 v68, v64, v65\n" \
  "v_cvt_pk_bf16_f32 v69, v66, v67\n" \
  "ds_write_b64 v251, v[68:69] offset:12288\n" \
  "v_accvgpr_read_b32 v72, a28\n" \
  "v_accvgpr_read_b32 v73, a29\n" \
  "v_accvgpr_read_b32 v74, a30\n" \
  "v_accvgpr_read_b32 v75, a31\n" \
  "v_pk_mul_f32 v[72:73], v[72:73], s[98:99]\n" \
  "v_pk_mul_f32 v[74:75], v[74:75], s[98:99]\n" \
  "v_cvt_pk_bf16_f32 v76, v72, v73\n" \
  "v_cvt_pk_bf16_f32 v77, v74, v75\n" \
  "ds_write_b64 v251, v[76:77] offset:12800\n" \
  "v_accvgpr_read_b32 v80, a44\n" \
  "v_accvgpr_read_b32 v81, a45\n" \
  "v_accvgpr_read_b32 v82, a46\n" \
  "v_accvgpr_read_b32 v83, a47\n" \
  "v_pk_mul_f32 v[80:81], v[80:81], s[98:99]\n" \
  "v_pk_mul_f32 v[82:83], v[82:83], s[98:99]\n" \
  "v_cvt_pk_bf16_f32 v84, v80, v81\n" \
  "v_cvt_pk_bf16_f32 v85, v82, v83\n" \
  "ds_write_b64 v251, v[84:85] offset:13312\n" \
  "v_accvgpr_read_b32 v88, a60\n" \
  "v_accvgpr_read_b32 v89, a61\n" \
  "v_accvgpr_read_b32 v90, a62\n" \
  "v_accvgpr_read_b32 v91, a63\n" \
  "v_pk_mul_f32 v[88:89], v[88:89], s[98:99]\n" \
  "v_pk_mul_f32 v[90:91], v[90:91], s[98:99]\n" \
  "v_cvt_pk_bf16_f32 v92, v88, v89\n" \
  "v_cvt_pk_bf16_f32 v93, v90, v91\n" \
  "ds_write_b64 v251, v[92:93] offset:13824\n" \
  "v_accvgpr_read_b32 v96, a76\n" \
  "v_accvgpr_read_b32 v97, a77\n" \
  "v_accvgpr_read_b32 v98, a78\n" \
  "v_accvgpr_read_b32 v99, a79\n" \
  "v_pk_mul_f32 v[96:97], v[96:97], s[98:99]\n" \
  "v_pk_mul_f32 v[98:99], v[98:99], s[98:99]\n" \
  "v_cvt_pk_bf16_f32 v100, v96, v97\n" \
  "v_cvt_pk_bf16_f32 v101, v98, v99\n" \
  "ds_write_b64 v251, v[100:101] offset:14336\n" \
  "v_accvgpr_read_b32 v104, a92\n" \
  "v_accvgpr_read_b32 v105, a93\n" \
  "v_accvgpr_read_b32 v106, a94\n" \
  "v_accvgpr_read_b32 v107, a95\n" \
  "v_pk_mul_f32 v[104:105], v[104:105], s[98:99]\n" \
  "v_pk_mul_f32 v[106:107], v[106:107], s[98:99]\n" \
  "v_cvt_pk_bf16_f32 v108, v104, v105\n" \
  "v_cvt_pk_bf16_f32 v109, v106, v107\n" \
  "ds_write_b64 v251, v[108:109] offset:14848\n" \
  "v_accvgpr_read_b32 v112, a108\n" \
  "v_accvgpr_read_b32 v113, a109\n" \
  "v_accvgpr_read_b32 v114, a110\n" \
  "v_accvgpr_read_b32 v115, a111\n" \
  "v_pk_mul_f32 v[112:113], v[112:113], s[98:99]\n" \
  "v_pk_mul_f32 v[114:115], v[114:115], s[98:99]\n" \
  "v_cvt_pk_bf16_f32 v116, v112, v113\n" \
  "v_cvt_pk_bf16_f32 v117, v114, v115\n" \
  "ds_write_b64 v251, v[116:117] offset:15360\n" \
  "v_accvgpr_read_b32 v120, a124\n" \
  "v_accvgpr_read_b32 v121, a125\n" \
  "v_accvgpr_read_b32 v122, a126\n" \
  "v_accvgpr_read_b32 v123, a127\n" \
  "v_pk_mul_f32 v[120:121], v[120:121], s[98:99]\n" \
  "v_pk_mul_f32 v[122:123], v[122:123], s[98:99]\n" \
  "v_cvt_pk_bf16_f32 v124, v120, v121\n" \
  "v_cvt_pk_bf16_f32 v125, v122, v123\n" \
  "ds_write_b64 v251, v[124:125] offset:15872\n" \
  "s_waitcnt lgkmcnt(0)\n" \
  "v_mbcnt_lo_u32_b32 v212, -1, 0\n" \
  "v_mbcnt_hi_u32_b32 v212, -1, v212\n" \
  "v_and_b32 v213, 15, v212\n" \
  "v_lshrrev_b32 v214, 4, v212\n" \
  "v_lshlrev_b32 v215, 4, v212\n" \
  "v_add_u32 v215, s68, v215\n" \
  "ds_read_b128 a[128:131], v215 offset:0\n" \
  "ds_read_b128 a[132:135], v215 offset:1024\n" \
  "ds_read_b128 a[136:139], v215 offset:2048\n" \
  "ds_read_b128 a[140:143], v215 offset:3072\n" \
  "ds_read_b128 a[144:147], v215 offset:4096\n" \
  "ds_read_b128 a[148:151], v215 offset:5120\n" \
  "ds_read_b128 a[152:155], v215 offset:6144\n" \
  "ds_read_b128 a[156:159], v215 offset:7168\n" \
  "ds_read_b128 a[160:163], v215 offset:8192\n" \
  "ds_read_b128 a[164:167], v215 offset:9216\n" \
  "ds_read_b128 a[168:171], v215 offset:10240\n" \
  "ds_read_b128 a[172:175], v215 offset:11264\n" \
  "ds_read_b128 a[176:179], v215 offset:12288\n" \
  "ds_read_b128 a[180:183], v215 offset:13312\n" \
  "ds_read_b128 a[184:187], v215 offset:14336\n" \
  "ds_read_b128 a[188:191], v215 offset:15360\n" \
  "v_lshlrev_b32 v216, 8, v213\n" \
  "v_or_b32 v192, 0, v214\n" \
  "v_xor_b32 v192, v192, v213\n" \
  "v_lshl_add_u32 v192, v192, 4, v216\n" \
  "v_or_b32 v193, 4, v214\n" \
  "v_xor_b32 v193, v193, v213\n" \
  "v_lshl_add_u32 v193, v193, 4, v216\n" \
  "v_or_b32 v194, 8, v214\n" \
  "v_xor_b32 v194, v194, v213\n" \
  "v_lshl_add_u32 v194, v194, 4, v216\n" \
  "v_or_b32 v195, 12, v214\n" \
  "v_xor_b32 v195, v195, v213\n" \
  "v_lshl_add_u32 v195, v195, 4, v216\n" \
  "v_bfe_u32 v215, v212, 2, 2\n" \
  "v_and_b32 v216, 3, v212\n" \
  "v_lshlrev_b32 v217, 10, v214\n" \
  "v_add_u32 v217, 0x10000, v217\n" \
  "v_lshl_add_u32 v217, v215, 6, v217\n" \
  "v_lshl_add_u32 v217, v216, 3, v217\n" \
  "v_and_b32 v218, 1, v214\n" \
  "v_xor_b32 v196, 0, v218\n" \
  "v_lshl_add_u32 v196, v196, 5, v217\n" \
  "v_xor_b32 v197, 1, v218\n" \
  "v_lshl_add_u32 v197, v197, 5, v217\n" \
  "v_or_b32 v200, 0, v214\n" \
  "v_xor_b32 v200, v200, v213\n" \
  "v_lshlrev_b32 v200, 4, v200\n" \
  "v_lshl_add_u32 v200, v214, 8, v200\n" \
  "v_or_b32 v201, 4, v214\n" \
  "v_xor_b32 v201, v201, v213\n" \
  "v_lshlrev_b32 v201, 4, v201\n" \
  "v_lshl_add_u32 v201, v214, 8, v201\n" \
  "v_or_b32 v202, 8, v214\n" \
  "v_xor_b32 v202, v202, v213\n" \
  "v_lshlrev_b32 v202, 4, v202\n" \
  "v_lshl_add_u32 v202, v214, 8, v202\n" \
  "v_or_b32 v203, 12, v214\n" \
  "v_xor_b32 v203, v203, v213\n" \
  "v_lshlrev_b32 v203, 4, v203\n" \
  "v_lshl_add_u32 v203, v214, 8, v203\n" \
  "v_lshlrev_b32 v217, 8, v215\n" \
  "v_lshl_add_u32 v217, v214, 6, v217\n" \
  "v_xor_b32 v204, 0, v216\n" \
  "v_lshl_add_u32 v204, v204, 4, v217\n" \
  "v_xor_b32 v205, 2, v216\n" \
  "v_lshl_add_u32 v205, v205, 4, v217\n" \
  "v_mov_b32 v112, 0\n" \
  "v_mov_b32 v113, 0\n" \
  "v_mov_b32 v114, 0\n" \
  "v_mov_b32 v115, 0\n" \
  "v_mov_b32 v124, 0x3f803f80\n" \
  "v_mov_b32 v125, 0x3f803f80\n" \
  "v_mov_b32 v126, 0x3f803f80\n" \
  "v_mov_b32 v127, 0x3f803f80\n" \
  "s_waitcnt vmcnt(0) lgkmcnt(0)\n" \
  "s_barrier\n" \
  "v_lshlrev_b32 v210, 7, v212\n" \
  "s_mov_b32 s70, %25\n" \
  "s_mov_b32 s71, %26\n" \
  "global_load_dword v211, v210, s[70:71]\n" \
  "ds_read_b128 a[192:195], v192 offset:0\n" \
  "ds_read_b128 a[196:199], v193 offset:0\n" \
  "ds_read_b128 a[200:203], v194 offset:0\n" \
  "ds_read_b128 a[204:207], v195 offset:0\n" \
  "ds_read_b128 a[208:211], v192 offset:4096\n" \
  "ds_read_b128 a[212:215], v193 offset:4096\n" \
  "ds_read_b128 a[216:219], v194 offset:4096\n" \
  "ds_read_b128 a[220:223], v195 offset:4096\n" \
  "s_waitcnt lgkmcnt(0)\n" \
  "v_mfma_f32_16x16x32_bf16 v[0:3], a[192:195], a[128:131], 0\n" \
  "v_mfma_f32_16x16x32_bf16 v[0:3], a[196:199], a[132:135], v[0:3]\n" \
  "v_mfma_f32_16x16x32_bf16 v[0:3], a[200:203], a[136:139], v[0:3]\n" \
  "v_mfma_f32_16x16x32_bf16 v[0:3], a[204:207], a[140:143], v[0:3]\n" \
  "v_mfma_f32_16x16x32_bf16 v[4:7], a[192:195], a[144:147], 0\n" \
  "v_mfma_f32_16x16x32_bf16 v[4:7], a[196:199], a[148:151], v[4:7]\n" \
  "v_mfma_f32_16x16x32_bf16 v[4:7], a[200:203], a[152:155], v[4:7]\n" \
  "v_mfma_f32_16x16x32_bf16 v[4:7], a[204:207], a[156:159], v[4:7]\n" \
  "v_mfma_f32_16x16x32_bf16 v[8:11], a[192:195], a[160:163], 0\n" \
  "v_mfma_f32_16x16x32_bf16 v[8:11], a[196:199], a[164:167], v[8:11]\n" \
  "v_mfma_f32_16x16x32_bf16 v[8:11], a[200:203], a[168:171], v[8:11]\n" \
  "v_mfma_f32_16x16x32_bf16 v[8:11], a[204:207], a[172:175], v[8:11]\n" \
  "v_mfma_f32_16x16x32_bf16 v[12:15], a[192:195], a[176:179], 0\n" \
  "ds_read_b128 a[192:195], v192 offset:8192\n" \
  "v_mfma_f32_16x16x32_bf16 v[12:15], a[196:199], a[180:183], v[12:15]\n" \
  "ds_read_b128 a[196:199], v193 offset:8192\n" \
  "v_mfma_f32_16x16x32_bf16 v[12:15], a[200:203], a[184:187], v[12:15]\n" \
  "ds_read_b128 a[200:203], v194 offset:8192\n" \
  "v_mfma_f32_16x16x32_bf16 v[12:15], a[204:207], a[188:191], v[12:15]\n" \
  "ds_read_b128 a[204:207], v195 offset:8192\n" \
  "v_mfma_f32_16x16x32_bf16 v[16:19], a[208:211], a[128:131], 0\n" \
  "v_mfma_f32_16x16x32_bf16 v[16:19], a[212:215], a[132:135], v[16:19]\n" \
  "v_mfma_f32_16x16x32_bf16 v[16:19], a[216:219], a[136:139], v[16:19]\n" \
  "v_mfma_f32_16x16x32_bf16 v[16:19], a[220:223], a[140:143], v[16:19]\n" \
  "v_mfma_f32_16x16x32_bf16 v[20:23], a[208:211], a[144:147], 0\n" \
  "v_mfma_f32_16x16x32_bf16 v[20:23], a[212:215], a[148:151], v[20:23]\n" \
  "v_mfma_f32_16x16x32_bf16 v[20:23], a[216:219], a[152:155], v[20:23]\n" \
  "v_mfma_f32_16x16x32_bf16 v[20:23], a[220:223], a[156:159], v[20:23]\n" \
  "v_mfma_f32_16x16x32_bf16 v[24:27], a[208:211], a[160:163], 0\n" \
  "v_mfma_f32_16x16x32_bf16 v[24:27], a[212:215], a[164:167], v[24:27]\n" \
  "v_mfma_f32_16x16x32_bf16 v[24:27], a[216:219], a[168:171], v[24:27]\n" \
  "v_mfma_f32_16x16x32_bf16 v[24:27], a[220:223], a[172:175], v[24:27]\n" \
  "v_mfma_f32_16x16x32_bf16 v[28:31], a[208:211], a[176:179], 0\n" \
  "ds_read_b128 a[208:211], v192 offset:12288\n" \
  "v_mfma_f32_16x16x32_bf16 v[28:31], a[212:215], a[180:183], v[28:31]\n" \
  "ds_read_b128 a[212:215], v193 offset:12288\n" \
  "v_mfma_f32_16x16x32_bf16 v[28:31], a[216:219], a[184:187], v[28:31]\n" \
  "ds_read_b128 a[216:219], v194 offset:12288\n" \
  "v_mfma_f32_16x16x32_bf16 v[28:31], a[220:223], a[188:191], v[28:31]\n" \
  "ds_read_b128 a[220:223], v195 offset:12288\n" \
  "s_waitcnt lgkmcnt(0)\n" \
  "v_mfma_f32_16x16x32_bf16 v[32:35], a[192:195], a[128:131], 0\n" \
  "ds_read_b64_tr_b16 v[128:129], v196 offset:0\n" \
  "v_mfma_f32_16x16x32_bf16 v[32:35], a[196:199], a[132:135], v[32:35]\n" \
  "ds_read_b64_tr_b16 v[130:131], v196 offset:4096\n" \
  "v_mfma_f32_16x16x32_bf16 v[32:35], a[200:203], a[136:139], v[32:35]\n" \
  "ds_read_b64_tr_b16 v[132:133], v197 offset:0\n" \
  "v_mfma_f32_16x16x32_bf16 v[32:35], a[204:207], a[140:143], v[32:35]\n" \
  "ds_read_b64_tr_b16 v[134:135], v197 offset:4096\n" \
  "v_mfma_f32_16x16x32_bf16 v[36:39], a[192:195], a[144:147], 0\n" \
  "ds_read_b64_tr_b16 v[136:137], v196 offset:256\n" \
  "v_mfma_f32_16x16x32_bf16 v[36:39], a[196:199], a[148:151], v[36:39]\n" \
  "ds_read_b64_tr_b16 v[138:139], v196 offset:4352\n" \
  "v_mfma_f32_16x16x32_bf16 v[36:39], a[200:203], a[152:155], v[36:39]\n" \
  "ds_read_b64_tr_b16 v[140:141], v197 offset:256\n" \
  "v_mfma_f32_16x16x32_bf16 v[36:39], a[204:207], a[156:159], v[36:39]\n" \
  "ds_read_b64_tr_b16 v[142:143], v197 offset:4352\n" \
  "v_mfma_f32_16x16x32_bf16 v[40:43], a[192:195], a[160:163], 0\n" \
  "ds_read_b64_tr_b16 v[144:145], v196 offset:512\n" \
  "v_mfma_f32_16x16x32_bf16 v[40:43], a[196:199], a[164:167], v[40:43]\n" \
  "ds_read_b64_tr_b16 v[146:147], v196 offset:4608\n" \
  "v_mfma_f32_16x16x32_bf16 v[40:43], a[200:203], a[168:171], v[40:43]\n" \
  "ds_read_b64_tr_b16 v[148:149], v197 offset:512\n" \
  "v_mfma_f32_16x16x32_bf16 v[40:43], a[204:207], a[172:175], v[40:43]\n" \
  "ds_read_b64_tr_b16 v[150:151], v197 offset:4608\n" \
  "v_mfma_f32_16x16x32_bf16 v[44:47], a[192:195], a[176:179], 0\n" \
  "ds_read_b64_tr_b16 v[152:153], v196 offset:768\n" \
  "ds_read_b128 a[192:195], v192 offset:16384\n" \
  "v_mfma_f32_16x16x32_bf16 v[44:47], a[196:199], a[180:183], v[44:47]\n" \
  "ds_read_b64_tr_b16 v[154:155], v196 offset:4864\n" \
  "ds_read_b128 a[196:199], v193 offset:16384\n" \
  "v_mfma_f32_16x16x32_bf16 v[44:47], a[200:203], a[184:187], v[44:47]\n" \
  "ds_read_b64_tr_b16 v[156:157], v197 offset:768\n" \
  "ds_read_b128 a[200:203], v194 offset:16384\n" \
  "v_mfma_f32_16x16x32_bf16 v[44:47], a[204:207], a[188:191], v[44:47]\n" \
  "ds_read_b64_tr_b16 v[158:159], v197 offset:4864\n" \
  "ds_read_b128 a[204:207], v195 offset:16384\n" \
  "v_mfma_f32_16x16x32_bf16 v[48:51], a[208:211], a[128:131], 0\n" \
  "v_mfma_f32_16x16x32_bf16 v[48:51], a[212:215], a[132:135], v[48:51]\n" \
  "v_mfma_f32_16x16x32_bf16 v[48:51], a[216:219], a[136:139], v[48:51]\n" \
  "v_mfma_f32_16x16x32_bf16 v[48:51], a[220:223], a[140:143], v[48:51]\n" \
  "v_mfma_f32_16x16x32_bf16 v[52:55], a[208:211], a[144:147], 0\n" \
  "v_mfma_f32_16x16x32_bf16 v[52:55], a[212:215], a[148:151], v[52:55]\n" \
  "v_mfma_f32_16x16x32_bf16 v[52:55], a[216:219], a[152:155], v[52:55]\n" \
  "v_mfma_f32_16x16x32_bf16 v[52:55], a[220:223], a[156:159], v[52:55]\n" \
  "v_mfma_f32_16x16x32_bf16 v[56:59], a[208:211], a[160:163], 0\n" \
  "v_mfma_f32_16x16x32_bf16 v[56:59], a[212:215], a[164:167], v[56:59]\n" \
  "v_mfma_f32_16x16x32_bf16 v[56:59], a[216:219], a[168:171], v[56:59]\n" \
  "v_mfma_f32_16x16x32_bf16 v[56:59], a[220:223], a[172:175], v[56:59]\n" \
  "v_mfma_f32_16x16x32_bf16 v[60:63], a[208:211], a[176:179], 0\n" \
  "v_mfma_f32_16x16x32_bf16 v[60:63], a[212:215], a[180:183], v[60:63]\n" \
  "v_mfma_f32_16x16x32_bf16 v[60:63], a[216:219], a[184:187], v[60:63]\n" \
  "ds_read_b128 a[208:211], v192 offset:20480\n" \
  "v_mfma_f32_16x16x32_bf16 v[60:63], a[220:223], a[188:191], v[60:63]\n" \
  "s_nop 7\n" \
  "s_nop 7\n" \
  "v_max3_f32 v120, v0, v1, v2\n" \
  "v_max3_f32 v120, v120, v3, v16\n" \
  "v_max3_f32 v120, v120, v17, v18\n" \
  "v_max3_f32 v120, v120, v19, v32\n" \
  "v_max3_f32 v120, v120, v33, v34\n" \
  "v_max3_f32 v120, v120, v35, v48\n" \
  "v_max3_f32 v120, v120, v49, v50\n" \
  "v_max_f32 v120, v120, v51\n" \
  "v_max3_f32 v121, v4, v5, v6\n" \
  "v_max3_f32 v121, v121, v7, v20\n" \
  "v_max3_f32 v121, v121, v21, v22\n" \
  "v_max3_f32 v121, v121, v23, v36\n" \
  "v_max3_f32 v121, v121, v37, v38\n" \
  "v_max3_f32 v121, v121, v39, v52\n" \
  "v_max3_f32 v121, v121, v53, v54\n" \
  "v_max_f32 v121, v121, v55\n" \
  "v_max3_f32 v122, v8, v9, v10\n" \
  "v_max3_f32 v122, v122, v11, v24\n" \
  "v_max3_f32 v122, v122, v25, v26\n" \
  "v_max3_f32 v122, v122, v27, v40\n" \
  "v_max3_f32 v122, v122, v41, v42\n" \
  "v_max3_f32 v122, v122, v43, v56\n" \
  "v_max3_f32 v122, v122, v57, v58\n" \
  "v_max_f32 v122, v122, v59\n" \
  "v_max3_f32 v123, v12, v13, v14\n" \
  "v_max3_f32 v123, v123, v15, v28\n" \
  "v_max3_f32 v123, v123, v29, v30\n" \
  "v_max3_f32 v123, v123, v31, v44\n" \
  "v_max3_f32 v123, v123, v45, v46\n" \
  "v_max3_f32 v123, v123, v47, v60\n" \
  "v_max3_f32 v123, v123, v61, v62\n" \
  "v_max_f32 v123, v123, v63\n" \
  "v_mov_b32 v222, v120\n" \
  "v_mov_b32 v223, v121\n" \
  "v_mov_b32 v224, v122\n" \
  "v_mov_b32 v225, v123\n" \
  "s_nop 1\n" \
  "v_permlane32_swap_b32 v120, v222\n" \
  "v_permlane32_swap_b32 v121, v223\n" \
  "v_permlane32_swap_b32 v122, v224\n" \
  "v_permlane32_swap_b32 v123, v225\n" \
  "s_nop 1\n" \
  "v_max_f32 v120, v120, v222\n" \
  "v_max_f32 v121, v121, v223\n" \
  "v_max_f32 v122, v122, v224\n" \
  "v_max_f32 v123, v123, v225\n" \
  "v_mov_b32 v222, v120\n" \
  "v_mov_b32 v223, v121\n" \
  "v_mov_b32 v224, v122\n" \
  "v_mov_b32 v225, v123\n" \
  "s_nop 1\n" \
  "v_permlane16_swap_b32 v120, v222\n" \
  "v_permlane16_swap_b32 v121, v223\n" \
  "v_permlane16_swap_b32 v122, v224\n" \
  "v_permlane16_swap_b32 v123, v225\n" \
  "s_nop 1\n" \
  "v_max_f32 v120, v120, v222\n" \
  "v_max_f32 v121, v121, v223\n" \
  "v_max_f32 v122, v122, v224\n" \
  "v_max_f32 v123, v123, v225\n" \
  "v_xor_b32 v220, 0x80000000, v120\n" \
  "v_mov_b32 v96, v220\n" \
  "v_mov_b32 v97, v220\n" \
  "v_mov_b32 v98, v220\n" \
  "v_mov_b32 v99, v220\n" \
  "v_sub_f32 v32, v32, v120\n" \
  "v_sub_f32 v33, v33, v120\n" \
  "v_sub_f32 v34, v34, v120\n" \
  "v_sub_f32 v35, v35, v120\n" \
  "v_sub_f32 v48, v48, v120\n" \
  "v_sub_f32 v49, v49, v120\n" \
  "v_sub_f32 v50, v50, v120\n" \
  "v_sub_f32 v51, v51, v120\n" \
  "v_xor_b32 v220, 0x80000000, v121\n" \
  "v_mov_b32 v100, v220\n" \
  "v_mov_b32 v101, v220\n" \
  "v_mov_b32 v102, v220\n" \
  "v_mov_b32 v103, v220\n" \
  "v_sub_f32 v36, v36, v121\n" \
  "v_sub_f32 v37, v37, v121\n" \
  "v_sub_f32 v38, v38, v121\n" \
  "v_sub_f32 v39, v39, v121\n" \
  "v_sub_f32 v52, v52, v121\n" \
  "v_sub_f32 v53, v53, v121\n" \
  "v_sub_f32 v54, v54, v121\n" \
  "v_sub_f32 v55, v55, v121\n" \
  "v_xor_b32 v220, 0x80000000, v122\n" \
  "v_mov_b32 v104, v220\n" \
  "v_mov_b32 v105, v220\n" \
  "v_mov_b32 v106, v220\n" \
  "v_mov_b32 v107, v220\n" \
  "v_sub_f32 v40, v40, v122\n" \
  "v_sub_f32 v41, v41, v122\n" \
  "v_sub_f32 v42, v42, v122\n" \
  "v_sub_f32 v43, v43, v122\n" \
  "v_sub_f32 v56, v56, v122\n" \
  "v_sub_f32 v57, v57, v122\n" \
  "v_sub_f32 v58, v58, v122\n" \
  "v_sub_f32 v59, v59, v122\n" \
  "v_xor_b32 v220, 0x80000000, v123\n" \
  "v_mov_b32 v108, v220\n" \
  "v_mov_b32 v109, v220\n" \
  "v_mov_b32 v110, v220\n" \
  "v_mov_b32 v111, v220\n" \
  "v_sub_f32 v44, v44, v123\n" \
  "v_sub_f32 v45, v45, v123\n" \
  "v_sub_f32 v46, v46, v123\n" \
  "v_sub_f32 v47, v47, v123\n" \
  "v_sub_f32 v60, v60, v123\n" \
  "v_sub_f32 v61, v61, v123\n" \
  "v_sub_f32 v62, v62, v123\n" \
  "v_sub_f32 v63, v63, v123\n" \
  "v_sub_f32 v0, v0, v120\n" \
  "v_sub_f32 v1, v1, v120\n" \
  "v_sub_f32 v2, v2, v120\n" \
  "v_sub_f32 v3, v3, v120\n" \
  "v_sub_f32 v16, v16, v120\n" \
  "v_sub_f32 v17, v17, v120\n" \
  "v_sub_f32 v18, v18, v120\n" \
  "v_sub_f32 v19, v19, v120\n" \
  "v_exp_f32 v0, v0\n" \
  "v_exp_f32 v1, v1\n" \
  "v_exp_f32 v2, v2\n" \
  "v_exp_f32 v3, v3\n" \
  "v_exp_f32 v16, v16\n" \
  "v_exp_f32 v17, v17\n" \
  "v_exp_f32 v18, v18\n" \
  "v_exp_f32 v19, v19\n" \
  "s_nop 0\n" \
  "v_cvt_pk_bf16_f32 v64, v0, v1\n" \
  "v_cvt_pk_bf16_f32 v65, v2, v3\n" \
  "v_cvt_pk_bf16_f32 v66, v16, v17\n" \
  "v_cvt_pk_bf16_f32 v67, v18, v19\n" \
  "v_sub_f32 v4, v4, v121\n" \
  "v_sub_f32 v5, v5, v121\n" \
  "v_sub_f32 v6, v6, v121\n" \
  "v_sub_f32 v7, v7, v121\n" \
  "v_sub_f32 v20, v20, v121\n" \
  "v_sub_f32 v21, v21, v121\n" \
  "v_sub_f32 v22, v22, v121\n" \
  "v_sub_f32 v23, v23, v121\n" \
  "v_exp_f32 v4, v4\n" \
  "v_exp_f32 v5, v5\n" \
  "v_exp_f32 v6, v6\n" \
  "v_exp_f32 v7, v7\n" \
  "v_exp_f32 v20, v20\n" \
  "v_exp_f32 v21, v21\n" \
  "v_exp_f32 v22, v22\n" \
  "v_exp_f32 v23, v23\n" \
  "s_nop 0\n" \
  "v_cvt_pk_bf16_f32 v68, v4, v5\n" \
  "v_cvt_pk_bf16_f32 v69, v6, v7\n" \
  "v_cvt_pk_bf16_f32 v70, v20, v21\n" \
  "v_cvt_pk_bf16_f32 v71, v22, v23\n" \
  "v_sub_f32 v8, v8, v122\n" \
  "v_sub_f32 v9, v9, v122\n" \
  "v_sub_f32 v10, v10, v122\n" \
  "v_sub_f32 v11, v11, v122\n" \
  "v_sub_f32 v24, v24, v122\n" \
  "v_sub_f32 v25, v25, v122\n" \
  "v_sub_f32 v26, v26, v122\n" \
  "v_sub_f32 v27, v27, v122\n" \
  "v_exp_f32 v8, v8\n" \
  "v_exp_f32 v9, v9\n" \
  "v_exp_f32 v10, v10\n" \
  "v_exp_f32 v11, v11\n" \
  "v_exp_f32 v24, v24\n" \
  "v_exp_f32 v25, v25\n" \
  "v_exp_f32 v26, v26\n" \
  "v_exp_f32 v27, v27\n" \
  "s_nop 0\n" \
  "v_cvt_pk_bf16_f32 v72, v8, v9\n" \
  "v_cvt_pk_bf16_f32 v73, v10, v11\n" \
  "v_cvt_pk_bf16_f32 v74, v24, v25\n" \
  "v_cvt_pk_bf16_f32 v75, v26, v27\n" \
  "v_sub_f32 v12, v12, v123\n" \
  "v_sub_f32 v13, v13, v123\n" \
  "v_sub_f32 v14, v14, v123\n" \
  "v_sub_f32 v15, v15, v123\n" \
  "v_sub_f32 v28, v28, v123\n" \
  "v_sub_f32 v29, v29, v123\n" \
  "v_sub_f32 v30, v30, v123\n" \
  "v_sub_f32 v31, v31, v123\n" \
  "v_exp_f32 v12, v12\n" \
  "v_exp_f32 v13, v13\n" \
  "v_exp_f32 v14, v14\n" \
  "v_exp_f32 v15, v15\n" \
  "v_exp_f32 v28, v28\n" \
  "v_exp_f32 v29, v29\n" \
  "v_exp_f32 v30, v30\n" \
  "v_exp_f32 v31, v31\n" \
  "s_nop 0\n" \
  "v_cvt_pk_bf16_f32 v76, v12, v13\n" \
  "v_cvt_pk_bf16_f32 v77, v14, v15\n" \
  "v_cvt_pk_bf16_f32 v78, v28, v29\n" \
  "v_cvt_pk_bf16_f32 v79, v30, v31\n" \
  "s_nop 1\n" \
  "v_mfma_f32_16x16x32_bf16 v[240:243], v[124:127], v[64:67], 0\n" \
  "v_mfma_f32_16x16x32_bf16 v[244:247], v[124:127], v[68:71], 0\n" \
  "v_mfma_f32_16x16x32_bf16 v[248:251], v[124:127], v[72:75], 0\n" \
  "v_mfma_f32_16x16x32_bf16 v[252:255], v[124:127], v[76:79], 0\n" \
  "s_nop 7\n" \
  "s_nop 3\n" \
  "s_add_u32 m0, s54, 0xc000\n" \
  "s_nop 0\n" \
  "buffer_load_dwordx4 v200, s[40:43], s52 offen offset:0 lds\n" \
  "buffer_load_dwordx4 v201, s[40:43], s52 offen offset:1024 lds\n" \
  "buffer_load_dwordx4 v202, s[40:43], s52 offen offset:2048 lds\n" \
  "buffer_load_dwordx4 v203, s[40:43], s52 offen offset:3072 lds\n" \
  "s_add_u32 m0, s55, 0x8000\n" \
  "s_nop 0\n" \
  "buffer_load_dwordx4 v204, s[44:47], s53 offen offset:0 lds\n" \
  "buffer_load_dwordx4 v205, s[44:47], s53 offen offset:1024 lds\n" \
  "buffer_load_dwordx4 v204, s[44:47], s53 offen offset:2048 lds\n" \
  "buffer_load_dwordx4 v205, s[44:47], s53 offen offset:3072 lds\n" \
  "s_waitcnt lgkmcnt(2)\n" \
  "v_mfma_f32_16x16x32_bf16 a[0:3], v[128:131], v[64:67], 0\n" \
  "ds_read_b128 a[212:215], v193 offset:20480\n" \
  "v_add_f32 v112, v112, v240\n" \
  "v_mfma_f32_16x16x32_bf16 a[4:7], v[128:131], v[68:71], 0\n" \
  "v_add_f32 v113, v113, v244\n" \
  "v_add_f32 v114, v114, v248\n" \
  "v_mfma_f32_16x16x32_bf16 a[8:11], v[128:131], v[72:75], 0\n" \
  "ds_read_b128 a[216:219], v194 offset:20480\n" \
  "v_add_f32 v115, v115, v252\n" \
  "v_mfma_f32_16x16x32_bf16 a[12:15], v[128:131], v[76:79], 0\n" \
  "v_exp_f32 v32, v32\n" \
  "v_mfma_f32_16x16x32_bf16 a[16:19], v[132:135], v[64:67], 0\n" \
  "ds_read_b128 a[220:223], v195 offset:20480\n" \
  "v_exp_f32 v33, v33\n" \
  "v_mfma_f32_16x16x32_bf16 a[20:23], v[132:135], v[68:71], 0\n" \
  "v_exp_f32 v34, v34\n" \
  "v_mfma_f32_16x16x32_bf16 a[24:27], v[132:135], v[72:75], 0\n" \
  "s_add_u32 s52, s52, 0x4000\n" \
  "v_cvt_pk_bf16_f32 v80, v32, v33\n" \
  "v_mfma_f32_16x16x32_bf16 a[28:31], v[132:135], v[76:79], 0\n" \
  "ds_read_b64_tr_b16 v[128:129], v196 offset:8192\n" \
  "v_exp_f32 v35, v35\n" \
  "v_mfma_f32_16x16x32_bf16 a[32:35], v[136:139], v[64:67], 0\n" \
  "s_add_u32 s53, s53, 0x4000\n" \
  "v_cvt_pk_bf16_f32 v81, v34, v35\n" \
  "v_mfma_f32_16x16x32_bf16 a[36:39], v[136:139], v[68:71], 0\n" \
  "ds_read_b64_tr_b16 v[130:131], v196 offset:12288\n" \
  "v_exp_f32 v48, v48\n" \
  "v_mfma_f32_16x16x32_bf16 a[40:43], v[136:139], v[72:75], 0\n" \
  "v_exp_f32 v49, v49\n" \
  "v_mfma_f32_16x16x32_bf16 a[44:47], v[136:139], v[76:79], 0\n" \
  "ds_read_b64_tr_b16 v[132:133], v197 offset:8192\n" \
  "v_cvt_pk_bf16_f32 v82, v48, v49\n" \
  "v_mfma_f32_16x16x32_bf16 a[48:51], v[140:143], v[64:67], 0\n" \
  "v_exp_f32 v50, v50\n" \
  "v_mfma_f32_16x16x32_bf16 a[52:55], v[140:143], v[68:71], 0\n" \
  "ds_read_b64_tr_b16 v[134:135], v197 offset:12288\n" \
  "v_exp_f32 v51, v51\n" \
  "v_mfma_f32_16x16x32_bf16 a[56:59], v[140:143], v[72:75], 0\n" \
  "v_exp_f32 v36, v36\n" \
  "v_mfma_f32_16x16x32_bf16 a[60:63], v[140:143], v[76:79], 0\n" \
  "ds_read_b64_tr_b16 v[136:137], v196 offset:8448\n" \
  "v_cvt_pk_bf16_f32 v83, v50, v51\n" \
  "v_mfma_f32_16x16x32_bf16 a[64:67], v[144:147], v[64:67], 0\n" \
  "v_exp_f32 v37, v37\n" \
  "v_mfma_f32_16x16x32_bf16 a[68:71], v[144:147], v[68:71], 0\n" \
  "ds_read_b64_tr_b16 v[138:139], v196 offset:12544\n" \
  "v_cvt_pk_bf16_f32 v84, v36, v37\n" \
  "v_mfma_f32_16x16x32_bf16 v[240:243], v[124:127], v[80:83], 0\n" \
  "v_exp_f32 v38, v38\n" \
  "v_mfma_f32_16x16x32_bf16 a[72:75], v[144:147], v[72:75], 0\n" \
  "v_exp_f32 v39, v39\n" \
  "v_mfma_f32_16x16x32_bf16 a[76:79], v[144:147], v[76:79], 0\n" \
  "ds_read_b64_tr_b16 v[140:141], v197 offset:8448\n" \
  "v_cvt_pk_bf16_f32 v85, v38, v39\n" \
  "v_mfma_f32_16x16x32_bf16 a[80:83], v[148:151], v[64:67], 0\n" \
  "v_exp_f32 v52, v52\n" \
  "v_mfma_f32_16x16x32_bf16 a[84:87], v[148:151], v[68:71], 0\n" \
  "ds_read_b64_tr_b16 v[142:143], v197 offset:12544\n" \
  "v_exp_f32 v53, v53\n" \
  "v_mfma_f32_16x16x32_bf16 a[88:91], v[148:151], v[72:75], 0\n" \
  "v_exp_f32 v54, v54\n" \
  "v_mfma_f32_16x16x32_bf16 a[92:95], v[148:151], v[76:79], 0\n" \
  "ds_read_b64_tr_b16 v[144:145], v196 offset:8704\n" \
  "v_cvt_pk_bf16_f32 v86, v52, v53\n" \
  "v_mfma_f32_16x16x32_bf16 a[96:99], v[152:155], v[64:67], 0\n" \
  "v_exp_f32 v55, v55\n" \
  "v_mfma_f32_16x16x32_bf16 a[100:103], v[152:155], v[68:71], 0\n" \
  "ds_read_b64_tr_b16 v[146:147], v196 offset:12800\n" \
  "v_cvt_pk_bf16_f32 v87, v54, v55\n" \
  "v_mfma_f32_16x16x32_bf16 a[104:107], v[152:155], v[72:75], 0\n" \
  "v_exp_f32 v40, v40\n" \
  "v_mfma_f32_16x16x32_bf16 a[108:111], v[152:155], v[76:79], 0\n" \
  "ds_read_b64_tr_b16 v[148:149], v197 offset:8704\n" \
  "v_exp_f32 v41, v41\n" \
  "v_mfma_f32_16x16x32_bf16 v[244:247], v[124:127], v[84:87], 0\n" \
  "v_mfma_f32_16x16x32_bf16 a[112:115], v[156:159], v[64:67], 0\n" \
  "v_exp_f32 v42, v42\n" \
  "v_mfma_f32_16x16x32_bf16 a[116:119], v[156:159], v[68:71], 0\n" \
  "ds_read_b64_tr_b16 v[150:151], v197 offset:12800\n" \
  "v_cvt_pk_bf16_f32 v88, v40, v41\n" \
  "v_mfma_f32_16x16x32_bf16 a[120:123], v[156:159], v[72:75], 0\n" \
  "v_exp_f32 v43, v43\n" \
  "v_mfma_f32_16x16x32_bf16 a[124:127], v[156:159], v[76:79], 0\n" \
  "ds_read_b64_tr_b16 v[152:153], v196 offset:8960\n" \
  "v_cvt_pk_bf16_f32 v89, v42, v43\n" \
  ".p2align 6\n" \
  "LOOP%=:\n" \
  "s_waitcnt lgkmcnt(13)\n" \
  "v_mfma_f32_16x16x32_bf16 v[0:3], a[192:195], a[128:131], v[96:99]\n" \
  "v_exp_f32 v56, v56\n" \
  "v_mfma_f32_16x16x32_bf16 v[0:3], a[196:199], a[132:135], v[0:3]\n" \
  "ds_read_b64_tr_b16 v[154:155], v196 offset:13056\n" \
  "v_mfma_f32_16x16x32_bf16 v[0:3], a[200:203], a[136:139], v[0:3]\n" \
  "v_exp_f32 v57, v57\n" \
  "v_mfma_f32_16x16x32_bf16 v[0:3], a[204:207], a[140:143], v[0:3]\n" \
  "ds_read_b64_tr_b16 v[156:157], v197 offset:8960\n" \
  "v_cvt_pk_bf16_f32 v90, v56, v57\n" \
  "v_mfma_f32_16x16x32_bf16 v[4:7], a[192:195], a[144:147], v[100:103]\n" \
  "v_exp_f32 v58, v58\n" \
  "v_mfma_f32_16x16x32_bf16 v[4:7], a[196:199], a[148:151], v[4:7]\n" \
  "ds_read_b64_tr_b16 v[158:159], v197 offset:13056\n" \
  "v_mfma_f32_16x16x32_bf16 v[4:7], a[200:203], a[152:155], v[4:7]\n" \
  "v_exp_f32 v59, v59\n" \
  "v_mfma_f32_16x16x32_bf16 v[4:7], a[204:207], a[156:159], v[4:7]\n" \
  "v_exp_f32 v44, v44\n" \
  "v_mfma_f32_16x16x32_bf16 v[8:11], a[192:195], a[160:163], v[104:107]\n" \
  "v_exp_f32 v45, v45\n" \
  "v_mfma_f32_16x16x32_bf16 v[8:11], a[196:199], a[164:167], v[8:11]\n" \
  "v_exp_f32 v46, v46\n" \
  "v_mfma_f32_16x16x32_bf16 v[8:11], a[200:203], a[168:171], v[8:11]\n" \
  "v_exp_f32 v47, v47\n" \
  "v_mfma_f32_16x16x32_bf16 v[8:11], a[204:207], a[172:175], v[8:11]\n" \
  "v_exp_f32 v60, v60\n" \
  "v_mfma_f32_16x16x32_bf16 v[12:15], a[192:195], a[176:179], v[108:111]\n" \
  "v_exp_f32 v61, v61\n" \
  "v_mfma_f32_16x16x32_bf16 v[12:15], a[196:199], a[180:183], v[12:15]\n" \
  "v_exp_f32 v62, v62\n" \
  "v_mfma_f32_16x16x32_bf16 v[12:15], a[200:203], a[184:187], v[12:15]\n" \
  "ds_read_b128 a[192:195], v192 offset:24576\n" \
  "v_cvt_pk_bf16_f32 v91, v58, v59\n" \
  "v_mfma_f32_16x16x32_bf16 v[12:15], a[204:207], a[188:191], v[12:15]\n" \
  "v_exp_f32 v63, v63\n" \
  "v_mfma_f32_16x16x32_bf16 v[16:19], a[208:211], a[128:131], v[96:99]\n" \
  "ds_read_b128 a[196:199], v193 offset:24576\n" \
  "v_cvt_pk_bf16_f32 v92, v44, v45\n" \
  "v_mfma_f32_16x16x32_bf16 v[248:251], v[124:127], v[88:91], 0\n" \
  "v_cvt_pk_bf16_f32 v93, v46, v47\n" \
  "v_cvt_pk_bf16_f32 v94, v60, v61\n" \
  "v_mfma_f32_16x16x32_bf16 v[16:19], a[212:215], a[132:135], v[16:19]\n" \
  "v_cvt_pk_bf16_f32 v95, v62, v63\n" \
  "v_mfma_f32_16x16x32_bf16 v[16:19], a[216:219], a[136:139], v[16:19]\n" \
  "ds_read_b128 a[200:203], v194 offset:24576\n" \
  "v_mfma_f32_16x16x32_bf16 v[16:19], a[220:223], a[140:143], v[16:19]\n" \
  "v_mfma_f32_16x16x32_bf16 v[252:255], v[124:127], v[92:95], 0\n" \
  "v_mfma_f32_16x16x32_bf16 v[20:23], a[208:211], a[144:147], v[100:103]\n" \
  "ds_read_b128 a[204:207], v195 offset:24576\n" \
  "v_mfma_f32_16x16x32_bf16 v[20:23], a[212:215], a[148:151], v[20:23]\n" \
  "v_mfma_f32_16x16x32_bf16 v[20:23], a[216:219], a[152:155], v[20:23]\n" \
  "v_mfma_f32_16x16x32_bf16 v[20:23], a[220:223], a[156:159], v[20:23]\n" \
  "v_mfma_f32_16x16x32_bf16 v[24:27], a[208:211], a[160:163], v[104:107]\n" \
  "v_mfma_f32_16x16x32_bf16 v[24:27], a[212:215], a[164:167], v[24:27]\n" \
  "v_mfma_f32_16x16x32_bf16 v[24:27], a[216:219], a[168:171], v[24:27]\n" \
  "v_mfma_f32_16x16x32_bf16 v[24:27], a[220:223], a[172:175], v[24:27]\n" \
  "v_mfma_f32_16x16x32_bf16 v[28:31], a[208:211], a[176:179], v[108:111]\n" \
  "v_mfma_f32_16x16x32_bf16 v[28:31], a[212:215], a[180:183], v[28:31]\n" \
  "v_max3_f32 v242, v240, v244, v248\n" \
  "v_max_f32 v242, v242, v252\n" \
  "v_mfma_f32_16x16x32_bf16 v[28:31], a[216:219], a[184:187], v[28:31]\n" \
  "ds_read_b128 a[208:211], v192 offset:28672\n" \
  "v_cmp_lt_f32 vcc, s59, v242\n" \
  "v_mfma_f32_16x16x32_bf16 v[28:31], a[220:223], a[188:191], v[28:31]\n" \
  "s_cbranch_vccnz TR_B1%=\n" \
  "RET_B1%=:\n" \
  "s_waitcnt lgkmcnt(5)\n" \
  "v_mfma_f32_16x16x32_bf16 a[0:3], v[128:131], v[80:83], a[0:3]\n" \
  "ds_read_b128 a[212:215], v193 offset:28672\n" \
  "v_add_f32 v112, v112, v240\n" \
  "v_mfma_f32_16x16x32_bf16 a[4:7], v[128:131], v[84:87], a[4:7]\n" \
  "v_add_f32 v113, v113, v244\n" \
  "v_add_f32 v114, v114, v248\n" \
  "v_mfma_f32_16x16x32_bf16 a[8:11], v[128:131], v[88:91], a[8:11]\n" \
  "ds_read_b128 a[216:219], v194 offset:28672\n" \
  "v_add_f32 v115, v115, v252\n" \
  "v_mfma_f32_16x16x32_bf16 a[12:15], v[128:131], v[92:95], a[12:15]\n" \
  "v_exp_f32 v0, v0\n" \
  "v_mfma_f32_16x16x32_bf16 a[16:19], v[132:135], v[80:83], a[16:19]\n" \
  "ds_read_b128 a[220:223], v195 offset:28672\n" \
  "v_exp_f32 v1, v1\n" \
  "v_mfma_f32_16x16x32_bf16 a[20:23], v[132:135], v[84:87], a[20:23]\n" \
  "s_waitcnt vmcnt(8)\n" \
  "s_barrier\n" \
  "v_mfma_f32_16x16x32_bf16 a[24:27], v[132:135], v[88:91], a[24:27]\n" \
  "v_exp_f32 v2, v2\n" \
  "v_mfma_f32_16x16x32_bf16 a[28:31], v[132:135], v[92:95], a[28:31]\n" \
  "ds_read_b64_tr_b16 v[128:129], v196 offset:16384\n" \
  "v_cvt_pk_bf16_f32 v64, v0, v1\n" \
  "v_mfma_f32_16x16x32_bf16 a[32:35], v[136:139], v[80:83], a[32:35]\n" \
  "v_exp_f32 v3, v3\n" \
  "v_mfma_f32_16x16x32_bf16 a[36:39], v[136:139], v[84:87], a[36:39]\n" \
  "ds_read_b64_tr_b16 v[130:131], v196 offset:20480\n" \
  "v_cvt_pk_bf16_f32 v65, v2, v3\n" \
  "v_mfma_f32_16x16x32_bf16 a[40:43], v[136:139], v[88:91], a[40:43]\n" \
  "v_exp_f32 v16, v16\n" \
  "v_mfma_f32_16x16x32_bf16 a[44:47], v[136:139], v[92:95], a[44:47]\n" \
  "ds_read_b64_tr_b16 v[132:133], v197 offset:16384\n" \
  "v_exp_f32 v17, v17\n" \
  "v_mfma_f32_16x16x32_bf16 a[48:51], v[140:143], v[80:83], a[48:51]\n" \
  "v_exp_f32 v18, v18\n" \
  "v_mfma_f32_16x16x32_bf16 a[52:55], v[140:143], v[84:87], a[52:55]\n" \
  "ds_read_b64_tr_b16 v[134:135], v197 offset:20480\n" \
  "v_cvt_pk_bf16_f32 v66, v16, v17\n" \
  "v_mfma_f32_16x16x32_bf16 a[56:59], v[140:143], v[88:91], a[56:59]\n" \
  "v_exp_f32 v19, v19\n" \
  "v_mfma_f32_16x16x32_bf16 a[60:63], v[140:143], v[92:95], a[60:63]\n" \
  "ds_read_b64_tr_b16 v[136:137], v196 offset:16640\n" \
  "v_cvt_pk_bf16_f32 v67, v18, v19\n" \
  "v_mfma_f32_16x16x32_bf16 a[64:67], v[144:147], v[80:83], a[64:67]\n" \
  "v_exp_f32 v4, v4\n" \
  "v_mfma_f32_16x16x32_bf16 a[68:71], v[144:147], v[84:87], a[68:71]\n" \
  "ds_read_b64_tr_b16 v[138:139], v196 offset:20736\n" \
  "v_exp_f32 v5, v5\n" \
  "v_mfma_f32_16x16x32_bf16 v[240:243], v[124:127], v[64:67], 0\n" \
  "v_exp_f32 v6, v6\n" \
  "v_mfma_f32_16x16x32_bf16 a[72:75], v[144:147], v[88:91], a[72:75]\n" \
  "v_exp_f32 v7, v7\n" \
  "v_mfma_f32_16x16x32_bf16 a[76:79], v[144:147], v[92:95], a[76:79]\n" \
  "ds_read_b64_tr_b16 v[140:141], v197 offset:16640\n" \
  "v_cvt_pk_bf16_f32 v68, v4, v5\n" \
  "v_mfma_f32_16x16x32_bf16 a[80:83], v[148:151], v[80:83], a[80:83]\n" \
  "v_exp_f32 v20, v20\n" \
  "v_mfma_f32_16x16x32_bf16 a[84:87], v[148:151], v[84:87], a[84:87]\n" \
  "ds_read_b64_tr_b16 v[142:143], v197 offset:20736\n" \
  "v_cvt_pk_bf16_f32 v69, v6, v7\n" \
  "v_mfma_f32_16x16x32_bf16 a[88:91], v[148:151], v[88:91], a[88:91]\n" \
  "v_exp_f32 v21, v21\n" \
  "v_mfma_f32_16x16x32_bf16 a[92:95], v[148:151], v[92:95], a[92:95]\n" \
  "ds_read_b64_tr_b16 v[144:145], v196 offset:16896\n" \
  "v_cvt_pk_bf16_f32 v70, v20, v21\n" \
  "v_mfma_f32_16x16x32_bf16 a[96:99], v[152:155], v[80:83], a[96:99]\n" \
  "v_exp_f32 v22, v22\n" \
  "v_mfma_f32_16x16x32_bf16 a[100:103], v[152:155], v[84:87], a[100:103]\n" \
  "ds_read_b64_tr_b16 v[146:147], v196 offset:20992\n" \
  "v_exp_f32 v23, v23\n" \
  "v_mfma_f32_16x16x32_bf16 a[104:107], v[152:155], v[88:91], a[104:107]\n" \
  "v_exp_f32 v8, v8\n" \
  "v_mfma_f32_16x16x32_bf16 a[108:111], v[152:155], v[92:95], a[108:111]\n" \
  "ds_read_b64_tr_b16 v[148:149], v197 offset:16896\n" \
  "v_cvt_pk_bf16_f32 v71, v22, v23\n" \
  "v_mfma_f32_16x16x32_bf16 a[112:115], v[156:159], v[80:83], a[112:115]\n" \
  "v_exp_f32 v9, v9\n" \
  "v_mfma_f32_16x16x32_bf16 a[116:119], v[156:159], v[84:87], a[116:119]\n" \
  "ds_read_b64_tr_b16 v[150:151], v197 offset:20992\n" \
  "v_cvt_pk_bf16_f32 v72, v8, v9\n" \
  "v_mfma_f32_16x16x32_bf16 v[244:247], v[124:127], v[68:71], 0\n" \
  "v_exp_f32 v10, v10\n" \
  "v_mfma_f32_16x16x32_bf16 a[120:123], v[156:159], v[88:91], a[120:123]\n" \
  "v_exp_f32 v11, v11\n" \
  "v_mfma_f32_16x16x32_bf16 a[124:127], v[156:159], v[92:95], a[124:127]\n" \
  "ds_read_b64_tr_b16 v[152:153], v196 offset:17152\n" \
  "v_cvt_pk_bf16_f32 v73, v10, v11\n" \
  "s_waitcnt lgkmcnt(13)\n" \
  "v_mfma_f32_16x16x32_bf16 v[32:35], a[192:195], a[128:131], v[96:99]\n" \
  "v_exp_f32 v24, v24\n" \
  "v_mfma_f32_16x16x32_bf16 v[32:35], a[196:199], a[132:135], v[32:35]\n" \
  "ds_read_b64_tr_b16 v[154:155], v196 offset:21248\n" \
  "v_exp_f32 v25, v25\n" \
  "v_mfma_f32_16x16x32_bf16 v[32:35], a[200:203], a[136:139], v[32:35]\n" \
  "v_exp_f32 v26, v26\n" \
  "v_mfma_f32_16x16x32_bf16 v[32:35], a[204:207], a[140:143], v[32:35]\n" \
  "ds_read_b64_tr_b16 v[156:157], v197 offset:17152\n" \
  "v_cvt_pk_bf16_f32 v74, v24, v25\n" \
  "v_mfma_f32_16x16x32_bf16 v[36:39], a[192:195], a[144:147], v[100:103]\n" \
  "v_exp_f32 v27, v27\n" \
  "s_add_u32 m0, s54, 0\n" \
  "v_mfma_f32_16x16x32_bf16 v[36:39], a[196:199], a[148:151], v[36:39]\n" \
  "ds_read_b64_tr_b16 v[158:159], v197 offset:21248\n" \
  "v_cvt_pk_bf16_f32 v75, v26, v27\n" \
  "v_mfma_f32_16x16x32_bf16 v[36:39], a[200:203], a[152:155], v[36:39]\n" \
  "buffer_load_dwordx4 v200, s[40:43], s52 offen offset:0 lds\n" \
  "v_mfma_f32_16x16x32_bf16 v[36:39], a[204:207], a[156:159], v[36:39]\n" \
  "v_exp_f32 v12, v12\n" \
  "v_mfma_f32_16x16x32_bf16 v[248:251], v[124:127], v[72:75], 0\n" \
  "v_exp_f32 v13, v13\n" \
  "v_mfma_f32_16x16x32_bf16 v[40:43], a[192:195], a[160:163], v[104:107]\n" \
  "buffer_load_dwordx4 v201, s[40:43], s52 offen offset:1024 lds\n" \
  "v_mfma_f32_16x16x32_bf16 v[40:43], a[196:199], a[164:167], v[40:43]\n" \
  "v_exp_f32 v14, v14\n" \
  "v_mfma_f32_16x16x32_bf16 v[40:43], a[200:203], a[168:171], v[40:43]\n" \
  "buffer_load_dwordx4 v202, s[40:43], s52 offen offset:2048 lds\n" \
  "v_mfma_f32_16x16x32_bf16 v[40:43], a[204:207], a[172:175], v[40:43]\n" \
  "v_exp_f32 v15, v15\n" \
  "v_mfma_f32_16x16x32_bf16 v[44:47], a[192:195], a[176:179], v[108:111]\n" \
  "buffer_load_dwordx4 v203, s[40:43], s52 offen offset:3072 lds\n" \
  "v_mfma_f32_16x16x32_bf16 v[44:47], a[196:199], a[180:183], v[44:47]\n" \
  "v_exp_f32 v28, v28\n" \
  "v_mfma_f32_16x16x32_bf16 v[44:47], a[200:203], a[184:187], v[44:47]\n" \
  "ds_read_b128 a[192:195], v192 offset:32768\n" \
  "v_cvt_pk_bf16_f32 v76, v12, v13\n" \
  "v_mfma_f32_16x16x32_bf16 v[44:47], a[204:207], a[188:191], v[44:47]\n" \
  "v_exp_f32 v29, v29\n" \
  "v_mfma_f32_16x16x32_bf16 v[48:51], a[208:211], a[128:131], v[96:99]\n" \
  "ds_read_b128 a[196:199], v193 offset:32768\n" \
  "v_cvt_pk_bf16_f32 v77, v14, v15\n" \
  "v_mfma_f32_16x16x32_bf16 v[48:51], a[212:215], a[132:135], v[48:51]\n" \
  "v_exp_f32 v30, v30\n" \
  "v_mfma_f32_16x16x32_bf16 v[48:51], a[216:219], a[136:139], v[48:51]\n" \
  "ds_read_b128 a[200:203], v194 offset:32768\n" \
  "v_cvt_pk_bf16_f32 v78, v28, v29\n" \
  "v_mfma_f32_16x16x32_bf16 v[48:51], a[220:223], a[140:143], v[48:51]\n" \
  "v_exp_f32 v31, v31\n" \
  "v_mfma_f32_16x16x32_bf16 v[52:55], a[208:211], a[144:147], v[100:103]\n" \
  "ds_read_b128 a[204:207], v195 offset:32768\n" \
  "s_add_u32 m0, s55, 49152\n" \
  "v_mfma_f32_16x16x32_bf16 v[52:55], a[212:215], a[148:151], v[52:55]\n" \
  "v_cvt_pk_bf16_f32 v79, v30, v31\n" \
  "v_mfma_f32_16x16x32_bf16 v[52:55], a[216:219], a[152:155], v[52:55]\n" \
  "buffer_load_dwordx4 v204, s[44:47], s53 offen offset:0 lds\n" \
  "v_mfma_f32_16x16x32_bf16 v[52:55], a[220:223], a[156:159], v[52:55]\n" \
  "v_mfma_f32_16x16x32_bf16 v[252:255], v[124:127], v[76:79], 0\n" \
  "v_mfma_f32_16x16x32_bf16 v[56:59], a[208:211], a[160:163], v[104:107]\n" \
  "buffer_load_dwordx4 v205, s[44:47], s53 offen offset:1024 lds\n" \
  "v_mfma_f32_16x16x32_bf16 v[56:59], a[212:215], a[164:167], v[56:59]\n" \
  "v_mfma_f32_16x16x32_bf16 v[56:59], a[216:219], a[168:171], v[56:59]\n" \
  "buffer_load_dwordx4 v204, s[44:47], s53 offen offset:2048 lds\n" \
  "v_mfma_f32_16x16x32_bf16 v[56:59], a[220:223], a[172:175], v[56:59]\n" \
  "v_mfma_f32_16x16x32_bf16 v[60:63], a[208:211], a[176:179], v[108:111]\n" \
  "buffer_load_dwordx4 v205, s[44:47], s53 offen offset:3072 lds\n" \
  "v_mfma_f32_16x16x32_bf16 v[60:63], a[212:215], a[180:183], v[60:63]\n" \
  "v_max3_f32 v242, v240, v244, v248\n" \
  "v_max_f32 v242, v242, v252\n" \
  "v_mfma_f32_16x16x32_bf16 v[60:63], a[216:219], a[184:187], v[60:63]\n" \
  "ds_read_b128 a[208:211], v192 offset:36864\n" \
  "v_cmp_lt_f32 vcc, s59, v242\n" \
  "v_mfma_f32_16x16x32_bf16 v[60:63], a[220:223], a[188:191], v[60:63]\n" \
  "s_cbranch_vccnz TR_A1%=\n" \
  "RET_A1%=:\n" \
  "s_waitcnt lgkmcnt(5)\n" \
  "v_mfma_f32_16x16x32_bf16 a[0:3], v[128:131], v[64:67], a[0:3]\n" \
  "ds_read_b128 a[212:215], v193 offset:36864\n" \
  "v_add_f32 v112, v112, v240\n" \
  "v_mfma_f32_16x16x32_bf16 a[4:7], v[128:131], v[68:71], a[4:7]\n" \
  "v_add_f32 v113, v113, v244\n" \
  "v_add_f32 v114, v114, v248\n" \
  "v_mfma_f32_16x16x32_bf16 a[8:11], v[128:131], v[72:75], a[8:11]\n" \
  "ds_read_b128 a[216:219], v194 offset:36864\n" \
  "v_add_f32 v115, v115, v252\n" \
  "v_mfma_f32_16x16x32_bf16 a[12:15], v[128:131], v[76:79], a[12:15]\n" \
  "v_exp_f32 v32, v32\n" \
  "v_mfma_f32_16x16x32_bf16 a[16:19], v[132:135], v[64:67], a[16:19]\n" \
  "ds_read_b128 a[220:223], v195 offset:36864\n" \
  "v_exp_f32 v33, v33\n" \
  "v_mfma_f32_16x16x32_bf16 a[20:23], v[132:135], v[68:71], a[20:23]\n" \
  "v_exp_f32 v34, v34\n" \
  "v_mfma_f32_16x16x32_bf16 a[24:27], v[132:135], v[72:75], a[24:27]\n" \
  "s_add_u32 s52, s52, 0x4000\n" \
  "v_cvt_pk_bf16_f32 v80, v32, v33\n" \
  "v_mfma_f32_16x16x32_bf16 a[28:31], v[132:135], v[76:79], a[28:31]\n" \
  "ds_read_b64_tr_b16 v[128:129], v196 offset:24576\n" \
  "v_exp_f32 v35, v35\n" \
  "v_mfma_f32_16x16x32_bf16 a[32:35], v[136:139], v[64:67], a[32:35]\n" \
  "s_add_u32 s53, s53, 0x4000\n" \
  "v_cvt_pk_bf16_f32 v81, v34, v35\n" \
  "v_mfma_f32_16x16x32_bf16 a[36:39], v[136:139], v[68:71], a[36:39]\n" \
  "ds_read_b64_tr_b16 v[130:131], v196 offset:28672\n" \
  "v_exp_f32 v48, v48\n" \
  "v_mfma_f32_16x16x32_bf16 a[40:43], v[136:139], v[72:75], a[40:43]\n" \
  "v_exp_f32 v49, v49\n" \
  "v_mfma_f32_16x16x32_bf16 a[44:47], v[136:139], v[76:79], a[44:47]\n" \
  "ds_read_b64_tr_b16 v[132:133], v197 offset:24576\n" \
  "v_cvt_pk_bf16_f32 v82, v48, v49\n" \
  "v_mfma_f32_16x16x32_bf16 a[48:51], v[140:143], v[64:67], a[48:51]\n" \
  "v_exp_f32 v50, v50\n" \
  "v_mfma_f32_16x16x32_bf16 a[52:55], v[140:143], v[68:71], a[52:55]\n" \
  "ds_read_b64_tr_b16 v[134:135], v197 offset:28672\n" \
  "v_exp_f32 v51, v51\n" \
  "v_mfma_f32_16x16x32_bf16 a[56:59], v[140:143], v[72:75], a[56:59]\n" \
  "v_exp_f32 v36, v36\n" \
  "v_mfma_f32_16x16x32_bf16 a[60:63], v[140:143], v[76:79], a[60:63]\n" \
  "ds_read_b64_tr_b16 v[136:137], v196 offset:24832\n" \
  "v_cvt_pk_bf16_f32 v83, v50, v51\n" \
  "v_mfma_f32_16x16x32_bf16 a[64:67], v[144:147], v[64:67], a[64:67]\n" \
  "v_exp_f32 v37, v37\n" \
  "v_mfma_f32_16x16x32_bf16 a[68:71], v[144:147], v[68:71], a[68:71]\n" \
  "ds_read_b64_tr_b16 v[138:139], v196 offset:28928\n" \
  "v_cvt_pk_bf16_f32 v84, v36, v37\n" \
  "v_mfma_f32_16x16x32_bf16 v[240:243], v[124:127], v[80:83], 0\n" \
  "v_exp_f32 v38, v38\n" \
  "v_mfma_f32_16x16x32_bf16 a[72:75], v[144:147], v[72:75], a[72:75]\n" \
  "v_exp_f32 v39, v39\n" \
  "v_mfma_f32_16x16x32_bf16 a[76:79], v[144:147], v[76:79], a[76:79]\n" \
  "ds_read_b64_tr_b16 v[140:141], v197 offset:24832\n" \
  "v_cvt_pk_bf16_f32 v85, v38, v39\n" \
  "v_mfma_f32_16x16x32_bf16 a[80:83], v[148:151], v[64:67], a[80:83]\n" \
  "v_exp_f32 v52, v52\n" \
  "v_mfma_f32_16x16x32_bf16 a[84:87], v[148:151], v[68:71], a[84:87]\n" \
  "ds_read_b64_tr_b16 v[142:143], v197 offset:28928\n" \
  "v_exp_f32 v53, v53\n" \
  "v_mfma_f32_16x16x32_bf16 a[88:91], v[148:151], v[72:75], a[88:91]\n" \
  "v_exp_f32 v54, v54\n" \
  "v_mfma_f32_16x16x32_bf16 a[92:95], v[148:151], v[76:79], a[92:95]\n" \
  "ds_read_b64_tr_b16 v[144:145], v196 offset:25088\n" \
  "v_cvt_pk_bf16_f32 v86, v52, v53\n" \
  "v_mfma_f32_16x16x32_bf16 a[96:99], v[152:155], v[64:67], a[96:99]\n" \
  "v_exp_f32 v55, v55\n" \
  "v_mfma_f32_16x16x32_bf16 a[100:103], v[152:155], v[68:71], a[100:103]\n" \
  "ds_read_b64_tr_b16 v[146:147], v196 offset:29184\n" \
  "v_cvt_pk_bf16_f32 v87, v54, v55\n" \
  "v_mfma_f32_16x16x32_bf16 a[104:107], v[152:155], v[72:75], a[104:107]\n" \
  "v_exp_f32 v40, v40\n" \
  "v_mfma_f32_16x16x32_bf16 a[108:111], v[152:155], v[76:79], a[108:111]\n" \
  "ds_read_b64_tr_b16 v[148:149], v197 offset:25088\n" \
  "v_exp_f32 v41, v41\n" \
  "v_mfma_f32_16x16x32_bf16 v[244:247], v[124:127], v[84:87], 0\n" \
  "v_mfma_f32_16x16x32_bf16 a[112:115], v[156:159], v[64:67], a[112:115]\n" \
  "v_exp_f32 v42, v42\n" \
  "v_mfma_f32_16x16x32_bf16 a[116:119], v[156:159], v[68:71], a[116:119]\n" \
  "ds_read_b64_tr_b16 v[150:151], v197 offset:29184\n" \
  "v_cvt_pk_bf16_f32 v88, v40, v41\n" \
  "v_mfma_f32_16x16x32_bf16 a[120:123], v[156:159], v[72:75], a[120:123]\n" \
  "v_exp_f32 v43, v43\n" \
  "v_mfma_f32_16x16x32_bf16 a[124:127], v[156:159], v[76:79], a[124:127]\n" \
  "ds_read_b64_tr_b16 v[152:153], v196 offset:25344\n" \
  "v_cvt_pk_bf16_f32 v89, v42, v43\n" \
  "s_waitcnt lgkmcnt(13)\n" \
  "v_mfma_f32_16x16x32_bf16 v[0:3], a[192:195], a[128:131], v[96:99]\n" \
  "v_exp_f32 v56, v56\n" \
  "v_mfma_f32_16x16x32_bf16 v[0:3], a[196:199], a[132:135], v[0:3]\n" \
  "ds_read_b64_tr_b16 v[154:155], v196 offset:29440\n" \
  "v_mfma_f32_16x16x32_bf16 v[0:3], a[200:203], a[136:139], v[0:3]\n" \
  "v_exp_f32 v57, v57\n" \
  "v_mfma_f32_16x16x32_bf16 v[0:3], a[204:207], a[140:143], v[0:3]\n" \
  "ds_read_b64_tr_b16 v[156:157], v197 offset:25344\n" \
  "v_cvt_pk_bf16_f32 v90, v56, v57\n" \
  "v_mfma_f32_16x16x32_bf16 v[4:7], a[192:195], a[144:147], v[100:103]\n" \
  "v_exp_f32 v58, v58\n" \
  "v_mfma_f32_16x16x32_bf16 v[4:7], a[196:199], a[148:151], v[4:7]\n" \
  "ds_read_b64_tr_b16 v[158:159], v197 offset:29440\n" \
  "v_mfma_f32_16x16x32_bf16 v[4:7], a[200:203], a[152:155], v[4:7]\n" \
  "v_exp_f32 v59, v59\n" \
  "v_mfma_f32_16x16x32_bf16 v[4:7], a[204:207], a[156:159], v[4:7]\n" \
  "v_exp_f32 v44, v44\n" \
  "v_mfma_f32_16x16x32_bf16 v[8:11], a[192:195], a[160:163], v[104:107]\n" \
  "v_exp_f32 v45, v45\n" \
  "v_mfma_f32_16x16x32_bf16 v[8:11], a[196:199], a[164:167], v[8:11]\n" \
  "v_exp_f32 v46, v46\n" \
  "v_mfma_f32_16x16x32_bf16 v[8:11], a[200:203], a[168:171], v[8:11]\n" \
  "v_exp_f32 v47, v47\n" \
  "v_mfma_f32_16x16x32_bf16 v[8:11], a[204:207], a[172:175], v[8:11]\n" \
  "v_exp_f32 v60, v60\n" \
  "v_mfma_f32_16x16x32_bf16 v[12:15], a[192:195], a[176:179], v[108:111]\n" \
  "v_exp_f32 v61, v61\n" \
  "v_mfma_f32_16x16x32_bf16 v[12:15], a[196:199], a[180:183], v[12:15]\n" \
  "v_exp_f32 v62, v62\n" \
  "v_mfma_f32_16x16x32_bf16 v[12:15], a[200:203], a[184:187], v[12:15]\n" \
  "ds_read_b128 a[192:195], v192 offset:40960\n" \
  "v_cvt_pk_bf16_f32 v91, v58, v59\n" \
  "v_mfma_f32_16x16x32_bf16 v[12:15], a[204:207], a[188:191], v[12:15]\n" \
  "v_exp_f32 v63, v63\n" \
  "v_mfma_f32_16x16x32_bf16 v[16:19], a[208:211], a[128:131], v[96:99]\n" \
  "ds_read_b128 a[196:199], v193 offset:40960\n" \
  "v_cvt_pk_bf16_f32 v92, v44, v45\n" \
  "v_mfma_f32_16x16x32_bf16 v[248:251], v[124:127], v[88:91], 0\n" \
  "v_cvt_pk_bf16_f32 v93, v46, v47\n" \
  "v_cvt_pk_bf16_f32 v94, v60, v61\n" \
  "v_mfma_f32_16x16x32_bf16 v[16:19], a[212:215], a[132:135], v[16:19]\n" \
  "v_cvt_pk_bf16_f32 v95, v62, v63\n" \
  "v_mfma_f32_16x16x32_bf16 v[16:19], a[216:219], a[136:139], v[16:19]\n" \
  "ds_read_b128 a[200:203], v194 offset:40960\n" \
  "v_mfma_f32_16x16x32_bf16 v[16:19], a[220:223], a[140:143], v[16:19]\n" \
  "v_mfma_f32_16x16x32_bf16 v[252:255], v[124:127], v[92:95], 0\n" \
  "v_mfma_f32_16x16x32_bf16 v[20:23], a[208:211], a[144:147], v[100:103]\n" \
  "ds_read_b128 a[204:207], v195 offset:40960\n" \
  "v_mfma_f32_16x16x32_bf16 v[20:23], a[212:215], a[148:151], v[20:23]\n" \
  "v_mfma_f32_16x16x32_bf16 v[20:23], a[216:219], a[152:155], v[20:23]\n" \
  "v_mfma_f32_16x16x32_bf16 v[20:23], a[220:223], a[156:159], v[20:23]\n" \
  "v_mfma_f32_16x16x32_bf16 v[24:27], a[208:211], a[160:163], v[104:107]\n" \
  "v_mfma_f32_16x16x32_bf16 v[24:27], a[212:215], a[164:167], v[24:27]\n" \
  "v_mfma_f32_16x16x32_bf16 v[24:27], a[216:219], a[168:171], v[24:27]\n" \
  "v_mfma_f32_16x16x32_bf16 v[24:27], a[220:223], a[172:175], v[24:27]\n" \
  "v_mfma_f32_16x16x32_bf16 v[28:31], a[208:211], a[176:179], v[108:111]\n" \
  "v_mfma_f32_16x16x32_bf16 v[28:31], a[212:215], a[180:183], v[28:31]\n" \
  "v_max3_f32 v242, v240, v244, v248\n" \
  "v_max_f32 v242, v242, v252\n" \
  "v_mfma_f32_16x16x32_bf16 v[28:31], a[216:219], a[184:187], v[28:31]\n" \
  "ds_read_b128 a[208:211], v192 offset:45056\n" \
  "v_cmp_lt_f32 vcc, s59, v242\n" \
  "v_mfma_f32_16x16x32_bf16 v[28:31], a[220:223], a[188:191], v[28:31]\n" \
  "s_cbranch_vccnz TR_B2%=\n" \
  "RET_B2%=:\n" \
  "s_waitcnt lgkmcnt(5)\n" \
  "v_mfma_f32_16x16x32_bf16 a[0:3], v[128:131], v[80:83], a[0:3]\n" \
  "ds_read_b128 a[212:215], v193 offset:45056\n" \
  "v_add_f32 v112, v112, v240\n" \
  "v_mfma_f32_16x16x32_bf16 a[4:7], v[128:131], v[84:87], a[4:7]\n" \
  "v_add_f32 v113, v113, v244\n" \
  "v_add_f32 v114, v114, v248\n" \
  "v_mfma_f32_16x16x32_bf16 a[8:11], v[128:131], v[88:91], a[8:11]\n" \
  "ds_read_b128 a[216:219], v194 offset:45056\n" \
  "v_add_f32 v115, v115, v252\n" \
  "v_mfma_f32_16x16x32_bf16 a[12:15], v[128:131], v[92:95], a[12:15]\n" \
  "v_exp_f32 v0, v0\n" \
  "v_mfma_f32_16x16x32_bf16 a[16:19], v[132:135], v[80:83], a[16:19]\n" \
  "ds_read_b128 a[220:223], v195 offset:45056\n" \
  "v_exp_f32 v1, v1\n" \
  "v_mfma_f32_16x16x32_bf16 a[20:23], v[132:135], v[84:87], a[20:23]\n" \
  "s_waitcnt vmcnt(8)\n" \
  "s_barrier\n" \
  "v_mfma_f32_16x16x32_bf16 a[24:27], v[132:135], v[88:91], a[24:27]\n" \
  "v_exp_f32 v2, v2\n" \
  "v_mfma_f32_16x16x32_bf16 a[28:31], v[132:135], v[92:95], a[28:31]\n" \
  "ds_read_b64_tr_b16 v[128:129], v196 offset:32768\n" \
  "v_cvt_pk_bf16_f32 v64, v0, v1\n" \
  "v_mfma_f32_16x16x32_bf16 a[32:35], v[136:139], v[80:83], a[32:35]\n" \
  "v_exp_f32 v3, v3\n" \
  "v_mfma_f32_16x16x32_bf16 a[36:39], v[136:139], v[84:87], a[36:39]\n" \
  "ds_read_b64_tr_b16 v[130:131], v196 offset:36864\n" \
  "v_cvt_pk_bf16_f32 v65, v2, v3\n" \
  "v_mfma_f32_16x16x32_bf16 a[40:43], v[136:139], v[88:91], a[40:43]\n" \
  "v_exp_f32 v16, v16\n" \
  "v_mfma_f32_16x16x32_bf16 a[44:47], v[136:139], v[92:95], a[44:47]\n" \
  "ds_read_b64_tr_b16 v[132:133], v197 offset:32768\n" \
  "v_exp_f32 v17, v17\n" \
  "v_mfma_f32_16x16x32_bf16 a[48:51], v[140:143], v[80:83], a[48:51]\n" \
  "v_exp_f32 v18, v18\n" \
  "v_mfma_f32_16x16x32_bf16 a[52:55], v[140:143], v[84:87], a[52:55]\n" \
  "ds_read_b64_tr_b16 v[134:135], v197 offset:36864\n" \
  "v_cvt_pk_bf16_f32 v66, v16, v17\n" \
  "v_mfma_f32_16x16x32_bf16 a[56:59], v[140:143], v[88:91], a[56:59]\n" \
  "v_exp_f32 v19, v19\n" \
  "v_mfma_f32_16x16x32_bf16 a[60:63], v[140:143], v[92:95], a[60:63]\n" \
  "ds_read_b64_tr_b16 v[136:137], v196 offset:33024\n" \
  "v_cvt_pk_bf16_f32 v67, v18, v19\n" \
  "v_mfma_f32_16x16x32_bf16 a[64:67], v[144:147], v[80:83], a[64:67]\n" \
  "v_exp_f32 v4, v4\n" \
  "v_mfma_f32_16x16x32_bf16 a[68:71], v[144:147], v[84:87], a[68:71]\n" \
  "ds_read_b64_tr_b16 v[138:139], v196 offset:37120\n" \
  "v_exp_f32 v5, v5\n" \
  "v_mfma_f32_16x16x32_bf16 v[240:243], v[124:127], v[64:67], 0\n" \
  "v_exp_f32 v6, v6\n" \
  "v_mfma_f32_16x16x32_bf16 a[72:75], v[144:147], v[88:91], a[72:75]\n" \
  "v_exp_f32 v7, v7\n" \
  "v_mfma_f32_16x16x32_bf16 a[76:79], v[144:147], v[92:95], a[76:79]\n" \
  "ds_read_b64_tr_b16 v[140:141], v197 offset:33024\n" \
  "v_cvt_pk_bf16_f32 v68, v4, v5\n" \
  "v_mfma_f32_16x16x32_bf16 a[80:83], v[148:151], v[80:83], a[80:83]\n" \
  "v_exp_f32 v20, v20\n" \
  "v_mfma_f32_16x16x32_bf16 a[84:87], v[148:151], v[84:87], a[84:87]\n" \
  "ds_read_b64_tr_b16 v[142:143], v197 offset:37120\n" \
  "v_cvt_pk_bf16_f32 v69, v6, v7\n" \
  "v_mfma_f32_16x16x32_bf16 a[88:91], v[148:151], v[88:91], a[88:91]\n" \
  "v_exp_f32 v21, v21\n" \
  "v_mfma_f32_16x16x32_bf16 a[92:95], v[148:151], v[92:95], a[92:95]\n" \
  "ds_read_b64_tr_b16 v[144:145], v196 offset:33280\n" \
  "v_cvt_pk_bf16_f32 v70, v20, v21\n" \
  "v_mfma_f32_16x16x32_bf16 a[96:99], v[152:155], v[80:83], a[96:99]\n" \
  "v_exp_f32 v22, v22\n" \
  "v_mfma_f32_16x16x32_bf16 a[100:103], v[152:155], v[84:87], a[100:103]\n" \
  "ds_read_b64_tr_b16 v[146:147], v196 offset:37376\n" \
  "v_exp_f32 v23, v23\n" \
  "v_mfma_f32_16x16x32_bf16 a[104:107], v[152:155], v[88:91], a[104:107]\n" \
  "v_exp_f32 v8, v8\n" \
  "v_mfma_f32_16x16x32_bf16 a[108:111], v[152:155], v[92:95], a[108:111]\n" \
  "ds_read_b64_tr_b16 v[148:149], v197 offset:33280\n" \
  "v_cvt_pk_bf16_f32 v71, v22, v23\n" \
  "v_mfma_f32_16x16x32_bf16 a[112:115], v[156:159], v[80:83], a[112:115]\n" \
  "v_exp_f32 v9, v9\n" \
  "v_mfma_f32_16x16x32_bf16 a[116:119], v[156:159], v[84:87], a[116:119]\n" \
  "ds_read_b64_tr_b16 v[150:151], v197 offset:37376\n" \
  "v_cvt_pk_bf16_f32 v72, v8, v9\n" \
  "v_mfma_f32_16x16x32_bf16 v[244:247], v[124:127], v[68:71], 0\n" \
  "v_exp_f32 v10, v10\n" \
  "v_mfma_f32_16x16x32_bf16 a[120:123], v[156:159], v[88:91], a[120:123]\n" \
  "v_exp_f32 v11, v11\n" \
  "v_mfma_f32_16x16x32_bf16 a[124:127], v[156:159], v[92:95], a[124:127]\n" \
  "ds_read_b64_tr_b16 v[152:153], v196 offset:33536\n" \
  "v_cvt_pk_bf16_f32 v73, v10, v11\n" \
  "s_waitcnt lgkmcnt(13)\n" \
  "v_mfma_f32_16x16x32_bf16 v[32:35], a[192:195], a[128:131], v[96:99]\n" \
  "v_exp_f32 v24, v24\n" \
  "v_mfma_f32_16x16x32_bf16 v[32:35], a[196:199], a[132:135], v[32:35]\n" \
  "ds_read_b64_tr_b16 v[154:155], v196 offset:37632\n" \
  "v_exp_f32 v25, v25\n" \
  "v_mfma_f32_16x16x32_bf16 v[32:35], a[200:203], a[136:139], v[32:35]\n" \
  "v_exp_f32 v26, v26\n" \
  "v_mfma_f32_16x16x32_bf16 v[32:35], a[204:207], a[140:143], v[32:35]\n" \
  "ds_read_b64_tr_b16 v[156:157], v197 offset:33536\n" \
  "v_cvt_pk_bf16_f32 v74, v24, v25\n" \
  "v_mfma_f32_16x16x32_bf16 v[36:39], a[192:195], a[144:147], v[100:103]\n" \
  "v_exp_f32 v27, v27\n" \
  "s_add_u32 m0, s54, 16384\n" \
  "v_mfma_f32_16x16x32_bf16 v[36:39], a[196:199], a[148:151], v[36:39]\n" \
  "ds_read_b64_tr_b16 v[158:159], v197 offset:37632\n" \
  "v_cvt_pk_bf16_f32 v75, v26, v27\n" \
  "v_mfma_f32_16x16x32_bf16 v[36:39], a[200:203], a[152:155], v[36:39]\n" \
  "buffer_load_dwordx4 v200, s[40:43], s52 offen offset:0 lds\n" \
  "v_mfma_f32_16x16x32_bf16 v[36:39], a[204:207], a[156:159], v[36:39]\n" \
  "v_exp_f32 v12, v12\n" \
  "v_mfma_f32_16x16x32_bf16 v[248:251], v[124:127], v[72:75], 0\n" \
  "v_exp_f32 v13, v13\n" \
  "v_mfma_f32_16x16x32_bf16 v[40:43], a[192:195], a[160:163], v[104:107]\n" \
  "buffer_load_dwordx4 v201, s[40:43], s52 offen offset:1024 lds\n" \
  "v_mfma_f32_16x16x32_bf16 v[40:43], a[196:199], a[164:167], v[40:43]\n" \
  "v_exp_f32 v14, v14\n" \
  "v_mfma_f32_16x16x32_bf16 v[40:43], a[200:203], a[168:171], v[40:43]\n" \
  "buffer_load_dwordx4 v202, s[40:43], s52 offen offset:2048 lds\n" \
  "v_mfma_f32_16x16x32_bf16 v[40:43], a[204:207], a[172:175], v[40:43]\n" \
  "v_exp_f32 v15, v15\n" \
  "v_mfma_f32_16x16x32_bf16 v[44:47], a[192:195], a[176:179], v[108:111]\n" \
  "buffer_load_dwordx4 v203, s[40:43], s52 offen offset:3072 lds\n" \
  "v_mfma_f32_16x16x32_bf16 v[44:47], a[196:199], a[180:183], v[44:47]\n" \
  "v_exp_f32 v28, v28\n" \
  "v_mfma_f32_16x16x32_bf16 v[44:47], a[200:203], a[184:187], v[44:47]\n" \
  "ds_read_b128 a[192:195], v192 offset:49152\n" \
  "v_cvt_pk_bf16_f32 v76, v12, v13\n" \
  "v_mfma_f32_16x16x32_bf16 v[44:47], a[204:207], a[188:191], v[44:47]\n" \
  "v_exp_f32 v29, v29\n" \
  "v_mfma_f32_16x16x32_bf16 v[48:51], a[208:211], a[128:131], v[96:99]\n" \
  "ds_read_b128 a[196:199], v193 offset:49152\n" \
  "v_cvt_pk_bf16_f32 v77, v14, v15\n" \
  "v_mfma_f32_16x16x32_bf16 v[48:51], a[212:215], a[132:135], v[48:51]\n" \
  "v_exp_f32 v30, v30\n" \
  "v_mfma_f32_16x16x32_bf16 v[48:51], a[216:219], a[136:139], v[48:51]\n" \
  "ds_read_b128 a[200:203], v194 offset:49152\n" \
  "v_cvt_pk_bf16_f32 v78, v28, v29\n" \
  "v_mfma_f32_16x16x32_bf16 v[48:51], a[220:223], a[140:143], v[48:51]\n" \
  "v_exp_f32 v31, v31\n" \
  "v_mfma_f32_16x16x32_bf16 v[52:55], a[208:211], a[144:147], v[100:103]\n" \
  "ds_read_b128 a[204:207], v195 offset:49152\n" \
  "s_add_u32 m0, s55, 0\n" \
  "v_mfma_f32_16x16x32_bf16 v[52:55], a[212:215], a[148:151], v[52:55]\n" \
  "v_cvt_pk_bf16_f32 v79, v30, v31\n" \
  "v_mfma_f32_16x16x32_bf16 v[52:55], a[216:219], a[152:155], v[52:55]\n" \
  "buffer_load_dwordx4 v204, s[44:47], s53 offen offset:0 lds\n" \
  "v_mfma_f32_16x16x32_bf16 v[52:55], a[220:223], a[156:159], v[52:55]\n" \
  "v_mfma_f32_16x16x32_bf16 v[252:255], v[124:127], v[76:79], 0\n" \
  "v_mfma_f32_16x16x32_bf16 v[56:59], a[208:211], a[160:163], v[104:107]\n" \
  "buffer_load_dwordx4 v205, s[44:47], s53 offen offset:1024 lds\n" \
  "v_mfma_f32_16x16x32_bf16 v[56:59], a[212:215], a[164:167], v[56:59]\n" \
  "v_mfma_f32_16x16x32_bf16 v[56:59], a[216:219], a[168:171], v[56:59]\n" \
  "buffer_load_dwordx4 v204, s[44:47], s53 offen offset:2048 lds\n" \
  "v_mfma_f32_16x16x32_bf16 v[56:59], a[220:223], a[172:175], v[56:59]\n" \
  "v_mfma_f32_16x16x32_bf16 v[60:63], a[208:211], a[176:179], v[108:111]\n" \
  "buffer_load_dwordx4 v205, s[44:47], s53 offen offset:3072 lds\n" \
  "v_mfma_f32_16x16x32_bf16 v[60:63], a[212:215], a[180:183], v[60:63]\n" \
  "v_max3_f32 v242, v240, v244, v248\n" \
  "v_max_f32 v242, v242, v252\n" \
  "v_mfma_f32_16x16x32_bf16 v[60:63], a[216:219], a[184:187], v[60:63]\n" \
  "ds_read_b128 a[208:211], v192 offset:53248\n" \
  "v_cmp_lt_f32 vcc, s59, v242\n" \
  "v_mfma_f32_16x16x32_bf16 v[60:63], a[220:223], a[188:191], v[60:63]\n" \
  "s_cbranch_vccnz TR_A2%=\n" \
  "RET_A2%=:\n" \
  "s_waitcnt lgkmcnt(5)\n" \
  "v_mfma_f32_16x16x32_bf16 a[0:3], v[128:131], v[64:67], a[0:3]\n" \
  "ds_read_b128 a[212:215], v193 offset:53248\n" \
  "v_add_f32 v112, v112, v240\n" \
  "v_mfma_f32_16x16x32_bf16 a[4:7], v[128:131], v[68:71], a[4:7]\n" \
  "v_add_f32 v113, v113, v244\n" \
  "v_add_f32 v114, v114, v248\n" \
  "v_mfma_f32_16x16x32_bf16 a[8:11], v[128:131], v[72:75], a[8:11]\n" \
  "ds_read_b128 a[216:219], v194 offset:53248\n" \
  "v_add_f32 v115, v115, v252\n" \
  "v_mfma_f32_16x16x32_bf16 a[12:15], v[128:131], v[76:79], a[12:15]\n" \
  "v_exp_f32 v32, v32\n" \
  "v_mfma_f32_16x16x32_bf16 a[16:19], v[132:135], v[64:67], a[16:19]\n" \
  "ds_read_b128 a[220:223], v195 offset:53248\n" \
  "v_exp_f32 v33, v33\n" \
  "v_mfma_f32_16x16x32_bf16 a[20:23], v[132:135], v[68:71], a[20:23]\n" \
  "v_exp_f32 v34, v34\n" \
  "v_mfma_f32_16x16x32_bf16 a[24:27], v[132:135], v[72:75], a[24:27]\n" \
  "s_add_u32 s52, s52, 0x4000\n" \
  "v_cvt_pk_bf16_f32 v80, v32, v33\n" \
  "v_mfma_f32_16x16x32_bf16 a[28:31], v[132:135], v[76:79], a[28:31]\n" \
  "ds_read_b64_tr_b16 v[128:129], v196 offset:40960\n" \
  "v_exp_f32 v35, v35\n" \
  "v_mfma_f32_16x16x32_bf16 a[32:35], v[136:139], v[64:67], a[32:35]\n" \
  "s_add_u32 s53, s53, 0x4000\n" \
  "v_cvt_pk_bf16_f32 v81, v34, v35\n" \
  "v_mfma_f32_16x16x32_bf16 a[36:39], v[136:139], v[68:71], a[36:39]\n" \
  "ds_read_b64_tr_b16 v[130:131], v196 offset:45056\n" \
  "v_exp_f32 v48, v48\n" \
  "v_mfma_f32_16x16x32_bf16 a[40:43], v[136:139], v[72:75], a[40:43]\n" \
  "v_exp_f32 v49, v49\n" \
  "v_mfma_f32_16x16x32_bf16 a[44:47], v[136:139], v[76:79], a[44:47]\n" \
  "ds_read_b64_tr_b16 v[132:133], v197 offset:40960\n" \
  "v_cvt_pk_bf16_f32 v82, v48, v49\n" \
  "v_mfma_f32_16x16x32_bf16 a[48:51], v[140:143], v[64:67], a[48:51]\n" \
  "v_exp_f32 v50, v50\n" \
  "v_mfma_f32_16x16x32_bf16 a[52:55], v[140:143], v[68:71], a[52:55]\n" \
  "ds_read_b64_tr_b16 v[134:135], v197 offset:45056\n" \
  "v_exp_f32 v51, v51\n" \
  "v_mfma_f32_16x16x32_bf16 a[56:59], v[140:143], v[72:75], a[56:59]\n" \
  "v_exp_f32 v36, v36\n" \
  "v_mfma_f32_16x16x32_bf16 a[60:63], v[140:143], v[76:79], a[60:63]\n" \
  "ds_read_b64_tr_b16 v[136:137], v196 offset:41216\n" \
  "v_cvt_pk_bf16_f32 v83, v50, v51\n" \
  "v_mfma_f32_16x16x32_bf16 a[64:67], v[144:147], v[64:67], a[64:67]\n" \
  "v_exp_f32 v37, v37\n" \
  "v_mfma_f32_16x16x32_bf16 a[68:71], v[144:147], v[68:71], a[68:71]\n" \
  "ds_read_b64_tr_b16 v[138:139], v196 offset:45312\n" \
  "v_cvt_pk_bf16_f32 v84, v36, v37\n" \
  "v_mfma_f32_16x16x32_bf16 v[240:243], v[124:127], v[80:83], 0\n" \
  "v_exp_f32 v38, v38\n" \
  "v_mfma_f32_16x16x32_bf16 a[72:75], v[144:147], v[72:75], a[72:75]\n" \
  "v_exp_f32 v39, v39\n" \
  "v_mfma_f32_16x16x32_bf16 a[76:79], v[144:147], v[76:79], a[76:79]\n" \
  "ds_read_b64_tr_b16 v[140:141], v197 offset:41216\n" \
  "v_cvt_pk_bf16_f32 v85, v38, v39\n" \
  "v_mfma_f32_16x16x32_bf16 a[80:83], v[148:151], v[64:67], a[80:83]\n" \
  "v_exp_f32 v52, v52\n" \
  "v_mfma_f32_16x16x32_bf16 a[84:87], v[148:151], v[68:71], a[84:87]\n" \
  "ds_read_b64_tr_b16 v[142:143], v197 offset:45312\n" \
  "v_exp_f32 v53, v53\n" \
  "v_mfma_f32_16x16x32_bf16 a[88:91], v[148:151], v[72:75], a[88:91]\n" \
  "v_exp_f32 v54, v54\n" \
  "v_mfma_f32_16x16x32_bf16 a[92:95], v[148:151], v[76:79], a[92:95]\n" \
  "ds_read_b64_tr_b16 v[144:145], v196 offset:41472\n" \
  "v_cvt_pk_bf16_f32 v86, v52, v53\n" \
  "v_mfma_f32_16x16x32_bf16 a[96:99], v[152:155], v[64:67], a[96:99]\n" \
  "v_exp_f32 v55, v55\n" \
  "v_mfma_f32_16x16x32_bf16 a[100:103], v[152:155], v[68:71], a[100:103]\n" \
  "ds_read_b64_tr_b16 v[146:147], v196 offset:45568\n" \
  "v_cvt_pk_bf16_f32 v87, v54, v55\n" \
  "v_mfma_f32_16x16x32_bf16 a[104:107], v[152:155], v[72:75], a[104:107]\n" \
  "v_exp_f32 v40, v40\n" \
  "v_mfma_f32_16x16x32_bf16 a[108:111], v[152:155], v[76:79], a[108:111]\n" \
  "ds_read_b64_tr_b16 v[148:149], v197 offset:41472\n" \
  "v_exp_f32 v41, v41\n" \
  "v_mfma_f32_16x16x32_bf16 v[244:247], v[124:127], v[84:87], 0\n" \
  "v_mfma_f32_16x16x32_bf16 a[112:115], v[156:159], v[64:67], a[112:115]\n" \
  "v_exp_f32 v42, v42\n" \
  "v_mfma_f32_16x16x32_bf16 a[116:119], v[156:159], v[68:71], a[116:119]\n" \
  "ds_read_b64_tr_b16 v[150:151], v197 offset:45568\n" \
  "v_cvt_pk_bf16_f32 v88, v40, v41\n" \
  "v_mfma_f32_16x16x32_bf16 a[120:123], v[156:159], v[72:75], a[120:123]\n" \
  "v_exp_f32 v43, v43\n" \
  "v_mfma_f32_16x16x32_bf16 a[124:127], v[156:159], v[76:79], a[124:127]\n" \
  "ds_read_b64_tr_b16 v[152:153], v196 offset:41728\n" \
  "v_cvt_pk_bf16_f32 v89, v42, v43\n" \
  "s_waitcnt lgkmcnt(13)\n" \
  "v_mfma_f32_16x16x32_bf16 v[0:3], a[192:195], a[128:131], v[96:99]\n" \
  "v_exp_f32 v56, v56\n" \
  "v_mfma_f32_16x16x32_bf16 v[0:3], a[196:199], a[132:135], v[0:3]\n" \
  "ds_read_b64_tr_b16 v[154:155], v196 offset:45824\n" \
  "v_mfma_f32_16x16x32_bf16 v[0:3], a[200:203], a[136:139], v[0:3]\n" \
  "v_exp_f32 v57, v57\n" \
  "v_mfma_f32_16x16x32_bf16 v[0:3], a[204:207], a[140:143], v[0:3]\n" \
  "ds_read_b64_tr_b16 v[156:157], v197 offset:41728\n" \
  "v_cvt_pk_bf16_f32 v90, v56, v57\n" \
  "v_mfma_f32_16x16x32_bf16 v[4:7], a[192:195], a[144:147], v[100:103]\n" \
  "v_exp_f32 v58, v58\n" \
  "v_mfma_f32_16x16x32_bf16 v[4:7], a[196:199], a[148:151], v[4:7]\n" \
  "ds_read_b64_tr_b16 v[158:159], v197 offset:45824\n" \
  "v_mfma_f32_16x16x32_bf16 v[4:7], a[200:203], a[152:155], v[4:7]\n" \
  "v_exp_f32 v59, v59\n" \
  "v_mfma_f32_16x16x32_bf16 v[4:7], a[204:207], a[156:159], v[4:7]\n" \
  "v_exp_f32 v44, v44\n" \
  "v_mfma_f32_16x16x32_bf16 v[8:11], a[192:195], a[160:163], v[104:107]\n" \
  "v_exp_f32 v45, v45\n" \
  "v_mfma_f32_16x16x32_bf16 v[8:11], a[196:199], a[164:167], v[8:11]\n" \
  "v_exp_f32 v46, v46\n" \
  "v_mfma_f32_16x16x32_bf16 v[8:11], a[200:203], a[168:171], v[8:11]\n" \
  "v_exp_f32 v47, v47\n" \
  "v_mfma_f32_16x16x32_bf16 v[8:11], a[204:207], a[172:175], v[8:11]\n" \
  "v_exp_f32 v60, v60\n" \
  "v_mfma_f32_16x16x32_bf16 v[12:15], a[192:195], a[176:179], v[108:111]\n" \
  "v_exp_f32 v61, v61\n" \
  "v_mfma_f32_16x16x32_bf16 v[12:15], a[196:199], a[180:183], v[12:15]\n" \
  "v_exp_f32 v62, v62\n" \
  "v_mfma_f32_16x16x32_bf16 v[12:15], a[200:203], a[184:187], v[12:15]\n" \
  "ds_read_b128 a[192:195], v192 offset:57344\n" \
  "v_cvt_pk_bf16_f32 v91, v58, v59\n" \
  "v_mfma_f32_16x16x32_bf16 v[12:15], a[204:207], a[188:191], v[12:15]\n" \
  "v_exp_f32 v63, v63\n" \
  "v_mfma_f32_16x16x32_bf16 v[16:19], a[208:211], a[128:131], v[96:99]\n" \
  "ds_read_b128 a[196:199], v193 offset:57344\n" \
  "v_cvt_pk_bf16_f32 v92, v44, v45\n" \
  "v_mfma_f32_16x16x32_bf16 v[248:251], v[124:127], v[88:91], 0\n" \
  "v_cvt_pk_bf16_f32 v93, v46, v47\n" \
  "v_cvt_pk_bf16_f32 v94, v60, v61\n" \
  "v_mfma_f32_16x16x32_bf16 v[16:19], a[212:215], a[132:135], v[16:19]\n" \
  "v_cvt_pk_bf16_f32 v95, v62, v63\n" \
  "v_mfma_f32_16x16x32_bf16 v[16:19], a[216:219], a[136:139], v[16:19]\n" \
  "ds_read_b128 a[200:203], v194 offset:57344\n" \
  "v_mfma_f32_16x16x32_bf16 v[16:19], a[220:223], a[140:143], v[16:19]\n" \
  "v_mfma_f32_16x16x32_bf16 v[252:255], v[124:127], v[92:95], 0\n" \
  "v_mfma_f32_16x16x32_bf16 v[20:23], a[208:211], a[144:147], v[100:103]\n" \
  "ds_read_b128 a[204:207], v195 offset:57344\n" \
  "v_mfma_f32_16x16x32_bf16 v[20:23], a[212:215], a[148:151], v[20:23]\n" \
  "v_mfma_f32_16x16x32_bf16 v[20:23], a[216:219], a[152:155], v[20:23]\n" \
  "v_mfma_f32_16x16x32_bf16 v[20:23], a[220:223], a[156:159], v[20:23]\n" \
  "v_mfma_f32_16x16x32_bf16 v[24:27], a[208:211], a[160:163], v[104:107]\n" \
  "v_mfma_f32_16x16x32_bf16 v[24:27], a[212:215], a[164:167], v[24:27]\n" \
  "v_mfma_f32_16x16x32_bf16 v[24:27], a[216:219], a[168:171], v[24:27]\n" \
  "v_mfma_f32_16x16x32_bf16 v[24:27], a[220:223], a[172:175], v[24:27]\n" \
  "v_mfma_f32_16x16x32_bf16 v[28:31], a[208:211], a[176:179], v[108:111]\n" \
  "v_mfma_f32_16x16x32_bf16 v[28:31], a[212:215], a[180:183], v[28:31]\n" \
  "v_max3_f32 v242, v240, v244, v248\n" \
  "v_max_f32 v242, v242, v252\n" \
  "v_mfma_f32_16x16x32_bf16 v[28:31], a[216:219], a[184:187], v[28:31]\n" \
  "ds_read_b128 a[208:211], v192 offset:61440\n" \
  "v_cmp_lt_f32 vcc, s59, v242\n" \
  "v_mfma_f32_16x16x32_bf16 v[28:31], a[220:223], a[188:191], v[28:31]\n" \
  "s_cbranch_vccnz TR_B3%=\n" \
  "RET_B3%=:\n" \
  "s_waitcnt lgkmcnt(5)\n" \
  "v_mfma_f32_16x16x32_bf16 a[0:3], v[128:131], v[80:83], a[0:3]\n" \
  "ds_read_b128 a[212:215], v193 offset:61440\n" \
  "v_add_f32 v112, v112, v240\n" \
  "v_mfma_f32_16x16x32_bf16 a[4:7], v[128:131], v[84:87], a[4:7]\n" \
  "v_add_f32 v113, v113, v244\n" \
  "v_add_f32 v114, v114, v248\n" \
  "v_mfma_f32_16x16x32_bf16 a[8:11], v[128:131], v[88:91], a[8:11]\n" \
  "ds_read_b128 a[216:219], v194 offset:61440\n" \
  "v_add_f32 v115, v115, v252\n" \
  "v_mfma_f32_16x16x32_bf16 a[12:15], v[128:131], v[92:95], a[12:15]\n" \
  "v_exp_f32 v0, v0\n" \
  "v_mfma_f32_16x16x32_bf16 a[16:19], v[132:135], v[80:83], a[16:19]\n" \
  "ds_read_b128 a[220:223], v195 offset:61440\n" \
  "v_exp_f32 v1, v1\n" \
  "v_mfma_f32_16x16x32_bf16 a[20:23], v[132:135], v[84:87], a[20:23]\n" \
  "s_waitcnt vmcnt(8)\n" \
  "s_barrier\n" \
  "v_mfma_f32_16x16x32_bf16 a[24:27], v[132:135], v[88:91], a[24:27]\n" \
  "v_exp_f32 v2, v2\n" \
  "v_mfma_f32_16x16x32_bf16 a[28:31], v[132:135], v[92:95], a[28:31]\n" \
  "ds_read_b64_tr_b16 v[128:129], v196 offset:49152\n" \
  "v_cvt_pk_bf16_f32 v64, v0, v1\n" \
  "v_mfma_f32_16x16x32_bf16 a[32:35], v[136:139], v[80:83], a[32:35]\n" \
  "v_exp_f32 v3, v3\n" \
  "v_mfma_f32_16x16x32_bf16 a[36:39], v[136:139], v[84:87], a[36:39]\n" \
  "ds_read_b64_tr_b16 v[130:131], v196 offset:53248\n" \
  "v_cvt_pk_bf16_f32 v65, v2, v3\n" \
  "v_mfma_f32_16x16x32_bf16 a[40:43], v[136:139], v[88:91], a[40:43]\n" \
  "v_exp_f32 v16, v16\n" \
  "v_mfma_f32_16x16x32_bf16 a[44:47], v[136:139], v[92:95], a[44:47]\n" \
  "ds_read_b64_tr_b16 v[132:133], v197 offset:49152\n" \
  "v_exp_f32 v17, v17\n" \
  "v_mfma_f32_16x16x32_bf16 a[48:51], v[140:143], v[80:83], a[48:51]\n" \
  "v_exp_f32 v18, v18\n" \
  "v_mfma_f32_16x16x32_bf16 a[52:55], v[140:143], v[84:87], a[52:55]\n" \
  "ds_read_b64_tr_b16 v[134:135], v197 offset:53248\n" \
  "v_cvt_pk_bf16_f32 v66, v16, v17\n" \
  "v_mfma_f32_16x16x32_bf16 a[56:59], v[140:143], v[88:91], a[56:59]\n" \
  "v_exp_f32 v19, v19\n" \
  "v_mfma_f32_16x16x32_bf16 a[60:63], v[140:143], v[92:95], a[60:63]\n" \
  "ds_read_b64_tr_b16 v[136:137], v196 offset:49408\n" \
  "v_cvt_pk_bf16_f32 v67, v18, v19\n" \
  "v_mfma_f32_16x16x32_bf16 a[64:67], v[144:147], v[80:83], a[64:67]\n" \
  "v_exp_f32 v4, v4\n" \
  "v_mfma_f32_16x16x32_bf16 a[68:71], v[144:147], v[84:87], a[68:71]\n" \
  "ds_read_b64_tr_b16 v[138:139], v196 offset:53504\n" \
  "v_exp_f32 v5, v5\n" \
  "v_mfma_f32_16x16x32_bf16 v[240:243], v[124:127], v[64:67], 0\n" \
  "v_exp_f32 v6, v6\n" \
  "v_mfma_f32_16x16x32_bf16 a[72:75], v[144:147], v[88:91], a[72:75]\n" \
  "v_exp_f32 v7, v7\n" \
  "v_mfma_f32_16x16x32_bf16 a[76:79], v[144:147], v[92:95], a[76:79]\n" \
  "ds_read_b64_tr_b16 v[140:141], v197 offset:49408\n" \
  "v_cvt_pk_bf16_f32 v68, v4, v5\n" \
  "v_mfma_f32_16x16x32_bf16 a[80:83], v[148:151], v[80:83], a[80:83]\n" \
  "v_exp_f32 v20, v20\n" \
  "v_mfma_f32_16x16x32_bf16 a[84:87], v[148:151], v[84:87], a[84:87]\n" \
  "ds_read_b64_tr_b16 v[142:143], v197 offset:53504\n" \
  "v_cvt_pk_bf16_f32 v69, v6, v7\n" \
  "v_mfma_f32_16x16x32_bf16 a[88:91], v[148:151], v[88:91], a[88:91]\n" \
  "v_exp_f32 v21, v21\n" \
  "v_mfma_f32_16x16x32_bf16 a[92:95], v[148:151], v[92:95], a[92:95]\n" \
  "ds_read_b64_tr_b16 v[144:145], v196 offset:49664\n" \
  "v_cvt_pk_bf16_f32 v70, v20, v21\n" \
  "v_mfma_f32_16x16x32_bf16 a[96:99], v[152:155], v[80:83], a[96:99]\n" \
  "v_exp_f32 v22, v22\n" \
  "v_mfma_f32_16x16x32_bf16 a[100:103], v[152:155], v[84:87], a[100:103]\n" \
  "ds_read_b64_tr_b16 v[146:147], v196 offset:53760\n" \
  "v_exp_f32 v23, v23\n" \
  "v_mfma_f32_16x16x32_bf16 a[104:107], v[152:155], v[88:91], a[104:107]\n" \
  "v_exp_f32 v8, v8\n" \
  "v_mfma_f32_16x16x32_bf16 a[108:111], v[152:155], v[92:95], a[108:111]\n" \
  "ds_read_b64_tr_b16 v[148:149], v197 offset:49664\n" \
  "v_cvt_pk_bf16_f32 v71, v22, v23\n" \
  "v_mfma_f32_16x16x32_bf16 a[112:115], v[156:159], v[80:83], a[112:115]\n" \
  "v_exp_f32 v9, v9\n" \
  "v_mfma_f32_16x16x32_bf16 a[116:119], v[156:159], v[84:87], a[116:119]\n" \
  "ds_read_b64_tr_b16 v[150:151], v197 offset:53760\n" \
  "v_cvt_pk_bf16_f32 v72, v8, v9\n" \
  "v_mfma_f32_16x16x32_bf16 v[244:247], v[124:127], v[68:71], 0\n" \
  "v_exp_f32 v10, v10\n" \
  "v_mfma_f32_16x16x32_bf16 a[120:123], v[156:159], v[88:91], a[120:123]\n" \
  "v_exp_f32 v11, v11\n" \
  "v_mfma_f32_16x16x32_bf16 a[124:127], v[156:159], v[92:95], a[124:127]\n" \
  "ds_read_b64_tr_b16 v[152:153], v196 offset:49920\n" \
  "v_cvt_pk_bf16_f32 v73, v10, v11\n" \
  "s_waitcnt lgkmcnt(13)\n" \
  "v_mfma_f32_16x16x32_bf16 v[32:35], a[192:195], a[128:131], v[96:99]\n" \
  "v_exp_f32 v24, v24\n" \
  "v_mfma_f32_16x16x32_bf16 v[32:35], a[196:199], a[132:135], v[32:35]\n" \
  "ds_read_b64_tr_b16 v[154:155], v196 offset:54016\n" \
  "v_exp_f32 v25, v25\n" \
  "v_mfma_f32_16x16x32_bf16 v[32:35], a[200:203], a[136:139], v[32:35]\n" \
  "v_exp_f32 v26, v26\n" \
  "v_mfma_f32_16x16x32_bf16 v[32:35], a[204:207], a[140:143], v[32:35]\n" \
  "ds_read_b64_tr_b16 v[156:157], v197 offset:49920\n" \
  "v_cvt_pk_bf16_f32 v74, v24, v25\n" \
  "v_mfma_f32_16x16x32_bf16 v[36:39], a[192:195], a[144:147], v[100:103]\n" \
  "v_exp_f32 v27, v27\n" \
  "s_add_u32 m0, s54, 32768\n" \
  "v_mfma_f32_16x16x32_bf16 v[36:39], a[196:199], a[148:151], v[36:39]\n" \
  "ds_read_b64_tr_b16 v[158:159], v197 offset:54016\n" \
  "v_cvt_pk_bf16_f32 v75, v26, v27\n" \
  "v_mfma_f32_16x16x32_bf16 v[36:39], a[200:203], a[152:155], v[36:39]\n" \
  "buffer_load_dwordx4 v200, s[40:43], s52 offen offset:0 lds\n" \
  "v_mfma_f32_16x16x32_bf16 v[36:39], a[204:207], a[156:159], v[36:39]\n" \
  "v_exp_f32 v12, v12\n" \
  "v_mfma_f32_16x16x32_bf16 v[248:251], v[124:127], v[72:75], 0\n" \
  "v_exp_f32 v13, v13\n" \
  "v_mfma_f32_16x16x32_bf16 v[40:43], a[192:195], a[160:163], v[104:107]\n" \
  "buffer_load_dwordx4 v201, s[40:43], s52 offen offset:1024 lds\n" \
  "v_mfma_f32_16x16x32_bf16 v[40:43], a[196:199], a[164:167], v[40:43]\n" \
  "v_exp_f32 v14, v14\n" \
  "v_mfma_f32_16x16x32_bf16 v[40:43], a[200:203], a[168:171], v[40:43]\n" \
  "buffer_load_dwordx4 v202, s[40:43], s52 offen offset:2048 lds\n" \
  "v_mfma_f32_16x16x32_bf16 v[40:43], a[204:207], a[172:175], v[40:43]\n" \
  "v_exp_f32 v15, v15\n" \
  "v_mfma_f32_16x16x32_bf16 v[44:47], a[192:195], a[176:179], v[108:111]\n" \
  "buffer_load_dwordx4 v203, s[40:43], s52 offen offset:3072 lds\n" \
  "v_mfma_f32_16x16x32_bf16 v[44:47], a[196:199], a[180:183], v[44:47]\n" \
  "v_exp_f32 v28, v28\n" \
  "v_mfma_f32_16x16x32_bf16 v[44:47], a[200:203], a[184:187], v[44:47]\n" \
  "ds_read_b128 a[192:195], v192 offset:0\n" \
  "v_cvt_pk_bf16_f32 v76, v12, v13\n" \
  "v_mfma_f32_16x16x32_bf16 v[44:47], a[204:207], a[188:191], v[44:47]\n" \
  "v_exp_f32 v29, v29\n" \
  "v_mfma_f32_16x16x32_bf16 v[48:51], a[208:211], a[128:131], v[96:99]\n" \
  "ds_read_b128 a[196:199], v193 offset:0\n" \
  "v_cvt_pk_bf16_f32 v77, v14, v15\n" \
  "v_mfma_f32_16x16x32_bf16 v[48:51], a[212:215], a[132:135], v[48:51]\n" \
  "v_exp_f32 v30, v30\n" \
  "v_mfma_f32_16x16x32_bf16 v[48:51], a[216:219], a[136:139], v[48:51]\n" \
  "ds_read_b128 a[200:203], v194 offset:0\n" \
  "v_cvt_pk_bf16_f32 v78, v28, v29\n" \
  "v_mfma_f32_16x16x32_bf16 v[48:51], a[220:223], a[140:143], v[48:51]\n" \
  "v_exp_f32 v31, v31\n" \
  "v_mfma_f32_16x16x32_bf16 v[52:55], a[208:211], a[144:147], v[100:103]\n" \
  "ds_read_b128 a[204:207], v195 offset:0\n" \
  "s_add_u32 m0, s55, 16384\n" \
  "v_mfma_f32_16x16x32_bf16 v[52:55], a[212:215], a[148:151], v[52:55]\n" \
  "v_cvt_pk_bf16_f32 v79, v30, v31\n" \
  "v_mfma_f32_16x16x32_bf16 v[52:55], a[216:219], a[152:155], v[52:55]\n" \
  "buffer_load_dwordx4 v204, s[44:47], s53 offen offset:0 lds\n" \
  "v_mfma_f32_16x16x32_bf16 v[52:55], a[220:223], a[156:159], v[52:55]\n" \
  "v_mfma_f32_16x16x32_bf16 v[252:255], v[124:127], v[76:79], 0\n" \
  "v_mfma_f32_16x16x32_bf16 v[56:59], a[208:211], a[160:163], v[104:107]\n" \
  "buffer_load_dwordx4 v205, s[44:47], s53 offen offset:1024 lds\n" \
  "v_mfma_f32_16x16x32_bf16 v[56:59], a[212:215], a[164:167], v[56:59]\n" \
  "v_mfma_f32_16x16x32_bf16 v[56:59], a[216:219], a[168:171], v[56:59]\n" \
  "buffer_load_dwordx4 v204, s[44:47], s53 offen offset:2048 lds\n" \
  "v_mfma_f32_16x16x32_bf16 v[56:59], a[220:223], a[172:175], v[56:59]\n" \
  "v_mfma_f32_16x16x32_bf16 v[60:63], a[208:211], a[176:179], v[108:111]\n" \
  "buffer_load_dwordx4 v205, s[44:47], s53 offen offset:3072 lds\n" \
  "v_mfma_f32_16x16x32_bf16 v[60:63], a[212:215], a[180:183], v[60:63]\n" \
  "v_max3_f32 v242, v240, v244, v248\n" \
  "v_max_f32 v242, v242, v252\n" \
  "v_mfma_f32_16x16x32_bf16 v[60:63], a[216:219], a[184:187], v[60:63]\n" \
  "ds_read_b128 a[208:211], v192 offset:4096\n" \
  "v_cmp_lt_f32 vcc, s59, v242\n" \
  "v_mfma_f32_16x16x32_bf16 v[60:63], a[220:223], a[188:191], v[60:63]\n" \
  "s_cbranch_vccnz TR_A3%=\n" \
  "RET_A3%=:\n" \
  "s_waitcnt lgkmcnt(5)\n" \
  "v_mfma_f32_16x16x32_bf16 a[0:3], v[128:131], v[64:67], a[0:3]\n" \
  "ds_read_b128 a[212:215], v193 offset:4096\n" \
  "v_add_f32 v112, v112, v240\n" \
  "v_mfma_f32_16x16x32_bf16 a[4:7], v[128:131], v[68:71], a[4:7]\n" \
  "v_add_f32 v113, v113, v244\n" \
  "v_add_f32 v114, v114, v248\n" \
  "v_mfma_f32_16x16x32_bf16 a[8:11], v[128:131], v[72:75], a[8:11]\n" \
  "ds_read_b128 a[216:219], v194 offset:4096\n" \
  "v_add_f32 v115, v115, v252\n" \
  "v_mfma_f32_16x16x32_bf16 a[12:15], v[128:131], v[76:79], a[12:15]\n" \
  "v_exp_f32 v32, v32\n" \
  "v_mfma_f32_16x16x32_bf16 a[16:19], v[132:135], v[64:67], a[16:19]\n" \
  "ds_read_b128 a[220:223], v195 offset:4096\n" \
  "v_exp_f32 v33, v33\n" \
  "v_mfma_f32_16x16x32_bf16 a[20:23], v[132:135], v[68:71], a[20:23]\n" \
  "v_exp_f32 v34, v34\n" \
  "v_mfma_f32_16x16x32_bf16 a[24:27], v[132:135], v[72:75], a[24:27]\n" \
  "s_add_u32 s52, s52, 0x4000\n" \
  "v_cvt_pk_bf16_f32 v80, v32, v33\n" \
  "v_mfma_f32_16x16x32_bf16 a[28:31], v[132:135], v[76:79], a[28:31]\n" \
  "ds_read_b64_tr_b16 v[128:129], v196 offset:57344\n" \
  "v_exp_f32 v35, v35\n" \
  "v_mfma_f32_16x16x32_bf16 a[32:35], v[136:139], v[64:67], a[32:35]\n" \
  "s_add_u32 s53, s53, 0x4000\n" \
  "v_cvt_pk_bf16_f32 v81, v34, v35\n" \
  "v_mfma_f32_16x16x32_bf16 a[36:39], v[136:139], v[68:71], a[36:39]\n" \
  "ds_read_b64_tr_b16 v[130:131], v196 offset:61440\n" \
  "v_exp_f32 v48, v48\n" \
  "v_mfma_f32_16x16x32_bf16 a[40:43], v[136:139], v[72:75], a[40:43]\n" \
  "v_exp_f32 v49, v49\n" \
  "v_mfma_f32_16x16x32_bf16 a[44:47], v[136:139], v[76:79], a[44:47]\n" \
  "ds_read_b64_tr_b16 v[132:133], v197 offset:57344\n" \
  "v_cvt_pk_bf16_f32 v82, v48, v49\n" \
  "v_mfma_f32_16x16x32_bf16 a[48:51], v[140:143], v[64:67], a[48:51]\n" \
  "v_exp_f32 v50, v50\n" \
  "v_mfma_f32_16x16x32_bf16 a[52:55], v[140:143], v[68:71], a[52:55]\n" \
  "ds_read_b64_tr_b16 v[134:135], v197 offset:61440\n" \
  "v_exp_f32 v51, v51\n" \
  "v_mfma_f32_16x16x32_bf16 a[56:59], v[140:143], v[72:75], a[56:59]\n" \
  "v_exp_f32 v36, v36\n" \
  "v_mfma_f32_16x16x32_bf16 a[60:63], v[140:143], v[76:79], a[60:63]\n" \
  "ds_read_b64_tr_b16 v[136:137], v196 offset:57600\n" \
  "v_cvt_pk_bf16_f32 v83, v50, v51\n" \
  "v_mfma_f32_16x16x32_bf16 a[64:67], v[144:147], v[64:67], a[64:67]\n" \
  "v_exp_f32 v37, v37\n" \
  "v_mfma_f32_16x16x32_bf16 a[68:71], v[144:147], v[68:71], a[68:71]\n" \
  "ds_read_b64_tr_b16 v[138:139], v196 offset:61696\n" \
  "v_cvt_pk_bf16_f32 v84, v36, v37\n" \
  "v_mfma_f32_16x16x32_bf16 v[240:243], v[124:127], v[80:83], 0\n" \
  "v_exp_f32 v38, v38\n" \
  "v_mfma_f32_16x16x32_bf16 a[72:75], v[144:147], v[72:75], a[72:75]\n" \
  "v_exp_f32 v39, v39\n" \
  "v_mfma_f32_16x16x32_bf16 a[76:79], v[144:147], v[76:79], a[76:79]\n" \
  "ds_read_b64_tr_b16 v[140:141], v197 offset:57600\n" \
  "v_cvt_pk_bf16_f32 v85, v38, v39\n" \
  "v_mfma_f32_16x16x32_bf16 a[80:83], v[148:151], v[64:67], a[80:83]\n" \
  "v_exp_f32 v52, v52\n" \
  "v_mfma_f32_16x16x32_bf16 a[84:87], v[148:151], v[68:71], a[84:87]\n" \
  "ds_read_b64_tr_b16 v[142:143], v197 offset:61696\n" \
  "v_exp_f32 v53, v53\n" \
  "v_mfma_f32_16x16x32_bf16 a[88:91], v[148:151], v[72:75], a[88:91]\n" \
  "v_exp_f32 v54, v54\n" \
  "v_mfma_f32_16x16x32_bf16 a[92:95], v[148:151], v[76:79], a[92:95]\n" \
  "ds_read_b64_tr_b16 v[144:145], v196 offset:57856\n" \
  "v_cvt_pk_bf16_f32 v86, v52, v53\n" \
  "v_mfma_f32_16x16x32_bf16 a[96:99], v[152:155], v[64:67], a[96:99]\n" \
  "v_exp_f32 v55, v55\n" \
  "v_mfma_f32_16x16x32_bf16 a[100:103], v[152:155], v[68:71], a[100:103]\n" \
  "ds_read_b64_tr_b16 v[146:147], v196 offset:61952\n" \
  "v_cvt_pk_bf16_f32 v87, v54, v55\n" \
  "v_mfma_f32_16x16x32_bf16 a[104:107], v[152:155], v[72:75], a[104:107]\n" \
  "v_exp_f32 v40, v40\n" \
  "v_mfma_f32_16x16x32_bf16 a[108:111], v[152:155], v[76:79], a[108:111]\n" \
  "ds_read_b64_tr_b16 v[148:149], v197 offset:57856\n" \
  "v_exp_f32 v41, v41\n" \
  "v_mfma_f32_16x16x32_bf16 v[244:247], v[124:127], v[84:87], 0\n" \
  "v_mfma_f32_16x16x32_bf16 a[112:115], v[156:159], v[64:67], a[112:115]\n" \
  "v_exp_f32 v42, v42\n" \
  "v_mfma_f32_16x16x32_bf16 a[116:119], v[156:159], v[68:71], a[116:119]\n" \
  "ds_read_b64_tr_b16 v[150:151], v197 offset:61952\n" \
  "v_cvt_pk_bf16_f32 v88, v40, v41\n" \
  "v_mfma_f32_16x16x32_bf16 a[120:123], v[156:159], v[72:75], a[120:123]\n" \
  "v_exp_f32 v43, v43\n" \
  "v_mfma_f32_16x16x32_bf16 a[124:127], v[156:159], v[76:79], a[124:127]\n" \
  "ds_read_b64_tr_b16 v[152:153], v196 offset:58112\n" \
  "v_cvt_pk_bf16_f32 v89, v42, v43\n" \
  "s_sub_u32 s58, s58, 1\n" \
  "s_cmp_eq_u32 s58, 0\n" \
  "s_cbranch_scc1 TAIL%=\n" \
  "s_waitcnt lgkmcnt(13)\n" \
  "v_mfma_f32_16x16x32_bf16 v[0:3], a[192:195], a[128:131], v[96:99]\n" \
  "v_exp_f32 v56, v56\n" \
  "v_mfma_f32_16x16x32_bf16 v[0:3], a[196:199], a[132:135], v[0:3]\n" \
  "ds_read_b64_tr_b16 v[154:155], v196 offset:62208\n" \
  "v_mfma_f32_16x16x32_bf16 v[0:3], a[200:203], a[136:139], v[0:3]\n" \
  "v_exp_f32 v57, v57\n" \
  "v_mfma_f32_16x16x32_bf16 v[0:3], a[204:207], a[140:143], v[0:3]\n" \
  "ds_read_b64_tr_b16 v[156:157], v197 offset:58112\n" \
  "v_cvt_pk_bf16_f32 v90, v56, v57\n" \
  "v_mfma_f32_16x16x32_bf16 v[4:7], a[192:195], a[144:147], v[100:103]\n" \
  "v_exp_f32 v58, v58\n" \
  "v_mfma_f32_16x16x32_bf16 v[4:7], a[196:199], a[148:151], v[4:7]\n" \
  "ds_read_b64_tr_b16 v[158:159], v197 offset:62208\n" \
  "v_mfma_f32_16x16x32_bf16 v[4:7], a[200:203], a[152:155], v[4:7]\n" \
  "v_exp_f32 v59, v59\n" \
  "v_mfma_f32_16x16x32_bf16 v[4:7], a[204:207], a[156:159], v[4:7]\n" \
  "v_exp_f32 v44, v44\n" \
  "v_mfma_f32_16x16x32_bf16 v[8:11], a[192:195], a[160:163], v[104:107]\n" \
  "v_exp_f32 v45, v45\n" \
  "v_mfma_f32_16x16x32_bf16 v[8:11], a[196:199], a[164:167], v[8:11]\n" \
  "v_exp_f32 v46, v46\n" \
  "v_mfma_f32_16x16x32_bf16 v[8:11], a[200:203], a[168:171], v[8:11]\n" \
  "v_exp_f32 v47, v47\n" \
  "v_mfma_f32_16x16x32_bf16 v[8:11], a[204:207], a[172:175], v[8:11]\n" \
  "v_exp_f32 v60, v60\n" \
  "v_mfma_f32_16x16x32_bf16 v[12:15], a[192:195], a[176:179], v[108:111]\n" \
  "v_exp_f32 v61, v61\n" \
  "v_mfma_f32_16x16x32_bf16 v[12:15], a[196:199], a[180:183], v[12:15]\n" \
  "v_exp_f32 v62, v62\n" \
  "v_mfma_f32_16x16x32_bf16 v[12:15], a[200:203], a[184:187], v[12:15]\n" \
  "ds_read_b128 a[192:195], v192 offset:8192\n" \
  "v_cvt_pk_bf16_f32 v91, v58, v59\n" \
  "v_mfma_f32_16x16x32_bf16 v[12:15], a[204:207], a[188:191], v[12:15]\n" \
  "v_exp_f32 v63, v63\n" \
  "v_mfma_f32_16x16x32_bf16 v[16:19], a[208:211], a[128:131], v[96:99]\n" \
  "ds_read_b128 a[196:199], v193 offset:8192\n" \
  "v_cvt_pk_bf16_f32 v92, v44, v45\n" \
  "v_mfma_f32_16x16x32_bf16 v[248:251], v[124:127], v[88:91], 0\n" \
  "v_cvt_pk_bf16_f32 v93, v46, v47\n" \
  "v_cvt_pk_bf16_f32 v94, v60, v61\n" \
  "v_mfma_f32_16x16x32_bf16 v[16:19], a[212:215], a[132:135], v[16:19]\n" \
  "v_cvt_pk_bf16_f32 v95, v62, v63\n" \
  "v_mfma_f32_16x16x32_bf16 v[16:19], a[216:219], a[136:139], v[16:19]\n" \
  "ds_read_b128 a[200:203], v194 offset:8192\n" \
  "v_mfma_f32_16x16x32_bf16 v[16:19], a[220:223], a[140:143], v[16:19]\n" \
  "v_mfma_f32_16x16x32_bf16 v[252:255], v[124:127], v[92:95], 0\n" \
  "v_mfma_f32_16x16x32_bf16 v[20:23], a[208:211], a[144:147], v[100:103]\n" \
  "ds_read_b128 a[204:207], v195 offset:8192\n" \
  "v_mfma_f32_16x16x32_bf16 v[20:23], a[212:215], a[148:151], v[20:23]\n" \
  "v_mfma_f32_16x16x32_bf16 v[20:23], a[216:219], a[152:155], v[20:23]\n" \
  "v_mfma_f32_16x16x32_bf16 v[20:23], a[220:223], a[156:159], v[20:23]\n" \
  "v_mfma_f32_16x16x32_bf16 v[24:27], a[208:211], a[160:163], v[104:107]\n" \
  "v_mfma_f32_16x16x32_bf16 v[24:27], a[212:215], a[164:167], v[24:27]\n" \
  "v_mfma_f32_16x16x32_bf16 v[24:27], a[216:219], a[168:171], v[24:27]\n" \
  "v_mfma_f32_16x16x32_bf16 v[24:27], a[220:223], a[172:175], v[24:27]\n" \
  "v_mfma_f32_16x16x32_bf16 v[28:31], a[208:211], a[176:179], v[108:111]\n" \
  "v_mfma_f32_16x16x32_bf16 v[28:31], a[212:215], a[180:183], v[28:31]\n" \
  "v_max3_f32 v242, v240, v244, v248\n" \
  "v_max_f32 v242, v242, v252\n" \
  "v_mfma_f32_16x16x32_bf16 v[28:31], a[216:219], a[184:187], v[28:31]\n" \
  "ds_read_b128 a[208:211], v192 offset:12288\n" \
  "v_cmp_lt_f32 vcc, s59, v242\n" \
  "v_mfma_f32_16x16x32_bf16 v[28:31], a[220:223], a[188:191], v[28:31]\n" \
  "s_cbranch_vccnz TR_B0%=\n" \
  "RET_B0%=:\n" \
  "s_waitcnt lgkmcnt(5)\n" \
  "v_mfma_f32_16x16x32_bf16 a[0:3], v[128:131], v[80:83], a[0:3]\n" \
  "ds_read_b128 a[212:215], v193 offset:12288\n" \
  "v_add_f32 v112, v112, v240\n" \
  "v_mfma_f32_16x16x32_bf16 a[4:7], v[128:131], v[84:87], a[4:7]\n" \
  "v_add_f32 v113, v113, v244\n" \
  "v_add_f32 v114, v114, v248\n" \
  "v_mfma_f32_16x16x32_bf16 a[8:11], v[128:131], v[88:91], a[8:11]\n" \
  "ds_read_b128 a[216:219], v194 offset:12288\n" \
  "v_add_f32 v115, v115, v252\n" \
  "v_mfma_f32_16x16x32_bf16 a[12:15], v[128:131], v[92:95], a[12:15]\n" \
  "v_exp_f32 v0, v0\n" \
  "v_mfma_f32_16x16x32_bf16 a[16:19], v[132:135], v[80:83], a[16:19]\n" \
  "ds_read_b128 a[220:223], v195 offset:12288\n" \
  "v_exp_f32 v1, v1\n" \
  "v_mfma_f32_16x16x32_bf16 a[20:23], v[132:135], v[84:87], a[20:23]\n" \
  "s_waitcnt vmcnt(8)\n" \
  "s_barrier\n" \
  "v_mfma_f32_16x16x32_bf16 a[24:27], v[132:135], v[88:91], a[24:27]\n" \
  "v_exp_f32 v2, v2\n" \
  "v_mfma_f32_16x16x32_bf16 a[28:31], v[132:135], v[92:95], a[28:31]\n" \
  "ds_read_b64_tr_b16 v[128:129], v196 offset:0\n" \
  "v_cvt_pk_bf16_f32 v64, v0, v1\n" \
  "v_mfma_f32_16x16x32_bf16 a[32:35], v[136:139], v[80:83], a[32:35]\n" \
  "v_exp_f32 v3, v3\n" \
  "v_mfma_f32_16x16x32_bf16 a[36:39], v[136:139], v[84:87], a[36:39]\n" \
  "ds_read_b64_tr_b16 v[130:131], v196 offset:4096\n" \
  "v_cvt_pk_bf16_f32 v65, v2, v3\n" \
  "v_mfma_f32_16x16x32_bf16 a[40:43], v[136:139], v[88:91], a[40:43]\n" \
  "v_exp_f32 v16, v16\n" \
  "v_mfma_f32_16x16x32_bf16 a[44:47], v[136:139], v[92:95], a[44:47]\n" \
  "ds_read_b64_tr_b16 v[132:133], v197 offset:0\n" \
  "v_exp_f32 v17, v17\n" \
  "v_mfma_f32_16x16x32_bf16 a[48:51], v[140:143], v[80:83], a[48:51]\n" \
  "v_exp_f32 v18, v18\n" \
  "v_mfma_f32_16x16x32_bf16 a[52:55], v[140:143], v[84:87], a[52:55]\n" \
  "ds_read_b64_tr_b16 v[134:135], v197 offset:4096\n" \
  "v_cvt_pk_bf16_f32 v66, v16, v17\n" \
  "v_mfma_f32_16x16x32_bf16 a[56:59], v[140:143], v[88:91], a[56:59]\n" \
  "v_exp_f32 v19, v19\n" \
  "v_mfma_f32_16x16x32_bf16 a[60:63], v[140:143], v[92:95], a[60:63]\n" \
  "ds_read_b64_tr_b16 v[136:137], v196 offset:256\n" \
  "v_cvt_pk_bf16_f32 v67, v18, v19\n" \
  "v_mfma_f32_16x16x32_bf16 a[64:67], v[144:147], v[80:83], a[64:67]\n" \
  "v_exp_f32 v4, v4\n" \
  "v_mfma_f32_16x16x32_bf16 a[68:71], v[144:147], v[84:87], a[68:71]\n" \
  "ds_read_b64_tr_b16 v[138:139], v196 offset:4352\n" \
  "v_exp_f32 v5, v5\n" \
  "v_mfma_f32_16x16x32_bf16 v[240:243], v[124:127], v[64:67], 0\n" \
  "v_exp_f32 v6, v6\n" \
  "v_mfma_f32_16x16x32_bf16 a[72:75], v[144:147], v[88:91], a[72:75]\n" \
  "v_exp_f32 v7, v7\n" \
  "v_mfma_f32_16x16x32_bf16 a[76:79], v[144:147], v[92:95], a[76:79]\n" \
  "ds_read_b64_tr_b16 v[140:141], v197 offset:256\n" \
  "v_cvt_pk_bf16_f32 v68, v4, v5\n" \
  "v_mfma_f32_16x16x32_bf16 a[80:83], v[148:151], v[80:83], a[80:83]\n" \
  "v_exp_f32 v20, v20\n" \
  "v_mfma_f32_16x16x32_bf16 a[84:87], v[148:151], v[84:87], a[84:87]\n" \
  "ds_read_b64_tr_b16 v[142:143], v197 offset:4352\n" \
  "v_cvt_pk_bf16_f32 v69, v6, v7\n" \
  "v_mfma_f32_16x16x32_bf16 a[88:91], v[148:151], v[88:91], a[88:91]\n" \
  "v_exp_f32 v21, v21\n" \
  "v_mfma_f32_16x16x32_bf16 a[92:95], v[148:151], v[92:95], a[92:95]\n" \
  "ds_read_b64_tr_b16 v[144:145], v196 offset:512\n" \
  "v_cvt_pk_bf16_f32 v70, v20, v21\n" \
  "v_mfma_f32_16x16x32_bf16 a[96:99], v[152:155], v[80:83], a[96:99]\n" \
  "v_exp_f32 v22, v22\n" \
  "v_mfma_f32_16x16x32_bf16 a[100:103], v[152:155], v[84:87], a[100:103]\n" \
  "ds_read_b64_tr_b16 v[146:147], v196 offset:4608\n" \
  "v_exp_f32 v23, v23\n" \
  "v_mfma_f32_16x16x32_bf16 a[104:107], v[152:155], v[88:91], a[104:107]\n" \
  "v_exp_f32 v8, v8\n" \
  "v_mfma_f32_16x16x32_bf16 a[108:111], v[152:155], v[92:95], a[108:111]\n" \
  "ds_read_b64_tr_b16 v[148:149], v197 offset:512\n" \
  "v_cvt_pk_bf16_f32 v71, v22, v23\n" \
  "v_mfma_f32_16x16x32_bf16 a[112:115], v[156:159], v[80:83], a[112:115]\n" \
  "v_exp_f32 v9, v9\n" \
  "v_mfma_f32_16x16x32_bf16 a[116:119], v[156:159], v[84:87], a[116:119]\n" \
  "ds_read_b64_tr_b16 v[150:151], v197 offset:4608\n" \
  "v_cvt_pk_bf16_f32 v72, v8, v9\n" \
  "v_mfma_f32_16x16x32_bf16 v[244:247], v[124:127], v[68:71], 0\n" \
  "v_exp_f32 v10, v10\n" \
  "v_mfma_f32_16x16x32_bf16 a[120:123], v[156:159], v[88:91], a[120:123]\n" \
  "v_exp_f32 v11, v11\n" \
  "v_mfma_f32_16x16x32_bf16 a[124:127], v[156:159], v[92:95], a[124:127]\n" \
  "ds_read_b64_tr_b16 v[152:153], v196 offset:768\n" \
  "v_cvt_pk_bf16_f32 v73, v10, v11\n" \
  "s_waitcnt lgkmcnt(13)\n" \
  "v_mfma_f32_16x16x32_bf16 v[32:35], a[192:195], a[128:131], v[96:99]\n" \
  "v_exp_f32 v24, v24\n" \
  "v_mfma_f32_16x16x32_bf16 v[32:35], a[196:199], a[132:135], v[32:35]\n" \
  "ds_read_b64_tr_b16 v[154:155], v196 offset:4864\n" \
  "v_exp_f32 v25, v25\n" \
  "v_mfma_f32_16x16x32_bf16 v[32:35], a[200:203], a[136:139], v[32:35]\n" \
  "v_exp_f32 v26, v26\n" \
  "v_mfma_f32_16x16x32_bf16 v[32:35], a[204:207], a[140:143], v[32:35]\n" \
  "ds_read_b64_tr_b16 v[156:157], v197 offset:768\n" \
  "v_cvt_pk_bf16_f32 v74, v24, v25\n" \
  "v_mfma_f32_16x16x32_bf16 v[36:39], a[192:195], a[144:147], v[100:103]\n" \
  "v_exp_f32 v27, v27\n" \
  "s_add_u32 m0, s54, 49152\n" \
  "v_mfma_f32_16x16x32_bf16 v[36:39], a[196:199], a[148:151], v[36:39]\n" \
  "ds_read_b64_tr_b16 v[158:159], v197 offset:4864\n" \
  "v_cvt_pk_bf16_f32 v75, v26, v27\n" \
  "v_mfma_f32_16x16x32_bf16 v[36:39], a[200:203], a[152:155], v[36:39]\n" \
  "buffer_load_dwordx4 v200, s[40:43], s52 offen offset:0 lds\n" \
  "v_mfma_f32_16x16x32_bf16 v[36:39], a[204:207], a[156:159], v[36:39]\n" \
  "v_exp_f32 v12, v12\n" \
  "v_mfma_f32_16x16x32_bf16 v[248:251], v[124:127], v[72:75], 0\n" \
  "v_exp_f32 v13, v13\n" \
  "v_mfma_f32_16x16x32_bf16 v[40:43], a[192:195], a[160:163], v[104:107]\n" \
  "buffer_load_dwordx4 v201, s[40:43], s52 offen offset:1024 lds\n" \
  "v_mfma_f32_16x16x32_bf16 v[40:43], a[196:199], a[164:167], v[40:43]\n" \
  "v_exp_f32 v14, v14\n" \
  "v_mfma_f32_16x16x32_bf16 v[40:43], a[200:203], a[168:171], v[40:43]\n" \
  "buffer_load_dwordx4 v202, s[40:43], s52 offen offset:2048 lds\n" \
  "v_mfma_f32_16x16x32_bf16 v[40:43], a[204:207], a[172:175], v[40:43]\n" \
  "v_exp_f32 v15, v15\n" \
  "v_mfma_f32_16x16x32_bf16 v[44:47], a[192:195], a[176:179], v[108:111]\n" \
  "buffer_load_dwordx4 v203, s[40:43], s52 offen offset:3072 lds\n" \
  "v_mfma_f32_16x16x32_bf16 v[44:47], a[196:199], a[180:183], v[44:47]\n" \
  "v_exp_f32 v28, v28\n" \
  "v_mfma_f32_16x16x32_bf16 v[44:47], a[200:203], a[184:187], v[44:47]\n" \
  "ds_read_b128 a[192:195], v192 offset:16384\n" \
  "v_cvt_pk_bf16_f32 v76, v12, v13\n" \
  "v_mfma_f32_16x16x32_bf16 v[44:47], a[204:207], a[188:191], v[44:47]\n" \
  "v_exp_f32 v29, v29\n" \
  "v_mfma_f32_16x16x32_bf16 v[48:51], a[208:211], a[128:131], v[96:99]\n" \
  "ds_read_b128 a[196:199], v193 offset:16384\n" \
  "v_cvt_pk_bf16_f32 v77, v14, v15\n" \
  "v_mfma_f32_16x16x32_bf16 v[48:51], a[212:215], a[132:135], v[48:51]\n" \
  "v_exp_f32 v30, v30\n" \
  "v_mfma_f32_16x16x32_bf16 v[48:51], a[216:219], a[136:139], v[48:51]\n" \
  "ds_read_b128 a[200:203], v194 offset:16384\n" \
  "v_cvt_pk_bf16_f32 v78, v28, v29\n" \
  "v_mfma_f32_16x16x32_bf16 v[48:51], a[220:223], a[140:143], v[48:51]\n" \
  "v_exp_f32 v31, v31\n" \
  "v_mfma_f32_16x16x32_bf16 v[52:55], a[208:211], a[144:147], v[100:103]\n" \
  "ds_read_b128 a[204:207], v195 offset:16384\n" \
  "s_add_u32 m0, s55, 32768\n" \
  "v_mfma_f32_16x16x32_bf16 v[52:55], a[212:215], a[148:151], v[52:55]\n" \
  "v_cvt_pk_bf16_f32 v79, v30, v31\n" \
  "v_mfma_f32_16x16x32_bf16 v[52:55], a[216:219], a[152:155], v[52:55]\n" \
  "buffer_load_dwordx4 v204, s[44:47], s53 offen offset:0 lds\n" \
  "v_mfma_f32_16x16x32_bf16 v[52:55], a[220:223], a[156:159], v[52:55]\n" \
  "v_mfma_f32_16x16x32_bf16 v[252:255], v[124:127], v[76:79], 0\n" \
  "v_mfma_f32_16x16x32_bf16 v[56:59], a[208:211], a[160:163], v[104:107]\n" \
  "buffer_load_dwordx4 v205, s[44:47], s53 offen offset:1024 lds\n" \
  "v_mfma_f32_16x16x32_bf16 v[56:59], a[212:215], a[164:167], v[56:59]\n" \
  "v_mfma_f32_16x16x32_bf16 v[56:59], a[216:219], a[168:171], v[56:59]\n" \
  "buffer_load_dwordx4 v204, s[44:47], s53 offen offset:2048 lds\n" \
  "v_mfma_f32_16x16x32_bf16 v[56:59], a[220:223], a[172:175], v[56:59]\n" \
  "v_mfma_f32_16x16x32_bf16 v[60:63], a[208:211], a[176:179], v[108:111]\n" \
  "buffer_load_dwordx4 v205, s[44:47], s53 offen offset:3072 lds\n" \
  "v_mfma_f32_16x16x32_bf16 v[60:63], a[212:215], a[180:183], v[60:63]\n" \
  "v_max3_f32 v242, v240, v244, v248\n" \
  "v_max_f32 v242, v242, v252\n" \
  "v_mfma_f32_16x16x32_bf16 v[60:63], a[216:219], a[184:187], v[60:63]\n" \
  "ds_read_b128 a[208:211], v192 offset:20480\n" \
  "v_cmp_lt_f32 vcc, s59, v242\n" \
  "v_mfma_f32_16x16x32_bf16 v[60:63], a[220:223], a[188:191], v[60:63]\n" \
  "s_cbranch_vccnz TR_A0%=\n" \
  "RET_A0%=:\n" \
  "s_waitcnt lgkmcnt(5)\n" \
  "v_mfma_f32_16x16x32_bf16 a[0:3], v[128:131], v[64:67], a[0:3]\n" \
  "ds_read_b128 a[212:215], v193 offset:20480\n" \
  "v_add_f32 v112, v112, v240\n" \
  "v_mfma_f32_16x16x32_bf16 a[4:7], v[128:131], v[68:71], a[4:7]\n" \
  "v_add_f32 v113, v113, v244\n" \
  "v_add_f32 v114, v114, v248\n" \
  "v_mfma_f32_16x16x32_bf16 a[8:11], v[128:131], v[72:75], a[8:11]\n" \
  "ds_read_b128 a[216:219], v194 offset:20480\n" \
  "v_add_f32 v115, v115, v252\n" \
  "v_mfma_f32_16x16x32_bf16 a[12:15], v[128:131], v[76:79], a[12:15]\n" \
  "v_exp_f32 v32, v32\n" \
  "v_mfma_f32_16x16x32_bf16 a[16:19], v[132:135], v[64:67], a[16:19]\n" \
  "ds_read_b128 a[220:223], v195 offset:20480\n" \
  "v_exp_f32 v33, v33\n" \
  "v_mfma_f32_16x16x32_bf16 a[20:23], v[132:135], v[68:71], a[20:23]\n" \
  "v_exp_f32 v34, v34\n" \
  "v_mfma_f32_16x16x32_bf16 a[24:27], v[132:135], v[72:75], a[24:27]\n" \
  "s_add_u32 s52, s52, 0x4000\n" \
  "v_cvt_pk_bf16_f32 v80, v32, v33\n" \
  "v_mfma_f32_16x16x32_bf16 a[28:31], v[132:135], v[76:79], a[28:31]\n" \
  "ds_read_b64_tr_b16 v[128:129], v196 offset:8192\n" \
  "v_exp_f32 v35, v35\n" \
  "v_mfma_f32_16x16x32_bf16 a[32:35], v[136:139], v[64:67], a[32:35]\n" \
  "s_add_u32 s53, s53, 0x4000\n" \
  "v_cvt_pk_bf16_f32 v81, v34, v35\n" \
  "v_mfma_f32_16x16x32_bf16 a[36:39], v[136:139], v[68:71], a[36:39]\n" \
  "ds_read_b64_tr_b16 v[130:131], v196 offset:12288\n" \
  "v_exp_f32 v48, v48\n" \
  "v_mfma_f32_16x16x32_bf16 a[40:43], v[136:139], v[72:75], a[40:43]\n" \
  "v_exp_f32 v49, v49\n" \
  "v_mfma_f32_16x16x32_bf16 a[44:47], v[136:139], v[76:79], a[44:47]\n" \
  "ds_read_b64_tr_b16 v[132:133], v197 offset:8192\n" \
  "v_cvt_pk_bf16_f32 v82, v48, v49\n" \
  "v_mfma_f32_16x16x32_bf16 a[48:51], v[140:143], v[64:67], a[48:51]\n" \
  "v_exp_f32 v50, v50\n" \
  "v_mfma_f32_16x16x32_bf16 a[52:55], v[140:143], v[68:71], a[52:55]\n" \
  "ds_read_b64_tr_b16 v[134:135], v197 offset:12288\n" \
  "v_exp_f32 v51, v51\n" \
  "v_mfma_f32_16x16x32_bf16 a[56:59], v[140:143], v[72:75], a[56:59]\n" \
  "v_exp_f32 v36, v36\n" \
  "v_mfma_f32_16x16x32_bf16 a[60:63], v[140:143], v[76:79], a[60:63]\n" \
  "ds_read_b64_tr_b16 v[136:137], v196 offset:8448\n" \
  "v_cvt_pk_bf16_f32 v83, v50, v51\n" \
  "v_mfma_f32_16x16x32_bf16 a[64:67], v[144:147], v[64:67], a[64:67]\n" \
  "v_exp_f32 v37, v37\n" \
  "v_mfma_f32_16x16x32_bf16 a[68:71], v[144:147], v[68:71], a[68:71]\n" \
  "ds_read_b64_tr_b16 v[138:139], v196 offset:12544\n" \
  "v_cvt_pk_bf16_f32 v84, v36, v37\n" \
  "v_mfma_f32_16x16x32_bf16 v[240:243], v[124:127], v[80:83], 0\n" \
  "v_exp_f32 v38, v38\n" \
  "v_mfma_f32_16x16x32_bf16 a[72:75], v[144:147], v[72:75], a[72:75]\n" \
  "v_exp_f32 v39, v39\n" \
  "v_mfma_f32_16x16x32_bf16 a[76:79], v[144:147], v[76:79], a[76:79]\n" \
  "ds_read_b64_tr_b16 v[140:141], v197 offset:8448\n" \
  "v_cvt_pk_bf16_f32 v85, v38, v39\n" \
  "v_mfma_f32_16x16x32_bf16 a[80:83], v[148:151], v[64:67], a[80:83]\n" \
  "v_exp_f32 v52, v52\n" \
  "v_mfma_f32_16x16x32_bf16 a[84:87], v[148:151], v[68:71], a[84:87]\n" \
  "ds_read_b64_tr_b16 v[142:143], v197 offset:12544\n" \
  "v_exp_f32 v53, v53\n" \
  "v_mfma_f32_16x16x32_bf16 a[88:91], v[148:151], v[72:75], a[88:91]\n" \
  "v_exp_f32 v54, v54\n" \
  "v_mfma_f32_16x16x32_bf16 a[92:95], v[148:151], v[76:79], a[92:95]\n" \
  "ds_read_b64_tr_b16 v[144:145], v196 offset:8704\n" \
  "v_cvt_pk_bf16_f32 v86, v52, v53\n" \
  "v_mfma_f32_16x16x32_bf16 a[96:99], v[152:155], v[64:67], a[96:99]\n" \
  "v_exp_f32 v55, v55\n" \
  "v_mfma_f32_16x16x32_bf16 a[100:103], v[152:155], v[68:71], a[100:103]\n" \
  "ds_read_b64_tr_b16 v[146:147], v196 offset:12800\n" \
  "v_cvt_pk_bf16_f32 v87, v54, v55\n" \
  "v_mfma_f32_16x16x32_bf16 a[104:107], v[152:155], v[72:75], a[104:107]\n" \
  "v_exp_f32 v40, v40\n" \
  "v_mfma_f32_16x16x32_bf16 a[108:111], v[152:155], v[76:79], a[108:111]\n" \
  "ds_read_b64_tr_b16 v[148:149], v197 offset:8704\n" \
  "v_exp_f32 v41, v41\n" \
  "v_mfma_f32_16x16x32_bf16 v[244:247], v[124:127], v[84:87], 0\n" \
  "v_mfma_f32_16x16x32_bf16 a[112:115], v[156:159], v[64:67], a[112:115]\n" \
  "v_exp_f32 v42, v42\n" \
  "v_mfma_f32_16x16x32_bf16 a[116:119], v[156:159], v[68:71], a[116:119]\n" \
  "ds_read_b64_tr_b16 v[150:151], v197 offset:12800\n" \
  "v_cvt_pk_bf16_f32 v88, v40, v41\n" \
  "v_mfma_f32_16x16x32_bf16 a[120:123], v[156:159], v[72:75], a[120:123]\n" \
  "v_exp_f32 v43, v43\n" \
  "v_mfma_f32_16x16x32_bf16 a[124:127], v[156:159], v[76:79], a[124:127]\n" \
  "ds_read_b64_tr_b16 v[152:153], v196 offset:8960\n" \
  "v_cvt_pk_bf16_f32 v89, v42, v43\n" \
  "s_branch LOOP%=\n" \
  "TAIL%=:\n" \
  "v_exp_f32 v56, v56\n" \
  "s_nop 0\n" \
  "ds_read_b64_tr_b16 v[154:155], v196 offset:62208\n" \
  "v_exp_f32 v57, v57\n" \
  "s_nop 0\n" \
  "ds_read_b64_tr_b16 v[156:157], v197 offset:58112\n" \
  "v_cvt_pk_bf16_f32 v90, v56, v57\n" \
  "v_exp_f32 v58, v58\n" \
  "s_nop 0\n" \
  "ds_read_b64_tr_b16 v[158:159], v197 offset:62208\n" \
  "v_exp_f32 v59, v59\n" \
  "v_exp_f32 v44, v44\n" \
  "v_exp_f32 v45, v45\n" \
  "v_exp_f32 v46, v46\n" \
  "v_exp_f32 v47, v47\n" \
  "v_exp_f32 v60, v60\n" \
  "v_exp_f32 v61, v61\n" \
  "v_exp_f32 v62, v62\n" \
  "s_nop 0\n" \
  "v_cvt_pk_bf16_f32 v91, v58, v59\n" \
  "v_exp_f32 v63, v63\n" \
  "s_nop 0\n" \
  "v_cvt_pk_bf16_f32 v92, v44, v45\n" \
  "s_nop 1\n" \
  "v_mfma_f32_16x16x32_bf16 v[248:251], v[124:127], v[88:91], 0\n" \
  "v_cvt_pk_bf16_f32 v93, v46, v47\n" \
  "v_cvt_pk_bf16_f32 v94, v60, v61\n" \
  "v_cvt_pk_bf16_f32 v95, v62, v63\n" \
  "s_nop 1\n" \
  "v_mfma_f32_16x16x32_bf16 v[252:255], v[124:127], v[92:95], 0\n" \
  "s_nop 7\n" \
  "s_nop 3\n" \
  "v_max3_f32 v242, v240, v244, v248\n" \
  "v_max_f32 v242, v242, v252\n" \
  "v_cmp_lt_f32 vcc, s59, v242\n" \
  "s_nop 4\n" \
  "s_cbranch_vccnz TR_T%=\n" \
  "RET_T%=:\n" \
  "s_nop 1\n" \
  "s_waitcnt lgkmcnt(0)\n" \
  "v_mfma_f32_16x16x32_bf16 a[0:3], v[128:131], v[80:83], a[0:3]\n" \
  "v_add_f32 v112, v112, v240\n" \
  "v_mfma_f32_16x16x32_bf16 a[4:7], v[128:131], v[84:87], a[4:7]\n" \
  "v_add_f32 v113, v113, v244\n" \
  "v_add_f32 v114, v114, v248\n" \
  "v_mfma_f32_16x16x32_bf16 a[8:11], v[128:131], v[88:91], a[8:11]\n" \
  "v_add_f32 v115, v115, v252\n" \
  "v_mfma_f32_16x16x32_bf16 a[12:15], v[128:131], v[92:95], a[12:15]\n" \
  "v_mfma_f32_16x16x32_bf16 a[16:19], v[132:135], v[80:83], a[16:19]\n" \
  "v_mfma_f32_16x16x32_bf16 a[20:23], v[132:135], v[84:87], a[20:23]\n" \
  "v_mfma_f32_16x16x32_bf16 a[24:27], v[132:135], v[88:91], a[24:27]\n" \
  "v_mfma_f32_16x16x32_bf16 a[28:31], v[132:135], v[92:95], a[28:31]\n" \
  "v_mfma_f32_16x16x32_bf16 a[32:35], v[136:139], v[80:83], a[32:35]\n" \
  "v_mfma_f32_16x16x32_bf16 a[36:39], v[136:139], v[84:87], a[36:39]\n" \
  "v_mfma_f32_16x16x32_bf16 a[40:43], v[136:139], v[88:91], a[40:43]\n" \
  "v_mfma_f32_16x16x32_bf16 a[44:47], v[136:139], v[92:95], a[44:47]\n" \
  "v_mfma_f32_16x16x32_bf16 a[48:51], v[140:143], v[80:83], a[48:51]\n" \
  "v_mfma_f32_16x16x32_bf16 a[52:55], v[140:143], v[84:87], a[52:55]\n" \
  "v_mfma_f32_16x16x32_bf16 a[56:59], v[140:143], v[88:91], a[56:59]\n" \
  "v_mfma_f32_16x16x32_bf16 a[60:63], v[140:143], v[92:95], a[60:63]\n" \
  "v_mfma_f32_16x16x32_bf16 a[64:67], v[144:147], v[80:83], a[64:67]\n" \
  "v_mfma_f32_16x16x32_bf16 a[68:71], v[144:147], v[84:87], a[68:71]\n" \
  "v_mfma_f32_16x16x32_bf16 a[72:75], v[144:147], v[88:91], a[72:75]\n" \
  "v_mfma_f32_16x16x32_bf16 a[76:79], v[144:147], v[92:95], a[76:79]\n" \
  "v_mfma_f32_16x16x32_bf16 a[80:83], v[148:151], v[80:83], a[80:83]\n" \
  "v_mfma_f32_16x16x32_bf16 a[84:87], v[148:151], v[84:87], a[84:87]\n" \
  "v_mfma_f32_16x16x32_bf16 a[88:91], v[148:151], v[88:91], a[88:91]\n" \
  "v_mfma_f32_16x16x32_bf16 a[92:95], v[148:151], v[92:95], a[92:95]\n" \
  "v_mfma_f32_16x16x32_bf16 a[96:99], v[152:155], v[80:83], a[96:99]\n" \
  "v_mfma_f32_16x16x32_bf16 a[100:103], v[152:155], v[84:87], a[100:103]\n" \
  "v_mfma_f32_16x16x32_bf16 a[104:107], v[152:155], v[88:91], a[104:107]\n" \
  "v_mfma_f32_16x16x32_bf16 a[108:111], v[152:155], v[92:95], a[108:111]\n" \
  "v_mfma_f32_16x16x32_bf16 a[112:115], v[156:159], v[80:83], a[112:115]\n" \
  "v_mfma_f32_16x16x32_bf16 a[116:119], v[156:159], v[84:87], a[116:119]\n" \
  "v_mfma_f32_16x16x32_bf16 a[120:123], v[156:159], v[88:91], a[120:123]\n" \
  "v_mfma_f32_16x16x32_bf16 a[124:127], v[156:159], v[92:95], a[124:127]\n" \
  "s_nop 7\n" \
  "s_nop 7\n" \
  "v_mbcnt_lo_u32_b32 v212, -1, 0\n" \
  "v_mbcnt_hi_u32_b32 v212, -1, v212\n" \
  "v_and_b32 v213, 15, v212\n" \
  "v_lshrrev_b32 v214, 4, v212\n" \
  "v_lshlrev_b32 v215, 4, v213\n" \
  "v_lshrrev_b32 v216, 1, v214\n" \
  "v_lshl_add_u32 v215, v216, 8, v215\n" \
  "v_and_b32 v216, 1, v214\n" \
  "v_lshl_add_u32 v215, v216, 9, v215\n" \
  "v_rcp_f32 v96, v112\n" \
  "v_rcp_f32 v98, v113\n" \
  "v_rcp_f32 v100, v114\n" \
  "v_rcp_f32 v102, v115\n" \
  "s_nop 0\n" \
  "v_accvgpr_read_b32 v0, a0\n" \
  "v_accvgpr_read_b32 v4, a16\n" \
  "v_accvgpr_read_b32 v1, a1\n" \
  "v_accvgpr_read_b32 v5, a17\n" \
  "v_accvgpr_read_b32 v2, a2\n" \
  "v_accvgpr_read_b32 v6, a18\n" \
  "v_accvgpr_read_b32 v3, a3\n" \
  "v_accvgpr_read_b32 v7, a19\n" \
  "v_pk_mul_f32 v[0:1], v[0:1], v[96:97] op_sel_hi:[1,0]\n" \
  "v_pk_mul_f32 v[2:3], v[2:3], v[96:97] op_sel_hi:[1,0]\n" \
  "v_pk_mul_f32 v[4:5], v[4:5], v[96:97] op_sel_hi:[1,0]\n" \
  "v_pk_mul_f32 v[6:7], v[6:7], v[96:97] op_sel_hi:[1,0]\n" \
  "v_cvt_pk_bf16_f32 v64, v0, v1\n" \
  "v_cvt_pk_bf16_f32 v65, v2, v3\n" \
  "v_cvt_pk_bf16_f32 v66, v4, v5\n" \
  "v_cvt_pk_bf16_f32 v67, v6, v7\n" \
  "s_nop 1\n" \
  "v_permlane16_swap_b32 v64, v66\n" \
  "v_permlane16_swap_b32 v65, v67\n" \
  "s_nop 1\n" \
  "buffer_store_dwordx4 v[64:67], v215, s[48:51], s67 offen offset:0\n" \
  "v_accvgpr_read_b32 v8, a32\n" \
  "v_accvgpr_read_b32 v12, a48\n" \
  "v_accvgpr_read_b32 v9, a33\n" \
  "v_accvgpr_read_b32 v13, a49\n" \
  "v_accvgpr_read_b32 v10, a34\n" \
  "v_accvgpr_read_b32 v14, a50\n" \
  "v_accvgpr_read_b32 v11, a35\n" \
  "v_accvgpr_read_b32 v15, a51\n" \
  "v_pk_mul_f32 v[8:9], v[8:9], v[96:97] op_sel_hi:[1,0]\n" \
  "v_pk_mul_f32 v[10:11], v[10:11], v[96:97] op_sel_hi:[1,0]\n" \
  "v_pk_mul_f32 v[12:13], v[12:13], v[96:97] op_sel_hi:[1,0]\n" \
  "v_pk_mul_f32 v[14:15], v[14:15], v[96:97] op_sel_hi:[1,0]\n" \
  "v_cvt_pk_bf16_f32 v68, v8, v9\n" \
  "v_cvt_pk_bf16_f32 v69, v10, v11\n" \
  "v_cvt_pk_bf16_f32 v70, v12, v13\n" \
  "v_cvt_pk_bf16_f32 v71, v14, v15\n" \
  "s_nop 1\n" \
  "v_permlane16_swap_b32 v68, v70\n" \
  "v_permlane16_swap_b32 v69, v71\n" \
  "s_nop 1\n" \
  "buffer_store_dwordx4 v[68:71], v215, s[48:51], s67 offen offset:1024\n" \
  "v_accvgpr_read_b32 v16, a64\n" \
  "v_accvgpr_read_b32 v20, a80\n" \
  "v_accvgpr_read_b32 v17, a65\n" \
  "v_accvgpr_read_b32 v21, a81\n" \
  "v_accvgpr_read_b32 v18, a66\n" \
  "v_accvgpr_read_b32 v22, a82\n" \
  "v_accvgpr_read_b32 v19, a67\n" \
  "v_accvgpr_read_b32 v23, a83\n" \
  "v_pk_mul_f32 v[16:17], v[16:17], v[96:97] op_sel_hi:[1,0]\n" \
  "v_pk_mul_f32 v[18:19], v[18:19], v[96:97] op_sel_hi:[1,0]\n" \
  "v_pk_mul_f32 v[20:21], v[20:21], v[96:97] op_sel_hi:[1,0]\n" \
  "v_pk_mul_f32 v[22:23], v[22:23], v[96:97] op_sel_hi:[1,0]\n" \
  "v_cvt_pk_bf16_f32 v72, v16, v17\n" \
  "v_cvt_pk_bf16_f32 v73, v18, v19\n" \
  "v_cvt_pk_bf16_f32 v74, v20, v21\n" \
  "v_cvt_pk_bf16_f32 v75, v22, v23\n" \
  "s_nop 1\n" \
  "v_permlane16_swap_b32 v72, v74\n" \
  "v_permlane16_swap_b32 v73, v75\n" \
  "s_nop 1\n" \
  "buffer_store_dwordx4 v[72:75], v215, s[48:51], s67 offen offset:2048\n" \
  "v_accvgpr_read_b32 v24, a96\n" \
  "v_accvgpr_read_b32 v28, a112\n" \
  "v_accvgpr_read_b32 v25, a97\n" \
  "v_accvgpr_read_b32 v29, a113\n" \
  "v_accvgpr_read_b32 v26, a98\n" \
  "v_accvgpr_read_b32 v30, a114\n" \
  "v_accvgpr_read_b32 v27, a99\n" \
  "v_accvgpr_read_b32 v31, a115\n" \
  "v_pk_mul_f32 v[24:25], v[24:25], v[96:97] op_sel_hi:[1,0]\n" \
  "v_pk_mul_f32 v[26:27], v[26:27], v[96:97] op_sel_hi:[1,0]\n" \
  "v_pk_mul_f32 v[28:29], v[28:29], v[96:97] op_sel_hi:[1,0]\n" \
  "v_pk_mul_f32 v[30:31], v[30:31], v[96:97] op_sel_hi:[1,0]\n" \
  "v_cvt_pk_bf16_f32 v76, v24, v25\n" \
  "v_cvt_pk_bf16_f32 v77, v26, v27\n" \
  "v_cvt_pk_bf16_f32 v78, v28, v29\n" \
  "v_cvt_pk_bf16_f32 v79, v30, v31\n" \
  "s_nop 1\n" \
  "v_permlane16_swap_b32 v76, v78\n" \
  "v_permlane16_swap_b32 v77, v79\n" \
  "s_nop 1\n" \
  "buffer_store_dwordx4 v[76:79], v215, s[48:51], s67 offen offset:3072\n" \
  "s_add_u32 s67, s67, 131072\n" \
  "v_accvgpr_read_b32 v32, a4\n" \
  "v_accvgpr_read_b32 v36, a20\n" \
  "v_accvgpr_read_b32 v33, a5\n" \
  "v_accvgpr_read_b32 v37, a21\n" \
  "v_accvgpr_read_b32 v34, a6\n" \
  "v_accvgpr_read_b32 v38, a22\n" \
  "v_accvgpr_read_b32 v35, a7\n" \
  "v_accvgpr_read_b32 v39, a23\n" \
  "v_pk_mul_f32 v[32:33], v[32:33], v[98:99] op_sel_hi:[1,0]\n" \
  "v_pk_mul_f32 v[34:35], v[34:35], v[98:99] op_sel_hi:[1,0]\n" \
  "v_pk_mul_f32 v[36:37], v[36:37], v[98:99] op_sel_hi:[1,0]\n" \
  "v_pk_mul_f32 v[38:39], v[38:39], v[98:99] op_sel_hi:[1,0]\n" \
  "v_cvt_pk_bf16_f32 v80, v32, v33\n" \
  "v_cvt_pk_bf16_f32 v81, v34, v35\n" \
  "v_cvt_pk_bf16_f32 v82, v36, v37\n" \
  "v_cvt_pk_bf16_f32 v83, v38, v39\n" \
  "s_nop 1\n" \
  "v_permlane16_swap_b32 v80, v82\n" \
  "v_permlane16_swap_b32 v81, v83\n" \
  "s_nop 1\n" \
  "buffer_store_dwordx4 v[80:83], v215, s[48:51], s67 offen offset:0\n" \
  "v_accvgpr_read_b32 v40, a36\n" \
  "v_accvgpr_read_b32 v44, a52\n" \
  "v_accvgpr_read_b32 v41, a37\n" \
  "v_accvgpr_read_b32 v45, a53\n" \
  "v_accvgpr_read_b32 v42, a38\n" \
  "v_accvgpr_read_b32 v46, a54\n" \
  "v_accvgpr_read_b32 v43, a39\n" \
  "v_accvgpr_read_b32 v47, a55\n" \
  "v_pk_mul_f32 v[40:41], v[40:41], v[98:99] op_sel_hi:[1,0]\n" \
  "v_pk_mul_f32 v[42:43], v[42:43], v[98:99] op_sel_hi:[1,0]\n" \
  "v_pk_mul_f32 v[44:45], v[44:45], v[98:99] op_sel_hi:[1,0]\n" \
  "v_pk_mul_f32 v[46:47], v[46:47], v[98:99] op_sel_hi:[1,0]\n" \
  "v_cvt_pk_bf16_f32 v84, v40, v41\n" \
  "v_cvt_pk_bf16_f32 v85, v42, v43\n" \
  "v_cvt_pk_bf16_f32 v86, v44, v45\n" \
  "v_cvt_pk_bf16_f32 v87, v46, v47\n" \
  "s_nop 1\n" \
  "v_permlane16_swap_b32 v84, v86\n" \
  "v_permlane16_swap_b32 v85, v87\n" \
  "s_nop 1\n" \
  "buffer_store_dwordx4 v[84:87], v215, s[48:51], s67 offen offset:1024\n" \
  "v_accvgpr_read_b32 v48, a68\n" \
  "v_accvgpr_read_b32 v52, a84\n" \
  "v_accvgpr_read_b32 v49, a69\n" \
  "v_accvgpr_read_b32 v53, a85\n" \
  "v_accvgpr_read_b32 v50, a70\n" \
  "v_accvgpr_read_b32 v54, a86\n" \
  "v_accvgpr_read_b32 v51, a71\n" \
  "v_accvgpr_read_b32 v55, a87\n" \
  "v_pk_mul_f32 v[48:49], v[48:49], v[98:99] op_sel_hi:[1,0]\n" \
  "v_pk_mul_f32 v[50:51], v[50:51], v[98:99] op_sel_hi:[1,0]\n" \
  "v_pk_mul_f32 v[52:53], v[52:53], v[98:99] op_sel_hi:[1,0]\n" \
  "v_pk_mul_f32 v[54:55], v[54:55], v[98:99] op_sel_hi:[1,0]\n" \
  "v_cvt_pk_bf16_f32 v88, v48, v49\n" \
  "v_cvt_pk_bf16_f32 v89, v50, v51\n" \
  "v_cvt_pk_bf16_f32 v90, v52, v53\n" \
  "v_cvt_pk_bf16_f32 v91, v54, v55\n" \
  "s_nop 1\n" \
  "v_permlane16_swap_b32 v88, v90\n" \
  "v_permlane16_swap_b32 v89, v91\n" \
  "s_nop 1\n" \
  "buffer_store_dwordx4 v[88:91], v215, s[48:51], s67 offen offset:2048\n" \
  "v_accvgpr_read_b32 v56, a100\n" \
  "v_accvgpr_read_b32 v60, a116\n" \
  "v_accvgpr_read_b32 v57, a101\n" \
  "v_accvgpr_read_b32 v61, a117\n" \
  "v_accvgpr_read_b32 v58, a102\n" \
  "v_accvgpr_read_b32 v62, a118\n" \
  "v_accvgpr_read_b32 v59, a103\n" \
  "v_accvgpr_read_b32 v63, a119\n" \
  "v_pk_mul_f32 v[56:57], v[56:57], v[98:99] op_sel_hi:[1,0]\n" \
  "v_pk_mul_f32 v[58:59], v[58:59], v[98:99] op_sel_hi:[1,0]\n" \
  "v_pk_mul_f32 v[60:61], v[60:61], v[98:99] op_sel_hi:[1,0]\n" \
  "v_pk_mul_f32 v[62:63], v[62:63], v[98:99] op_sel_hi:[1,0]\n" \
  "v_cvt_pk_bf16_f32 v92, v56, v57\n" \
  "v_cvt_pk_bf16_f32 v93, v58, v59\n" \
  "v_cvt_pk_bf16_f32 v94, v60, v61\n" \
  "v_cvt_pk_bf16_f32 v95, v62, v63\n" \
  "s_nop 1\n" \
  "v_permlane16_swap_b32 v92, v94\n" \
  "v_permlane16_swap_b32 v93, v95\n" \
  "s_nop 1\n" \
  "buffer_store_dwordx4 v[92:95], v215, s[48:51], s67 offen offset:3072\n" \
  "s_add_u32 s67, s67, 131072\n" \
  "v_accvgpr_read_b32 v0, a8\n" \
  "v_accvgpr_read_b32 v4, a24\n" \
  "v_accvgpr_read_b32 v1, a9\n" \
  "v_accvgpr_read_b32 v5, a25\n" \
  "v_accvgpr_read_b32 v2, a10\n" \
  "v_accvgpr_read_b32 v6, a26\n" \
  "v_accvgpr_read_b32 v3, a11\n" \
  "v_accvgpr_read_b32 v7, a27\n" \
  "v_pk_mul_f32 v[0:1], v[0:1], v[100:101] op_sel_hi:[1,0]\n" \
  "v_pk_mul_f32 v[2:3], v[2:3], v[100:101] op_sel_hi:[1,0]\n" \
  "v_pk_mul_f32 v[4:5], v[4:5], v[100:101] op_sel_hi:[1,0]\n" \
  "v_pk_mul_f32 v[6:7], v[6:7], v[100:101] op_sel_hi:[1,0]\n" \
  "v_cvt_pk_bf16_f32 v64, v0, v1\n" \
  "v_cvt_pk_bf16_f32 v65, v2, v3\n" \
  "v_cvt_pk_bf16_f32 v66, v4, v5\n" \
  "v_cvt_pk_bf16_f32 v67, v6, v7\n" \
  "s_nop 1\n" \
  "v_permlane16_swap_b32 v64, v66\n" \
  "v_permlane16_swap_b32 v65, v67\n" \
  "s_nop 1\n" \
  "buffer_store_dwordx4 v[64:67], v215, s[48:51], s67 offen offset:0\n" \
  "v_accvgpr_read_b32 v8, a40\n" \
  "v_accvgpr_read_b32 v12, a56\n" \
  "v_accvgpr_read_b32 v9, a41\n" \
  "v_accvgpr_read_b32 v13, a57\n" \
  "v_accvgpr_read_b32 v10, a42\n" \
  "v_accvgpr_read_b32 v14, a58\n" \
  "v_accvgpr_read_b32 v11, a43\n" \
  "v_accvgpr_read_b32 v15, a59\n" \
  "v_pk_mul_f32 v[8:9], v[8:9], v[100:101] op_sel_hi:[1,0]\n" \
  "v_pk_mul_f32 v[10:11], v[10:11], v[100:101] op_sel_hi:[1,0]\n" \
  "v_pk_mul_f32 v[12:13], v[12:13], v[100:101] op_sel_hi:[1,0]\n" \
  "v_pk_mul_f32 v[14:15], v[14:15], v[100:101] op_sel_hi:[1,0]\n" \
  "v_cvt_pk_bf16_f32 v68, v8, v9\n" \
  "v_cvt_pk_bf16_f32 v69, v10, v11\n" \
  "v_cvt_pk_bf16_f32 v70, v12, v13\n" \
  "v_cvt_pk_bf16_f32 v71, v14, v15\n" \
  "s_nop 1\n" \
  "v_permlane16_swap_b32 v68, v70\n" \
  "v_permlane16_swap_b32 v69, v71\n" \
  "s_nop 1\n" \
  "buffer_store_dwordx4 v[68:71], v215, s[48:51], s67 offen offset:1024\n" \
  "v_accvgpr_read_b32 v16, a72\n" \
  "v_accvgpr_read_b32 v20, a88\n" \
  "v_accvgpr_read_b32 v17, a73\n" \
  "v_accvgpr_read_b32 v21, a89\n" \
  "v_accvgpr_read_b32 v18, a74\n" \
  "v_accvgpr_read_b32 v22, a90\n" \
  "v_accvgpr_read_b32 v19, a75\n" \
  "v_accvgpr_read_b32 v23, a91\n" \
  "v_pk_mul_f32 v[16:17], v[16:17], v[100:101] op_sel_hi:[1,0]\n" \
  "v_pk_mul_f32 v[18:19], v[18:19], v[100:101] op_sel_hi:[1,0]\n" \
  "v_pk_mul_f32 v[20:21], v[20:21], v[100:101] op_sel_hi:[1,0]\n" \
  "v_pk_mul_f32 v[22:23], v[22:23], v[100:101] op_sel_hi:[1,0]\n" \
  "v_cvt_pk_bf16_f32 v72, v16, v17\n" \
  "v_cvt_pk_bf16_f32 v73, v18, v19\n" \
  "v_cvt_pk_bf16_f32 v74, v20, v21\n" \
  "v_cvt_pk_bf16_f32 v75, v22, v23\n" \
  "s_nop 1\n" \
  "v_permlane16_swap_b32 v72, v74\n" \
  "v_permlane16_swap_b32 v73, v75\n" \
  "s_nop 1\n" \
  "buffer_store_dwordx4 v[72:75], v215, s[48:51], s67 offen offset:2048\n" \
  "v_accvgpr_read_b32 v24, a104\n" \
  "v_accvgpr_read_b32 v28, a120\n" \
  "v_accvgpr_read_b32 v25, a105\n" \
  "v_accvgpr_read_b32 v29, a121\n" \
  "v_accvgpr_read_b32 v26, a106\n" \
  "v_accvgpr_read_b32 v30, a122\n" \
  "v_accvgpr_read_b32 v27, a107\n" \
  "v_accvgpr_read_b32 v31, a123\n" \
  "v_pk_mul_f32 v[24:25], v[24:25], v[100:101] op_sel_hi:[1,0]\n" \
  "v_pk_mul_f32 v[26:27], v[26:27], v[100:101] op_sel_hi:[1,0]\n" \
  "v_pk_mul_f32 v[28:29], v[28:29], v[100:101] op_sel_hi:[1,0]\n" \
  "v_pk_mul_f32 v[30:31], v[30:31], v[100:101] op_sel_hi:[1,0]\n" \
  "v_cvt_pk_bf16_f32 v76, v24, v25\n" \
  "v_cvt_pk_bf16_f32 v77, v26, v27\n" \
  "v_cvt_pk_bf16_f32 v78, v28, v29\n" \
  "v_cvt_pk_bf16_f32 v79, v30, v31\n" \
  "s_nop 1\n" \
  "v_permlane16_swap_b32 v76, v78\n" \
  "v_permlane16_swap_b32 v77, v79\n" \
  "s_nop 1\n" \
  "buffer_store_dwordx4 v[76:79], v215, s[48:51], s67 offen offset:3072\n" \
  "s_add_u32 s67, s67, 131072\n" \
  "v_accvgpr_read_b32 v32, a12\n" \
  "v_accvgpr_read_b32 v36, a28\n" \
  "v_accvgpr_read_b32 v33, a13\n" \
  "v_accvgpr_read_b32 v37, a29\n" \
  "v_accvgpr_read_b32 v34, a14\n" \
  "v_accvgpr_read_b32 v38, a30\n" \
  "v_accvgpr_read_b32 v35, a15\n" \
  "v_accvgpr_read_b32 v39, a31\n" \
  "v_pk_mul_f32 v[32:33], v[32:33], v[102:103] op_sel_hi:[1,0]\n" \
  "v_pk_mul_f32 v[34:35], v[34:35], v[102:103] op_sel_hi:[1,0]\n" \
  "v_pk_mul_f32 v[36:37], v[36:37], v[102:103] op_sel_hi:[1,0]\n" \
  "v_pk_mul_f32 v[38:39], v[38:39], v[102:103] op_sel_hi:[1,0]\n" \
  "v_cvt_pk_bf16_f32 v80, v32, v33\n" \
  "v_cvt_pk_bf16_f32 v81, v34, v35\n" \
  "v_cvt_pk_bf16_f32 v82, v36, v37\n" \
  "v_cvt_pk_bf16_f32 v83, v38, v39\n" \
  "s_nop 1\n" \
  "v_permlane16_swap_b32 v80, v82\n" \
  "v_permlane16_swap_b32 v81, v83\n" \
  "s_nop 1\n" \
  "buffer_store_dwordx4 v[80:83], v215, s[48:51], s67 offen offset:0\n" \
  "v_accvgpr_read_b32 v40, a44\n" \
  "v_accvgpr_read_b32 v44, a60\n" \
  "v_accvgpr_read_b32 v41, a45\n" \
  "v_accvgpr_read_b32 v45, a61\n" \
  "v_accvgpr_read_b32 v42, a46\n" \
  "v_accvgpr_read_b32 v46, a62\n" \
  "v_accvgpr_read_b32 v43, a47\n" \
  "v_accvgpr_read_b32 v47, a63\n" \
  "v_pk_mul_f32 v[40:41], v[40:41], v[102:103] op_sel_hi:[1,0]\n" \
  "v_pk_mul_f32 v[42:43], v[42:43], v[102:103] op_sel_hi:[1,0]\n" \
  "v_pk_mul_f32 v[44:45], v[44:45], v[102:103] op_sel_hi:[1,0]\n" \
  "v_pk_mul_f32 v[46:47], v[46:47], v[102:103] op_sel_hi:[1,0]\n" \
  "v_cvt_pk_bf16_f32 v84, v40, v41\n" \
  "v_cvt_pk_bf16_f32 v85, v42, v43\n" \
  "v_cvt_pk_bf16_f32 v86, v44, v45\n" \
  "v_cvt_pk_bf16_f32 v87, v46, v47\n" \
  "s_nop 1\n" \
  "v_permlane16_swap_b32 v84, v86\n" \
  "v_permlane16_swap_b32 v85, v87\n" \
  "s_nop 1\n" \
  "buffer_store_dwordx4 v[84:87], v215, s[48:51], s67 offen offset:1024\n" \
  "v_accvgpr_read_b32 v48, a76\n" \
  "v_accvgpr_read_b32 v52, a92\n" \
  "v_accvgpr_read_b32 v49, a77\n" \
  "v_accvgpr_read_b32 v53, a93\n" \
  "v_accvgpr_read_b32 v50, a78\n" \
  "v_accvgpr_read_b32 v54, a94\n" \
  "v_accvgpr_read_b32 v51, a79\n" \
  "v_accvgpr_read_b32 v55, a95\n" \
  "v_pk_mul_f32 v[48:49], v[48:49], v[102:103] op_sel_hi:[1,0]\n" \
  "v_pk_mul_f32 v[50:51], v[50:51], v[102:103] op_sel_hi:[1,0]\n" \
  "v_pk_mul_f32 v[52:53], v[52:53], v[102:103] op_sel_hi:[1,0]\n" \
  "v_pk_mul_f32 v[54:55], v[54:55], v[102:103] op_sel_hi:[1,0]\n" \
  "v_cvt_pk_bf16_f32 v88, v48, v49\n" \
  "v_cvt_pk_bf16_f32 v89, v50, v51\n" \
  "v_cvt_pk_bf16_f32 v90, v52, v53\n" \
  "v_cvt_pk_bf16_f32 v91, v54, v55\n" \
  "s_nop 1\n" \
  "v_permlane16_swap_b32 v88, v90\n" \
  "v_permlane16_swap_b32 v89, v91\n" \
  "s_nop 1\n" \
  "buffer_store_dwordx4 v[88:91], v215, s[48:51], s67 offen offset:2048\n" \
  "v_accvgpr_read_b32 v56, a108\n" \
  "v_accvgpr_read_b32 v60, a124\n" \
  "v_accvgpr_read_b32 v57, a109\n" \
  "v_accvgpr_read_b32 v61, a125\n" \
  "v_accvgpr_read_b32 v58, a110\n" \
  "v_accvgpr_read_b32 v62, a126\n" \
  "v_accvgpr_read_b32 v59, a111\n" \
  "v_accvgpr_read_b32 v63, a127\n" \
  "v_pk_mul_f32 v[56:57], v[56:57], v[102:103] op_sel_hi:[1,0]\n" \
  "v_pk_mul_f32 v[58:59], v[58:59], v[102:103] op_sel_hi:[1,0]\n" \
  "v_pk_mul_f32 v[60:61], v[60:61], v[102:103] op_sel_hi:[1,0]\n" \
  "v_pk_mul_f32 v[62:63], v[62:63], v[102:103] op_sel_hi:[1,0]\n" \
  "v_cvt_pk_bf16_f32 v92, v56, v57\n" \
  "v_cvt_pk_bf16_f32 v93, v58, v59\n" \
  "v_cvt_pk_bf16_f32 v94, v60, v61\n" \
  "v_cvt_pk_bf16_f32 v95, v62, v63\n" \
  "s_nop 1\n" \
  "v_permlane16_swap_b32 v92, v94\n" \
  "v_permlane16_swap_b32 v93, v95\n" \
  "s_nop 1\n" \
  "buffer_store_dwordx4 v[92:95], v215, s[48:51], s67 offen offset:3072\n" \
  "s_branch END%=\n" \
  "TR_A0%=:\n" \
  "s_mov_b32 s62, 0\n" \
  "s_mov_b32 s69, 16384\n" \
  "s_getpc_b64 s[60:61]\n" \
  "s_branch FB_A%=\n" \
  "s_branch RET_A0%=\n" \
  "TR_B0%=:\n" \
  "s_mov_b32 s62, 49152\n" \
  "s_mov_b32 s69, 0\n" \
  "s_getpc_b64 s[60:61]\n" \
  "s_branch FB_B%=\n" \
  "s_branch RET_B0%=\n" \
  "TR_A1%=:\n" \
  "s_mov_b32 s62, 16384\n" \
  "s_mov_b32 s69, 32768\n" \
  "s_getpc_b64 s[60:61]\n" \
  "s_branch FB_A%=\n" \
  "s_branch RET_A1%=\n" \
  "TR_B1%=:\n" \
  "s_mov_b32 s62, 0\n" \
  "s_mov_b32 s69, 16384\n" \
  "s_getpc_b64 s[60:61]\n" \
  "s_branch FB_B%=\n" \
  "s_branch RET_B1%=\n" \
  "TR_A2%=:\n" \
  "s_mov_b32 s62, 32768\n" \
  "s_mov_b32 s69, 49152\n" \
  "s_getpc_b64 s[60:61]\n" \
  "s_branch FB_A%=\n" \
  "s_branch RET_A2%=\n" \
  "TR_B2%=:\n" \
  "s_mov_b32 s62, 16384\n" \
  "s_mov_b32 s69, 32768\n" \
  "s_getpc_b64 s[60:61]\n" \
  "s_branch FB_B%=\n" \
  "s_branch RET_B2%=\n" \
  "TR_A3%=:\n" \
  "s_mov_b32 s62, 49152\n" \
  "s_mov_b32 s69, 0\n" \
  "s_getpc_b64 s[60:61]\n" \
  "s_branch FB_A%=\n" \
  "s_branch RET_A3%=\n" \
  "TR_B3%=:\n" \
  "s_mov_b32 s62, 32768\n" \
  "s_mov_b32 s69, 49152\n" \
  "s_getpc_b64 s[60:61]\n" \
  "s_branch FB_B%=\n" \
  "s_branch RET_B3%=\n" \
  "TR_T%=:\n" \
  "s_mov_b32 s62, 49152\n" \
  "s_mov_b32 s69, 0\n" \
  "s_getpc_b64 s[60:61]\n" \
  "s_branch FB_B%=\n" \
  "s_branch RET_T%=\n" \
  "FB_A%=:\n" \
  "s_waitcnt lgkmcnt(0)\n" \
  "s_nop 7\n" \
  "s_nop 7\n" \
  "v_add_u32 v232, s62, v192\n" \
  "v_add_u32 v236, s69, v192\n" \
  "v_add_u32 v233, s62, v193\n" \
  "v_add_u32 v237, s69, v193\n" \
  "v_add_u32 v234, s62, v194\n" \
  "v_add_u32 v238, s69, v194\n" \
  "v_add_u32 v235, s62, v195\n" \
  "v_add_u32 v239, s69, v195\n" \
  "ds_read_b128 v[160:163], v232 offset:0\n" \
  "ds_read_b128 v[164:167], v233 offset:0\n" \
  "ds_read_b128 v[168:171], v234 offset:0\n" \
  "ds_read_b128 v[172:175], v235 offset:0\n" \
  "ds_read_b128 v[176:179], v232 offset:4096\n" \
  "ds_read_b128 v[180:183], v233 offset:4096\n" \
  "ds_read_b128 v[184:187], v234 offset:4096\n" \
  "ds_read_b128 v[188:191], v235 offset:4096\n" \
  "s_waitcnt lgkmcnt(0)\n" \
  "v_mfma_f32_16x16x32_bf16 v[0:3], v[160:163], a[128:131], 0\n" \
  "v_mfma_f32_16x16x32_bf16 v[0:3], v[164:167], a[132:135], v[0:3]\n" \
  "v_mfma_f32_16x16x32_bf16 v[0:3], v[168:171], a[136:139], v[0:3]\n" \
  "v_mfma_f32_16x16x32_bf16 v[0:3], v[172:175], a[140:143], v[0:3]\n" \
  "v_mfma_f32_16x16x32_bf16 v[4:7], v[160:163], a[144:147], 0\n" \
  "v_mfma_f32_16x16x32_bf16 v[4:7], v[164:167], a[148:151], v[4:7]\n" \
  "v_mfma_f32_16x16x32_bf16 v[4:7], v[168:171], a[152:155], v[4:7]\n" \
  "v_mfma_f32_16x16x32_bf16 v[4:7], v[172:175], a[156:159], v[4:7]\n" \
  "v_mfma_f32_16x16x32_bf16 v[8:11], v[160:163], a[160:163], 0\n" \
  "v_mfma_f32_16x16x32_bf16 v[8:11], v[164:167], a[164:167], v[8:11]\n" \
  "v_mfma_f32_16x16x32_bf16 v[8:11], v[168:171], a[168:171], v[8:11]\n" \
  "v_mfma_f32_16x16x32_bf16 v[8:11], v[172:175], a[172:175], v[8:11]\n" \
  "v_mfma_f32_16x16x32_bf16 v[12:15], v[160:163], a[176:179], 0\n" \
  "v_mfma_f32_16x16x32_bf16 v[12:15], v[164:167], a[180:183], v[12:15]\n" \
  "v_mfma_f32_16x16x32_bf16 v[12:15], v[168:171], a[184:187], v[12:15]\n" \
  "v_mfma_f32_16x16x32_bf16 v[12:15], v[172:175], a[188:191], v[12:15]\n" \
  "v_mfma_f32_16x16x32_bf16 v[16:19], v[176:179], a[128:131], 0\n" \
  "v_mfma_f32_16x16x32_bf16 v[16:19], v[180:183], a[132:135], v[16:19]\n" \
  "v_mfma_f32_16x16x32_bf16 v[16:19], v[184:187], a[136:139], v[16:19]\n" \
  "v_mfma_f32_16x16x32_bf16 v[16:19], v[188:191], a[140:143], v[16:19]\n" \
  "v_mfma_f32_16x16x32_bf16 v[20:23], v[176:179], a[144:147], 0\n" \
  "v_mfma_f32_16x16x32_bf16 v[20:23], v[180:183], a[148:151], v[20:23]\n" \
  "v_mfma_f32_16x16x32_bf16 v[20:23], v[184:187], a[152:155], v[20:23]\n" \
  "v_mfma_f32_16x16x32_bf16 v[20:23], v[188:191], a[156:159], v[20:23]\n" \
  "v_mfma_f32_16x16x32_bf16 v[24:27], v[176:179], a[160:163], 0\n" \
  "v_mfma_f32_16x16x32_bf16 v[24:27], v[180:183], a[164:167], v[24:27]\n" \
  "v_mfma_f32_16x16x32_bf16 v[24:27], v[184:187], a[168:171], v[24:27]\n" \
  "v_mfma_f32_16x16x32_bf16 v[24:27], v[188:191], a[172:175], v[24:27]\n" \
  "v_mfma_f32_16x16x32_bf16 v[28:31], v[176:179], a[176:179], 0\n" \
  "v_mfma_f32_16x16x32_bf16 v[28:31], v[180:183], a[180:183], v[28:31]\n" \
  "v_mfma_f32_16x16x32_bf16 v[28:31], v[184:187], a[184:187], v[28:31]\n" \
  "v_mfma_f32_16x16x32_bf16 v[28:31], v[188:191], a[188:191], v[28:31]\n" \
  "ds_read_b128 v[160:163], v232 offset:8192\n" \
  "ds_read_b128 v[164:167], v233 offset:8192\n" \
  "ds_read_b128 v[168:171], v234 offset:8192\n" \
  "ds_read_b128 v[172:175], v235 offset:8192\n" \
  "ds_read_b128 v[176:179], v232 offset:12288\n" \
  "ds_read_b128 v[180:183], v233 offset:12288\n" \
  "ds_read_b128 v[184:187], v234 offset:12288\n" \
  "ds_read_b128 v[188:191], v235 offset:12288\n" \
  "s_nop 7\n" \
  "s_nop 7\n" \
  "v_max3_f32 v212, v0, v1, v2\n" \
  "v_max3_f32 v212, v212, v3, v16\n" \
  "v_max3_f32 v212, v212, v17, v18\n" \
  "v_max_f32 v212, v212, v19\n" \
  "v_max3_f32 v213, v4, v5, v6\n" \
  "v_max3_f32 v213, v213, v7, v20\n" \
  "v_max3_f32 v213, v213, v21, v22\n" \
  "v_max_f32 v213, v213, v23\n" \
  "v_max3_f32 v214, v8, v9, v10\n" \
  "v_max3_f32 v214, v214, v11, v24\n" \
  "v_max3_f32 v214, v214, v25, v26\n" \
  "v_max_f32 v214, v214, v27\n" \
  "v_max3_f32 v215, v12, v13, v14\n" \
  "v_max3_f32 v215, v215, v15, v28\n" \
  "v_max3_f32 v215, v215, v29, v30\n" \
  "v_max_f32 v215, v215, v31\n" \
  "v_mov_b32 v222, v212\n" \
  "v_mov_b32 v223, v213\n" \
  "v_mov_b32 v224, v214\n" \
  "v_mov_b32 v225, v215\n" \
  "s_nop 1\n" \
  "v_permlane32_swap_b32 v212, v222\n" \
  "v_permlane32_swap_b32 v213, v223\n" \
  "v_permlane32_swap_b32 v214, v224\n" \
  "v_permlane32_swap_b32 v215, v225\n" \
  "s_nop 1\n" \
  "v_max_f32 v212, v212, v222\n" \
  "v_max_f32 v213, v213, v223\n" \
  "v_max_f32 v214, v214, v224\n" \
  "v_max_f32 v215, v215, v225\n" \
  "v_mov_b32 v222, v212\n" \
  "v_mov_b32 v223, v213\n" \
  "v_mov_b32 v224, v214\n" \
  "v_mov_b32 v225, v215\n" \
  "s_nop 1\n" \
  "v_permlane16_swap_b32 v212, v222\n" \
  "v_permlane16_swap_b32 v213, v223\n" \
  "v_permlane16_swap_b32 v214, v224\n" \
  "v_permlane16_swap_b32 v215, v225\n" \
  "s_nop 1\n" \
  "v_max_f32 v212, v212, v222\n" \
  "v_max_f32 v213, v213, v223\n" \
  "v_max_f32 v214, v214, v224\n" \
  "v_max_f32 v215, v215, v225\n" \
  "v_max_f32 v212, v212, v120\n" \
  "v_sub_f32 v216, v120, v212\n" \
  "v_exp_f32 v216, v216\n" \
  "v_mov_b32 v120, v212\n" \
  "v_xor_b32 v220, 0x80000000, v212\n" \
  "v_mov_b32 v96, v220\n" \
  "v_mov_b32 v97, v220\n" \
  "v_mov_b32 v98, v220\n" \
  "v_mov_b32 v99, v220\n" \
  "v_mul_f32 v112, v112, v216\n" \
  "v_max_f32 v213, v213, v121\n" \
  "v_sub_f32 v217, v121, v213\n" \
  "v_exp_f32 v217, v217\n" \
  "v_mov_b32 v121, v213\n" \
  "v_xor_b32 v220, 0x80000000, v213\n" \
  "v_mov_b32 v100, v220\n" \
  "v_mov_b32 v101, v220\n" \
  "v_mov_b32 v102, v220\n" \
  "v_mov_b32 v103, v220\n" \
  "v_mul_f32 v113, v113, v217\n" \
  "v_max_f32 v214, v214, v122\n" \
  "v_sub_f32 v218, v122, v214\n" \
  "v_exp_f32 v218, v218\n" \
  "v_mov_b32 v122, v214\n" \
  "v_xor_b32 v220, 0x80000000, v214\n" \
  "v_mov_b32 v104, v220\n" \
  "v_mov_b32 v105, v220\n" \
  "v_mov_b32 v106, v220\n" \
  "v_mov_b32 v107, v220\n" \
  "v_mul_f32 v114, v114, v218\n" \
  "v_max_f32 v215, v215, v123\n" \
  "v_sub_f32 v219, v123, v215\n" \
  "v_exp_f32 v219, v219\n" \
  "v_mov_b32 v123, v215\n" \
  "v_xor_b32 v220, 0x80000000, v215\n" \
  "v_mov_b32 v108, v220\n" \
  "v_mov_b32 v109, v220\n" \
  "v_mov_b32 v110, v220\n" \
  "v_mov_b32 v111, v220\n" \
  "v_mul_f32 v115, v115, v219\n" \
  "v_accvgpr_read_b32 v222, a0\n" \
  "v_mul_f32 v222, v222, v216\n" \
  "v_accvgpr_write_b32 a0, v222\n" \
  "v_accvgpr_read_b32 v223, a1\n" \
  "v_mul_f32 v223, v223, v216\n" \
  "v_accvgpr_write_b32 a1, v223\n" \
  "v_accvgpr_read_b32 v224, a2\n" \
  "v_mul_f32 v224, v224, v216\n" \
  "v_accvgpr_write_b32 a2, v224\n" \
  "v_accvgpr_read_b32 v225, a3\n" \
  "v_mul_f32 v225, v225, v216\n" \
  "v_accvgpr_write_b32 a3, v225\n" \
  "v_accvgpr_read_b32 v222, a4\n" \
  "v_mul_f32 v222, v222, v217\n" \
  "v_accvgpr_write_b32 a4, v222\n" \
  "v_accvgpr_read_b32 v223, a5\n" \
  "v_mul_f32 v223, v223, v217\n" \
  "v_accvgpr_write_b32 a5, v223\n" \
  "v_accvgpr_read_b32 v224, a6\n" \
  "v_mul_f32 v224, v224, v217\n" \
  "v_accvgpr_write_b32 a6, v224\n" \
  "v_accvgpr_read_b32 v225, a7\n" \
  "v_mul_f32 v225, v225, v217\n" \
  "v_accvgpr_write_b32 a7, v225\n" \
  "v_accvgpr_read_b32 v222, a8\n" \
  "v_mul_f32 v222, v222, v218\n" \
  "v_accvgpr_write_b32 a8, v222\n" \
  "v_accvgpr_read_b32 v223, a9\n" \
  "v_mul_f32 v223, v223, v218\n" \
  "v_accvgpr_write_b32 a9, v223\n" \
  "v_accvgpr_read_b32 v224, a10\n" \
  "v_mul_f32 v224, v224, v218\n" \
  "v_accvgpr_write_b32 a10, v224\n" \
  "v_accvgpr_read_b32 v225, a11\n" \
  "v_mul_f32 v225, v225, v218\n" \
  "v_accvgpr_write_b32 a11, v225\n" \
  "v_accvgpr_read_b32 v222, a12\n" \
  "v_mul_f32 v222, v222, v219\n" \
  "v_accvgpr_write_b32 a12, v222\n" \
  "v_accvgpr_read_b32 v223, a13\n" \
  "v_mul_f32 v223, v223, v219\n" \
  "v_accvgpr_write_b32 a13, v223\n" \
  "v_accvgpr_read_b32 v224, a14\n" \
  "v_mul_f32 v224, v224, v219\n" \
  "v_accvgpr_write_b32 a14, v224\n" \
  "v_accvgpr_read_b32 v225, a15\n" \
  "v_mul_f32 v225, v225, v219\n" \
  "v_accvgpr_write_b32 a15, v225\n" \
  "v_accvgpr_read_b32 v222, a16\n" \
  "v_mul_f32 v222, v222, v216\n" \
  "v_accvgpr_write_b32 a16, v222\n" \
  "v_accvgpr_read_b32 v223, a17\n" \
  "v_mul_f32 v223, v223, v216\n" \
  "v_accvgpr_write_b32 a17, v223\n" \
  "v_accvgpr_read_b32 v224, a18\n" \
  "v_mul_f32 v224, v224, v216\n" \
  "v_accvgpr_write_b32 a18, v224\n" \
  "v_accvgpr_read_b32 v225, a19\n" \
  "v_mul_f32 v225, v225, v216\n" \
  "v_accvgpr_write_b32 a19, v225\n" \
  "v_accvgpr_read_b32 v222, a20\n" \
  "v_mul_f32 v222, v222, v217\n" \
  "v_accvgpr_write_b32 a20, v222\n" \
  "v_accvgpr_read_b32 v223, a21\n" \
  "v_mul_f32 v223, v223, v217\n" \
  "v_accvgpr_write_b32 a21, v223\n" \
  "v_accvgpr_read_b32 v224, a22\n" \
  "v_mul_f32 v224, v224, v217\n" \
  "v_accvgpr_write_b32 a22, v224\n" \
  "v_accvgpr_read_b32 v225, a23\n" \
  "v_mul_f32 v225, v225, v217\n" \
  "v_accvgpr_write_b32 a23, v225\n" \
  "v_accvgpr_read_b32 v222, a24\n" \
  "v_mul_f32 v222, v222, v218\n" \
  "v_accvgpr_write_b32 a24, v222\n" \
  "v_accvgpr_read_b32 v223, a25\n" \
  "v_mul_f32 v223, v223, v218\n" \
  "v_accvgpr_write_b32 a25, v223\n" \
  "v_accvgpr_read_b32 v224, a26\n" \
  "v_mul_f32 v224, v224, v218\n" \
  "v_accvgpr_write_b32 a26, v224\n" \
  "v_accvgpr_read_b32 v225, a27\n" \
  "v_mul_f32 v225, v225, v218\n" \
  "v_accvgpr_write_b32 a27, v225\n" \
  "v_accvgpr_read_b32 v222, a28\n" \
  "v_mul_f32 v222, v222, v219\n" \
  "v_accvgpr_write_b32 a28, v222\n" \
  "v_accvgpr_read_b32 v223, a29\n" \
  "v_mul_f32 v223, v223, v219\n" \
  "v_accvgpr_write_b32 a29, v223\n" \
  "v_accvgpr_read_b32 v224, a30\n" \
  "v_mul_f32 v224, v224, v219\n" \
  "v_accvgpr_write_b32 a30, v224\n" \
  "v_accvgpr_read_b32 v225, a31\n" \
  "v_mul_f32 v225, v225, v219\n" \
  "v_accvgpr_write_b32 a31, v225\n" \
  "v_accvgpr_read_b32 v222, a32\n" \
  "v_mul_f32 v222, v222, v216\n" \
  "v_accvgpr_write_b32 a32, v222\n" \
  "v_accvgpr_read_b32 v223, a33\n" \
  "v_mul_f32 v223, v223, v216\n" \
  "v_accvgpr_write_b32 a33, v223\n" \
  "v_accvgpr_read_b32 v224, a34\n" \
  "v_mul_f32 v224, v224, v216\n" \
  "v_accvgpr_write_b32 a34, v224\n" \
  "v_accvgpr_read_b32 v225, a35\n" \
  "v_mul_f32 v225, v225, v216\n" \
  "v_accvgpr_write_b32 a35, v225\n" \
  "v_accvgpr_read_b32 v222, a36\n" \
  "v_mul_f32 v222, v222, v217\n" \
  "v_accvgpr_write_b32 a36, v222\n" \
  "v_accvgpr_read_b32 v223, a37\n" \
  "v_mul_f32 v223, v223, v217\n" \
  "v_accvgpr_write_b32 a37, v223\n" \
  "v_accvgpr_read_b32 v224, a38\n" \
  "v_mul_f32 v224, v224, v217\n" \
  "v_accvgpr_write_b32 a38, v224\n" \
  "v_accvgpr_read_b32 v225, a39\n" \
  "v_mul_f32 v225, v225, v217\n" \
  "v_accvgpr_write_b32 a39, v225\n" \
  "v_accvgpr_read_b32 v222, a40\n" \
  "v_mul_f32 v222, v222, v218\n" \
  "v_accvgpr_write_b32 a40, v222\n" \
  "v_accvgpr_read_b32 v223, a41\n" \
  "v_mul_f32 v223, v223, v218\n" \
  "v_accvgpr_write_b32 a41, v223\n" \
  "v_accvgpr_read_b32 v224, a42\n" \
  "v_mul_f32 v224, v224, v218\n" \
  "v_accvgpr_write_b32 a42, v224\n" \
  "v_accvgpr_read_b32 v225, a43\n" \
  "v_mul_f32 v225, v225, v218\n" \
  "v_accvgpr_write_b32 a43, v225\n" \
  "v_accvgpr_read_b32 v222, a44\n" \
  "v_mul_f32 v222, v222, v219\n" \
  "v_accvgpr_write_b32 a44, v222\n" \
  "v_accvgpr_read_b32 v223, a45\n" \
  "v_mul_f32 v223, v223, v219\n" \
  "v_accvgpr_write_b32 a45, v223\n" \
  "v_accvgpr_read_b32 v224, a46\n" \
  "v_mul_f32 v224, v224, v219\n" \
  "v_accvgpr_write_b32 a46, v224\n" \
  "v_accvgpr_read_b32 v225, a47\n" \
  "v_mul_f32 v225, v225, v219\n" \
  "v_accvgpr_write_b32 a47, v225\n" \
  "v_accvgpr_read_b32 v222, a48\n" \
  "v_mul_f32 v222, v222, v216\n" \
  "v_accvgpr_write_b32 a48, v222\n" \
  "v_accvgpr_read_b32 v223, a49\n" \
  "v_mul_f32 v223, v223, v216\n" \
  "v_accvgpr_write_b32 a49, v223\n" \
  "v_accvgpr_read_b32 v224, a50\n" \
  "v_mul_f32 v224, v224, v216\n" \
  "v_accvgpr_write_b32 a50, v224\n" \
  "v_accvgpr_read_b32 v225, a51\n" \
  "v_mul_f32 v225, v225, v216\n" \
  "v_accvgpr_write_b32 a51, v225\n" \
  "v_accvgpr_read_b32 v222, a52\n" \
  "v_mul_f32 v222, v222, v217\n" \
  "v_accvgpr_write_b32 a52, v222\n" \
  "v_accvgpr_read_b32 v223, a53\n" \
  "v_mul_f32 v223, v223, v217\n" \
  "v_accvgpr_write_b32 a53, v223\n" \
  "v_accvgpr_read_b32 v224, a54\n" \
  "v_mul_f32 v224, v224, v217\n" \
  "v_accvgpr_write_b32 a54, v224\n" \
  "v_accvgpr_read_b32 v225, a55\n" \
  "v_mul_f32 v225, v225, v217\n" \
  "v_accvgpr_write_b32 a55, v225\n" \
  "v_accvgpr_read_b32 v222, a56\n" \
  "v_mul_f32 v222, v222, v218\n" \
  "v_accvgpr_write_b32 a56, v222\n" \
  "v_accvgpr_read_b32 v223, a57\n" \
  "v_mul_f32 v223, v223, v218\n" \
  "v_accvgpr_write_b32 a57, v223\n" \
  "v_accvgpr_read_b32 v224, a58\n" \
  "v_mul_f32 v224, v224, v218\n" \
  "v_accvgpr_write_b32 a58, v224\n" \
  "v_accvgpr_read_b32 v225, a59\n" \
  "v_mul_f32 v225, v225, v218\n" \
  "v_accvgpr_write_b32 a59, v225\n" \
  "v_accvgpr_read_b32 v222, a60\n" \
  "v_mul_f32 v222, v222, v219\n" \
  "v_accvgpr_write_b32 a60, v222\n" \
  "v_accvgpr_read_b32 v223, a61\n" \
  "v_mul_f32 v223, v223, v219\n" \
  "v_accvgpr_write_b32 a61, v223\n" \
  "v_accvgpr_read_b32 v224, a62\n" \
  "v_mul_f32 v224, v224, v219\n" \
  "v_accvgpr_write_b32 a62, v224\n" \
  "v_accvgpr_read_b32 v225, a63\n" \
  "v_mul_f32 v225, v225, v219\n" \
  "v_accvgpr_write_b32 a63, v225\n" \
  "v_accvgpr_read_b32 v222, a64\n" \
  "v_mul_f32 v222, v222, v216\n" \
  "v_accvgpr_write_b32 a64, v222\n" \
  "v_accvgpr_read_b32 v223, a65\n" \
  "v_mul_f32 v223, v223, v216\n" \
  "v_accvgpr_write_b32 a65, v223\n" \
  "v_accvgpr_read_b32 v224, a66\n" \
  "v_mul_f32 v224, v224, v216\n" \
  "v_accvgpr_write_b32 a66, v224\n" \
  "v_accvgpr_read_b32 v225, a67\n" \
  "v_mul_f32 v225, v225, v216\n" \
  "v_accvgpr_write_b32 a67, v225\n" \
  "v_accvgpr_read_b32 v222, a68\n" \
  "v_mul_f32 v222, v222, v217\n" \
  "v_accvgpr_write_b32 a68, v222\n" \
  "v_accvgpr_read_b32 v223, a69\n" \
  "v_mul_f32 v223, v223, v217\n" \
  "v_accvgpr_write_b32 a69, v223\n" \
  "v_accvgpr_read_b32 v224, a70\n" \
  "v_mul_f32 v224, v224, v217\n" \
  "v_accvgpr_write_b32 a70, v224\n" \
  "v_accvgpr_read_b32 v225, a71\n" \
  "v_mul_f32 v225, v225, v217\n" \
  "v_accvgpr_write_b32 a71, v225\n" \
  "v_accvgpr_read_b32 v222, a72\n" \
  "v_mul_f32 v222, v222, v218\n" \
  "v_accvgpr_write_b32 a72, v222\n" \
  "v_accvgpr_read_b32 v223, a73\n" \
  "v_mul_f32 v223, v223, v218\n" \
  "v_accvgpr_write_b32 a73, v223\n" \
  "v_accvgpr_read_b32 v224, a74\n" \
  "v_mul_f32 v224, v224, v218\n" \
  "v_accvgpr_write_b32 a74, v224\n" \
  "v_accvgpr_read_b32 v225, a75\n" \
  "v_mul_f32 v225, v225, v218\n" \
  "v_accvgpr_write_b32 a75, v225\n" \
  "v_accvgpr_read_b32 v222, a76\n" \
  "v_mul_f32 v222, v222, v219\n" \
  "v_accvgpr_write_b32 a76, v222\n" \
  "v_accvgpr_read_b32 v223, a77\n" \
  "v_mul_f32 v223, v223, v219\n" \
  "v_accvgpr_write_b32 a77, v223\n" \
  "v_accvgpr_read_b32 v224, a78\n" \
  "v_mul_f32 v224, v224, v219\n" \
  "v_accvgpr_write_b32 a78, v224\n" \
  "v_accvgpr_read_b32 v225, a79\n" \
  "v_mul_f32 v225, v225, v219\n" \
  "v_accvgpr_write_b32 a79, v225\n" \
  "v_accvgpr_read_b32 v222, a80\n" \
  "v_mul_f32 v222, v222, v216\n" \
  "v_accvgpr_write_b32 a80, v222\n" \
  "v_accvgpr_read_b32 v223, a81\n" \
  "v_mul_f32 v223, v223, v216\n" \
  "v_accvgpr_write_b32 a81, v223\n" \
  "v_accvgpr_read_b32 v224, a82\n" \
  "v_mul_f32 v224, v224, v216\n" \
  "v_accvgpr_write_b32 a82, v224\n" \
  "v_accvgpr_read_b32 v225, a83\n" \
  "v_mul_f32 v225, v225, v216\n" \
  "v_accvgpr_write_b32 a83, v225\n" \
  "v_accvgpr_read_b32 v222, a84\n" \
  "v_mul_f32 v222, v222, v217\n" \
  "v_accvgpr_write_b32 a84, v222\n" \
  "v_accvgpr_read_b32 v223, a85\n" \
  "v_mul_f32 v223, v223, v217\n" \
  "v_accvgpr_write_b32 a85, v223\n" \
  "v_accvgpr_read_b32 v224, a86\n" \
  "v_mul_f32 v224, v224, v217\n" \
  "v_accvgpr_write_b32 a86, v224\n" \
  "v_accvgpr_read_b32 v225, a87\n" \
  "v_mul_f32 v225, v225, v217\n" \
  "v_accvgpr_write_b32 a87, v225\n" \
  "v_accvgpr_read_b32 v222, a88\n" \
  "v_mul_f32 v222, v222, v218\n" \
  "v_accvgpr_write_b32 a88, v222\n" \
  "v_accvgpr_read_b32 v223, a89\n" \
  "v_mul_f32 v223, v223, v218\n" \
  "v_accvgpr_write_b32 a89, v223\n" \
  "v_accvgpr_read_b32 v224, a90\n" \
  "v_mul_f32 v224, v224, v218\n" \
  "v_accvgpr_write_b32 a90, v224\n" \
  "v_accvgpr_read_b32 v225, a91\n" \
  "v_mul_f32 v225, v225, v218\n" \
  "v_accvgpr_write_b32 a91, v225\n" \
  "v_accvgpr_read_b32 v222, a92\n" \
  "v_mul_f32 v222, v222, v219\n" \
  "v_accvgpr_write_b32 a92, v222\n" \
  "v_accvgpr_read_b32 v223, a93\n" \
  "v_mul_f32 v223, v223, v219\n" \
  "v_accvgpr_write_b32 a93, v223\n" \
  "v_accvgpr_read_b32 v224, a94\n" \
  "v_mul_f32 v224, v224, v219\n" \
  "v_accvgpr_write_b32 a94, v224\n" \
  "v_accvgpr_read_b32 v225, a95\n" \
  "v_mul_f32 v225, v225, v219\n" \
  "v_accvgpr_write_b32 a95, v225\n" \
  "v_accvgpr_read_b32 v222, a96\n" \
  "v_mul_f32 v222, v222, v216\n" \
  "v_accvgpr_write_b32 a96, v222\n" \
  "v_accvgpr_read_b32 v223, a97\n" \
  "v_mul_f32 v223, v223, v216\n" \
  "v_accvgpr_write_b32 a97, v223\n" \
  "v_accvgpr_read_b32 v224, a98\n" \
  "v_mul_f32 v224, v224, v216\n" \
  "v_accvgpr_write_b32 a98, v224\n" \
  "v_accvgpr_read_b32 v225, a99\n" \
  "v_mul_f32 v225, v225, v216\n" \
  "v_accvgpr_write_b32 a99, v225\n" \
  "v_accvgpr_read_b32 v222, a100\n" \
  "v_mul_f32 v222, v222, v217\n" \
  "v_accvgpr_write_b32 a100, v222\n" \
  "v_accvgpr_read_b32 v223, a101\n" \
  "v_mul_f32 v223, v223, v217\n" \
  "v_accvgpr_write_b32 a101, v223\n" \
  "v_accvgpr_read_b32 v224, a102\n" \
  "v_mul_f32 v224, v224, v217\n" \
  "v_accvgpr_write_b32 a102, v224\n" \
  "v_accvgpr_read_b32 v225, a103\n" \
  "v_mul_f32 v225, v225, v217\n" \
  "v_accvgpr_write_b32 a103, v225\n" \
  "v_accvgpr_read_b32 v222, a104\n" \
  "v_mul_f32 v222, v222, v218\n" \
  "v_accvgpr_write_b32 a104, v222\n" \
  "v_accvgpr_read_b32 v223, a105\n" \
  "v_mul_f32 v223, v223, v218\n" \
  "v_accvgpr_write_b32 a105, v223\n" \
  "v_accvgpr_read_b32 v224, a106\n" \
  "v_mul_f32 v224, v224, v218\n" \
  "v_accvgpr_write_b32 a106, v224\n" \
  "v_accvgpr_read_b32 v225, a107\n" \
  "v_mul_f32 v225, v225, v218\n" \
  "v_accvgpr_write_b32 a107, v225\n" \
  "v_accvgpr_read_b32 v222, a108\n" \
  "v_mul_f32 v222, v222, v219\n" \
  "v_accvgpr_write_b32 a108, v222\n" \
  "v_accvgpr_read_b32 v223, a109\n" \
  "v_mul_f32 v223, v223, v219\n" \
  "v_accvgpr_write_b32 a109, v223\n" \
  "v_accvgpr_read_b32 v224, a110\n" \
  "v_mul_f32 v224, v224, v219\n" \
  "v_accvgpr_write_b32 a110, v224\n" \
  "v_accvgpr_read_b32 v225, a111\n" \
  "v_mul_f32 v225, v225, v219\n" \
  "v_accvgpr_write_b32 a111, v225\n" \
  "v_accvgpr_read_b32 v222, a112\n" \
  "v_mul_f32 v222, v222, v216\n" \
  "v_accvgpr_write_b32 a112, v222\n" \
  "v_accvgpr_read_b32 v223, a113\n" \
  "v_mul_f32 v223, v223, v216\n" \
  "v_accvgpr_write_b32 a113, v223\n" \
  "v_accvgpr_read_b32 v224, a114\n" \
  "v_mul_f32 v224, v224, v216\n" \
  "v_accvgpr_write_b32 a114, v224\n" \
  "v_accvgpr_read_b32 v225, a115\n" \
  "v_mul_f32 v225, v225, v216\n" \
  "v_accvgpr_write_b32 a115, v225\n" \
  "v_accvgpr_read_b32 v222, a116\n" \
  "v_mul_f32 v222, v222, v217\n" \
  "v_accvgpr_write_b32 a116, v222\n" \
  "v_accvgpr_read_b32 v223, a117\n" \
  "v_mul_f32 v223, v223, v217\n" \
  "v_accvgpr_write_b32 a117, v223\n" \
  "v_accvgpr_read_b32 v224, a118\n" \
  "v_mul_f32 v224, v224, v217\n" \
  "v_accvgpr_write_b32 a118, v224\n" \
  "v_accvgpr_read_b32 v225, a119\n" \
  "v_mul_f32 v225, v225, v217\n" \
  "v_accvgpr_write_b32 a119, v225\n" \
  "v_accvgpr_read_b32 v222, a120\n" \
  "v_mul_f32 v222, v222, v218\n" \
  "v_accvgpr_write_b32 a120, v222\n" \
  "v_accvgpr_read_b32 v223, a121\n" \
  "v_mul_f32 v223, v223, v218\n" \
  "v_accvgpr_write_b32 a121, v223\n" \
  "v_accvgpr_read_b32 v224, a122\n" \
  "v_mul_f32 v224, v224, v218\n" \
  "v_accvgpr_write_b32 a122, v224\n" \
  "v_accvgpr_read_b32 v225, a123\n" \
  "v_mul_f32 v225, v225, v218\n" \
  "v_accvgpr_write_b32 a123, v225\n" \
  "v_accvgpr_read_b32 v222, a124\n" \
  "v_mul_f32 v222, v222, v219\n" \
  "v_accvgpr_write_b32 a124, v222\n" \
  "v_accvgpr_read_b32 v223, a125\n" \
  "v_mul_f32 v223, v223, v219\n" \
  "v_accvgpr_write_b32 a125, v223\n" \
  "v_accvgpr_read_b32 v224, a126\n" \
  "v_mul_f32 v224, v224, v219\n" \
  "v_accvgpr_write_b32 a126, v224\n" \
  "v_accvgpr_read_b32 v225, a127\n" \
  "v_mul_f32 v225, v225, v219\n" \
  "v_accvgpr_write_b32 a127, v225\n" \
  "v_sub_f32 v0, v0, v120\n" \
  "v_sub_f32 v1, v1, v120\n" \
  "v_sub_f32 v2, v2, v120\n" \
  "v_sub_f32 v3, v3, v120\n" \
  "v_sub_f32 v16, v16, v120\n" \
  "v_sub_f32 v17, v17, v120\n" \
  "v_sub_f32 v18, v18, v120\n" \
  "v_sub_f32 v19, v19, v120\n" \
  "v_exp_f32 v0, v0\n" \
  "v_exp_f32 v1, v1\n" \
  "v_exp_f32 v2, v2\n" \
  "v_exp_f32 v3, v3\n" \
  "v_exp_f32 v16, v16\n" \
  "v_exp_f32 v17, v17\n" \
  "v_exp_f32 v18, v18\n" \
  "v_exp_f32 v19, v19\n" \
  "s_nop 0\n" \
  "v_cvt_pk_bf16_f32 v64, v0, v1\n" \
  "v_cvt_pk_bf16_f32 v65, v2, v3\n" \
  "v_cvt_pk_bf16_f32 v66, v16, v17\n" \
  "v_cvt_pk_bf16_f32 v67, v18, v19\n" \
  "v_sub_f32 v4, v4, v121\n" \
  "v_sub_f32 v5, v5, v121\n" \
  "v_sub_f32 v6, v6, v121\n" \
  "v_sub_f32 v7, v7, v121\n" \
  "v_sub_f32 v20, v20, v121\n" \
  "v_sub_f32 v21, v21, v121\n" \
  "v_sub_f32 v22, v22, v121\n" \
  "v_sub_f32 v23, v23, v121\n" \
  "v_exp_f32 v4, v4\n" \
  "v_exp_f32 v5, v5\n" \
  "v_exp_f32 v6, v6\n" \
  "v_exp_f32 v7, v7\n" \
  "v_exp_f32 v20, v20\n" \
  "v_exp_f32 v21, v21\n" \
  "v_exp_f32 v22, v22\n" \
  "v_exp_f32 v23, v23\n" \
  "s_nop 0\n" \
  "v_cvt_pk_bf16_f32 v68, v4, v5\n" \
  "v_cvt_pk_bf16_f32 v69, v6, v7\n" \
  "v_cvt_pk_bf16_f32 v70, v20, v21\n" \
  "v_cvt_pk_bf16_f32 v71, v22, v23\n" \
  "v_sub_f32 v8, v8, v122\n" \
  "v_sub_f32 v9, v9, v122\n" \
  "v_sub_f32 v10, v10, v122\n" \
  "v_sub_f32 v11, v11, v122\n" \
  "v_sub_f32 v24, v24, v122\n" \
  "v_sub_f32 v25, v25, v122\n" \
  "v_sub_f32 v26, v26, v122\n" \
  "v_sub_f32 v27, v27, v122\n" \
  "v_exp_f32 v8, v8\n" \
  "v_exp_f32 v9, v9\n" \
  "v_exp_f32 v10, v10\n" \
  "v_exp_f32 v11, v11\n" \
  "v_exp_f32 v24, v24\n" \
  "v_exp_f32 v25, v25\n" \
  "v_exp_f32 v26, v26\n" \
  "v_exp_f32 v27, v27\n" \
  "s_nop 0\n" \
  "v_cvt_pk_bf16_f32 v72, v8, v9\n" \
  "v_cvt_pk_bf16_f32 v73, v10, v11\n" \
  "v_cvt_pk_bf16_f32 v74, v24, v25\n" \
  "v_cvt_pk_bf16_f32 v75, v26, v27\n" \
  "v_sub_f32 v12, v12, v123\n" \
  "v_sub_f32 v13, v13, v123\n" \
  "v_sub_f32 v14, v14, v123\n" \
  "v_sub_f32 v15, v15, v123\n" \
  "v_sub_f32 v28, v28, v123\n" \
  "v_sub_f32 v29, v29, v123\n" \
  "v_sub_f32 v30, v30, v123\n" \
  "v_sub_f32 v31, v31, v123\n" \
  "v_exp_f32 v12, v12\n" \
  "v_exp_f32 v13, v13\n" \
  "v_exp_f32 v14, v14\n" \
  "v_exp_f32 v15, v15\n" \
  "v_exp_f32 v28, v28\n" \
  "v_exp_f32 v29, v29\n" \
  "v_exp_f32 v30, v30\n" \
  "v_exp_f32 v31, v31\n" \
  "s_nop 0\n" \
  "v_cvt_pk_bf16_f32 v76, v12, v13\n" \
  "v_cvt_pk_bf16_f32 v77, v14, v15\n" \
  "v_cvt_pk_bf16_f32 v78, v28, v29\n" \
  "v_cvt_pk_bf16_f32 v79, v30, v31\n" \
  "s_nop 1\n" \
  "v_mfma_f32_16x16x32_bf16 v[240:243], v[124:127], v[64:67], 0\n" \
  "v_mfma_f32_16x16x32_bf16 v[244:247], v[124:127], v[68:71], 0\n" \
  "v_mfma_f32_16x16x32_bf16 v[248:251], v[124:127], v[72:75], 0\n" \
  "v_mfma_f32_16x16x32_bf16 v[252:255], v[124:127], v[76:79], 0\n" \
  "s_nop 7\n" \
  "s_nop 3\n" \
  "s_waitcnt lgkmcnt(0)\n" \
  "s_nop 1\n" \
  "v_mfma_f32_16x16x32_bf16 v[32:35], v[160:163], a[128:131], v[96:99]\n" \
  "v_mfma_f32_16x16x32_bf16 v[32:35], v[164:167], a[132:135], v[32:35]\n" \
  "v_mfma_f32_16x16x32_bf16 v[32:35], v[168:171], a[136:139], v[32:35]\n" \
  "v_mfma_f32_16x16x32_bf16 v[32:35], v[172:175], a[140:143], v[32:35]\n" \
  "v_mfma_f32_16x16x32_bf16 v[36:39], v[160:163], a[144:147], v[100:103]\n" \
  "v_mfma_f32_16x16x32_bf16 v[36:39], v[164:167], a[148:151], v[36:39]\n" \
  "v_mfma_f32_16x16x32_bf16 v[36:39], v[168:171], a[152:155], v[36:39]\n" \
  "v_mfma_f32_16x16x32_bf16 v[36:39], v[172:175], a[156:159], v[36:39]\n" \
  "v_mfma_f32_16x16x32_bf16 v[40:43], v[160:163], a[160:163], v[104:107]\n" \
  "v_mfma_f32_16x16x32_bf16 v[40:43], v[164:167], a[164:167], v[40:43]\n" \
  "v_mfma_f32_16x16x32_bf16 v[40:43], v[168:171], a[168:171], v[40:43]\n" \
  "v_mfma_f32_16x16x32_bf16 v[40:43], v[172:175], a[172:175], v[40:43]\n" \
  "v_mfma_f32_16x16x32_bf16 v[44:47], v[160:163], a[176:179], v[108:111]\n" \
  "v_mfma_f32_16x16x32_bf16 v[44:47], v[164:167], a[180:183], v[44:47]\n" \
  "v_mfma_f32_16x16x32_bf16 v[44:47], v[168:171], a[184:187], v[44:47]\n" \
  "v_mfma_f32_16x16x32_bf16 v[44:47], v[172:175], a[188:191], v[44:47]\n" \
  "v_mfma_f32_16x16x32_bf16 v[48:51], v[176:179], a[128:131], v[96:99]\n" \
  "v_mfma_f32_16x16x32_bf16 v[48:51], v[180:183], a[132:135], v[48:51]\n" \
  "v_mfma_f32_16x16x32_bf16 v[48:51], v[184:187], a[136:139], v[48:51]\n" \
  "v_mfma_f32_16x16x32_bf16 v[48:51], v[188:191], a[140:143], v[48:51]\n" \
  "v_mfma_f32_16x16x32_bf16 v[52:55], v[176:179], a[144:147], v[100:103]\n" \
  "v_mfma_f32_16x16x32_bf16 v[52:55], v[180:183], a[148:151], v[52:55]\n" \
  "v_mfma_f32_16x16x32_bf16 v[52:55], v[184:187], a[152:155], v[52:55]\n" \
  "v_mfma_f32_16x16x32_bf16 v[52:55], v[188:191], a[156:159], v[52:55]\n" \
  "v_mfma_f32_16x16x32_bf16 v[56:59], v[176:179], a[160:163], v[104:107]\n" \
  "v_mfma_f32_16x16x32_bf16 v[56:59], v[180:183], a[164:167], v[56:59]\n" \
  "v_mfma_f32_16x16x32_bf16 v[56:59], v[184:187], a[168:171], v[56:59]\n" \
  "v_mfma_f32_16x16x32_bf16 v[56:59], v[188:191], a[172:175], v[56:59]\n" \
  "v_mfma_f32_16x16x32_bf16 v[60:63], v[176:179], a[176:179], v[108:111]\n" \
  "v_mfma_f32_16x16x32_bf16 v[60:63], v[180:183], a[180:183], v[60:63]\n" \
  "v_mfma_f32_16x16x32_bf16 v[60:63], v[184:187], a[184:187], v[60:63]\n" \
  "v_mfma_f32_16x16x32_bf16 v[60:63], v[188:191], a[188:191], v[60:63]\n" \
  "s_nop 7\n" \
  "s_nop 7\n" \
  "s_add_u32 s60, s60, 4\n" \
  "s_addc_u32 s61, s61, 0\n" \
  "s_setpc_b64 s[60:61]\n" \
  "FB_B%=:\n" \
  "s_waitcnt lgkmcnt(0)\n" \
  "s_nop 7\n" \
  "s_nop 7\n" \
  "v_add_u32 v232, s62, v192\n" \
  "v_add_u32 v236, s69, v192\n" \
  "v_add_u32 v233, s62, v193\n" \
  "v_add_u32 v237, s69, v193\n" \
  "v_add_u32 v234, s62, v194\n" \
  "v_add_u32 v238, s69, v194\n" \
  "v_add_u32 v235, s62, v195\n" \
  "v_add_u32 v239, s69, v195\n" \
  "ds_read_b128 v[160:163], v232 offset:8192\n" \
  "ds_read_b128 v[164:167], v233 offset:8192\n" \
  "ds_read_b128 v[168:171], v234 offset:8192\n" \
  "ds_read_b128 v[172:175], v235 offset:8192\n" \
  "ds_read_b128 v[176:179], v232 offset:12288\n" \
  "ds_read_b128 v[180:183], v233 offset:12288\n" \
  "ds_read_b128 v[184:187], v234 offset:12288\n" \
  "ds_read_b128 v[188:191], v235 offset:12288\n" \
  "s_waitcnt lgkmcnt(0)\n" \
  "v_mfma_f32_16x16x32_bf16 v[32:35], v[160:163], a[128:131], 0\n" \
  "v_mfma_f32_16x16x32_bf16 v[32:35], v[164:167], a[132:135], v[32:35]\n" \
  "v_mfma_f32_16x16x32_bf16 v[32:35], v[168:171], a[136:139], v[32:35]\n" \
  "v_mfma_f32_16x16x32_bf16 v[32:35], v[172:175], a[140:143], v[32:35]\n" \
  "v_mfma_f32_16x16x32_bf16 v[36:39], v[160:163], a[144:147], 0\n" \
  "v_mfma_f32_16x16x32_bf16 v[36:39], v[164:167], a[148:151], v[36:39]\n" \
  "v_mfma_f32_16x16x32_bf16 v[36:39], v[168:171], a[152:155], v[36:39]\n" \
  "v_mfma_f32_16x16x32_bf16 v[36:39], v[172:175], a[156:159], v[36:39]\n" \
  "v_mfma_f32_16x16x32_bf16 v[40:43], v[160:163], a[160:163], 0\n" \
  "v_mfma_f32_16x16x32_bf16 v[40:43], v[164:167], a[164:167], v[40:43]\n" \
  "v_mfma_f32_16x16x32_bf16 v[40:43], v[168:171], a[168:171], v[40:43]\n" \
  "v_mfma_f32_16x16x32_bf16 v[40:43], v[172:175], a[172:175], v[40:43]\n" \
  "v_mfma_f32_16x16x32_bf16 v[44:47], v[160:163], a[176:179], 0\n" \
  "v_mfma_f32_16x16x32_bf16 v[44:47], v[164:167], a[180:183], v[44:47]\n" \
  "v_mfma_f32_16x16x32_bf16 v[44:47], v[168:171], a[184:187], v[44:47]\n" \
  "v_mfma_f32_16x16x32_bf16 v[44:47], v[172:175], a[188:191], v[44:47]\n" \
  "v_mfma_f32_16x16x32_bf16 v[48:51], v[176:179], a[128:131], 0\n" \
  "v_mfma_f32_16x16x32_bf16 v[48:51], v[180:183], a[132:135], v[48:51]\n" \
  "v_mfma_f32_16x16x32_bf16 v[48:51], v[184:187], a[136:139], v[48:51]\n" \
  "v_mfma_f32_16x16x32_bf16 v[48:51], v[188:191], a[140:143], v[48:51]\n" \
  "v_mfma_f32_16x16x32_bf16 v[52:55], v[176:179], a[144:147], 0\n" \
  "v_mfma_f32_16x16x32_bf16 v[52:55], v[180:183], a[148:151], v[52:55]\n" \
  "v_mfma_f32_16x16x32_bf16 v[52:55], v[184:187], a[152:155], v[52:55]\n" \
  "v_mfma_f32_16x16x32_bf16 v[52:55], v[188:191], a[156:159], v[52:55]\n" \
  "v_mfma_f32_16x16x32_bf16 v[56:59], v[176:179], a[160:163], 0\n" \
  "v_mfma_f32_16x16x32_bf16 v[56:59], v[180:183], a[164:167], v[56:59]\n" \
  "v_mfma_f32_16x16x32_bf16 v[56:59], v[184:187], a[168:171], v[56:59]\n" \
  "v_mfma_f32_16x16x32_bf16 v[56:59], v[188:191], a[172:175], v[56:59]\n" \
  "v_mfma_f32_16x16x32_bf16 v[60:63], v[176:179], a[176:179], 0\n" \
  "v_mfma_f32_16x16x32_bf16 v[60:63], v[180:183], a[180:183], v[60:63]\n" \
  "v_mfma_f32_16x16x32_bf16 v[60:63], v[184:187], a[184:187], v[60:63]\n" \
  "v_mfma_f32_16x16x32_bf16 v[60:63], v[188:191], a[188:191], v[60:63]\n" \
  "ds_read_b128 v[160:163], v236 offset:0\n" \
  "ds_read_b128 v[164:167], v237 offset:0\n" \
  "ds_read_b128 v[168:171], v238 offset:0\n" \
  "ds_read_b128 v[172:175], v239 offset:0\n" \
  "ds_read_b128 v[176:179], v236 offset:4096\n" \
  "ds_read_b128 v[180:183], v237 offset:4096\n" \
  "ds_read_b128 v[184:187], v238 offset:4096\n" \
  "ds_read_b128 v[188:191], v239 offset:4096\n" \
  "s_nop 7\n" \
  "s_nop 7\n" \
  "v_max3_f32 v212, v32, v33, v34\n" \
  "v_max3_f32 v212, v212, v35, v48\n" \
  "v_max3_f32 v212, v212, v49, v50\n" \
  "v_max_f32 v212, v212, v51\n" \
  "v_max3_f32 v213, v36, v37, v38\n" \
  "v_max3_f32 v213, v213, v39, v52\n" \
  "v_max3_f32 v213, v213, v53, v54\n" \
  "v_max_f32 v213, v213, v55\n" \
  "v_max3_f32 v214, v40, v41, v42\n" \
  "v_max3_f32 v214, v214, v43, v56\n" \
  "v_max3_f32 v214, v214, v57, v58\n" \
  "v_max_f32 v214, v214, v59\n" \
  "v_max3_f32 v215, v44, v45, v46\n" \
  "v_max3_f32 v215, v215, v47, v60\n" \
  "v_max3_f32 v215, v215, v61, v62\n" \
  "v_max_f32 v215, v215, v63\n" \
  "v_mov_b32 v222, v212\n" \
  "v_mov_b32 v223, v213\n" \
  "v_mov_b32 v224, v214\n" \
  "v_mov_b32 v225, v215\n" \
  "s_nop 1\n" \
  "v_permlane32_swap_b32 v212, v222\n" \
  "v_permlane32_swap_b32 v213, v223\n" \
  "v_permlane32_swap_b32 v214, v224\n" \
  "v_permlane32_swap_b32 v215, v225\n" \
  "s_nop 1\n" \
  "v_max_f32 v212, v212, v222\n" \
  "v_max_f32 v213, v213, v223\n" \
  "v_max_f32 v214, v214, v224\n" \
  "v_max_f32 v215, v215, v225\n" \
  "v_mov_b32 v222, v212\n" \
  "v_mov_b32 v223, v213\n" \
  "v_mov_b32 v224, v214\n" \
  "v_mov_b32 v225, v215\n" \
  "s_nop 1\n" \
  "v_permlane16_swap_b32 v212, v222\n" \
  "v_permlane16_swap_b32 v213, v223\n" \
  "v_permlane16_swap_b32 v214, v224\n" \
  "v_permlane16_swap_b32 v215, v225\n" \
  "s_nop 1\n" \
  "v_max_f32 v212, v212, v222\n" \
  "v_max_f32 v213, v213, v223\n" \
  "v_max_f32 v214, v214, v224\n" \
  "v_max_f32 v215, v215, v225\n" \
  "v_max_f32 v212, v212, v120\n" \
  "v_sub_f32 v216, v120, v212\n" \
  "v_exp_f32 v216, v216\n" \
  "v_mov_b32 v120, v212\n" \
  "v_xor_b32 v220, 0x80000000, v212\n" \
  "v_mov_b32 v96, v220\n" \
  "v_mov_b32 v97, v220\n" \
  "v_mov_b32 v98, v220\n" \
  "v_mov_b32 v99, v220\n" \
  "v_mul_f32 v112, v112, v216\n" \
  "v_max_f32 v213, v213, v121\n" \
  "v_sub_f32 v217, v121, v213\n" \
  "v_exp_f32 v217, v217\n" \
  "v_mov_b32 v121, v213\n" \
  "v_xor_b32 v220, 0x80000000, v213\n" \
  "v_mov_b32 v100, v220\n" \
  "v_mov_b32 v101, v220\n" \
  "v_mov_b32 v102, v220\n" \
  "v_mov_b32 v103, v220\n" \
  "v_mul_f32 v113, v113, v217\n" \
  "v_max_f32 v214, v214, v122\n" \
  "v_sub_f32 v218, v122, v214\n" \
  "v_exp_f32 v218, v218\n" \
  "v_mov_b32 v122, v214\n" \
  "v_xor_b32 v220, 0x80000000, v214\n" \
  "v_mov_b32 v104, v220\n" \
  "v_mov_b32 v105, v220\n" \
  "v_mov_b32 v106, v220\n" \
  "v_mov_b32 v107, v220\n" \
  "v_mul_f32 v114, v114, v218\n" \
  "v_max_f32 v215, v215, v123\n" \
  "v_sub_f32 v219, v123, v215\n" \
  "v_exp_f32 v219, v219\n" \
  "v_mov_b32 v123, v215\n" \
  "v_xor_b32 v220, 0x80000000, v215\n" \
  "v_mov_b32 v108, v220\n" \
  "v_mov_b32 v109, v220\n" \
  "v_mov_b32 v110, v220\n" \
  "v_mov_b32 v111, v220\n" \
  "v_mul_f32 v115, v115, v219\n" \
  "v_accvgpr_read_b32 v222, a0\n" \
  "v_mul_f32 v222, v222, v216\n" \
  "v_accvgpr_write_b32 a0, v222\n" \
  "v_accvgpr_read_b32 v223, a1\n" \
  "v_mul_f32 v223, v223, v216\n" \
  "v_accvgpr_write_b32 a1, v223\n" \
  "v_accvgpr_read_b32 v224, a2\n" \
  "v_mul_f32 v224, v224, v216\n" \
  "v_accvgpr_write_b32 a2, v224\n" \
  "v_accvgpr_read_b32 v225, a3\n" \
  "v_mul_f32 v225, v225, v216\n" \
  "v_accvgpr_write_b32 a3, v225\n" \
  "v_accvgpr_read_b32 v222, a4\n" \
  "v_mul_f32 v222, v222, v217\n" \
  "v_accvgpr_write_b32 a4, v222\n" \
  "v_accvgpr_read_b32 v223, a5\n" \
  "v_mul_f32 v223, v223, v217\n" \
  "v_accvgpr_write_b32 a5, v223\n" \
  "v_accvgpr_read_b32 v224, a6\n" \
  "v_mul_f32 v224, v224, v217\n" \
  "v_accvgpr_write_b32 a6, v224\n" \
  "v_accvgpr_read_b32 v225, a7\n" \
  "v_mul_f32 v225, v225, v217\n" \
  "v_accvgpr_write_b32 a7, v225\n" \
  "v_accvgpr_read_b32 v222, a8\n" \
  "v_mul_f32 v222, v222, v218\n" \
  "v_accvgpr_write_b32 a8, v222\n" \
  "v_accvgpr_read_b32 v223, a9\n" \
  "v_mul_f32 v223, v223, v218\n" \
  "v_accvgpr_write_b32 a9, v223\n" \
  "v_accvgpr_read_b32 v224, a10\n" \
  "v_mul_f32 v224, v224, v218\n" \
  "v_accvgpr_write_b32 a10, v224\n" \
  "v_accvgpr_read_b32 v225, a11\n" \
  "v_mul_f32 v225, v225, v218\n" \
  "v_accvgpr_write_b32 a11, v225\n" \
  "v_accvgpr_read_b32 v222, a12\n" \
  "v_mul_f32 v222, v222, v219\n" \
  "v_accvgpr_write_b32 a12, v222\n" \
  "v_accvgpr_read_b32 v223, a13\n" \
  "v_mul_f32 v223, v223, v219\n" \
  "v_accvgpr_write_b32 a13, v223\n" \
  "v_accvgpr_read_b32 v224, a14\n" \
  "v_mul_f32 v224, v224, v219\n" \
  "v_accvgpr_write_b32 a14, v224\n" \
  "v_accvgpr_read_b32 v225, a15\n" \
  "v_mul_f32 v225, v225, v219\n" \
  "v_accvgpr_write_b32 a15, v225\n" \
  "v_accvgpr_read_b32 v222, a16\n" \
  "v_mul_f32 v222, v222, v216\n" \
  "v_accvgpr_write_b32 a16, v222\n" \
  "v_accvgpr_read_b32 v223, a17\n" \
  "v_mul_f32 v223, v223, v216\n" \
  "v_accvgpr_write_b32 a17, v223\n" \
  "v_accvgpr_read_b32 v224, a18\n" \
  "v_mul_f32 v224, v224, v216\n" \
  "v_accvgpr_write_b32 a18, v224\n" \
  "v_accvgpr_read_b32 v225, a19\n" \
  "v_mul_f32 v225, v225, v216\n" \
  "v_accvgpr_write_b32 a19, v225\n" \
  "v_accvgpr_read_b32 v222, a20\n" \
  "v_mul_f32 v222, v222, v217\n" \
  "v_accvgpr_write_b32 a20, v222\n" \
  "v_accvgpr_read_b32 v223, a21\n" \
  "v_mul_f32 v223, v223, v217\n" \
  "v_accvgpr_write_b32 a21, v223\n" \
  "v_accvgpr_read_b32 v224, a22\n" \
  "v_mul_f32 v224, v224, v217\n" \
  "v_accvgpr_write_b32 a22, v224\n" \
  "v_accvgpr_read_b32 v225, a23\n" \
  "v_mul_f32 v225, v225, v217\n" \
  "v_accvgpr_write_b32 a23, v225\n" \
  "v_accvgpr_read_b32 v222, a24\n" \
  "v_mul_f32 v222, v222, v218\n" \
  "v_accvgpr_write_b32 a24, v222\n" \
  "v_accvgpr_read_b32 v223, a25\n" \
  "v_mul_f32 v223, v223, v218\n" \
  "v_accvgpr_write_b32 a25, v223\n" \
  "v_accvgpr_read_b32 v224, a26\n" \
  "v_mul_f32 v224, v224, v218\n" \
  "v_accvgpr_write_b32 a26, v224\n" \
  "v_accvgpr_read_b32 v225, a27\n" \
  "v_mul_f32 v225, v225, v218\n" \
  "v_accvgpr_write_b32 a27, v225\n" \
  "v_accvgpr_read_b32 v222, a28\n" \
  "v_mul_f32 v222, v222, v219\n" \
  "v_accvgpr_write_b32 a28, v222\n" \
  "v_accvgpr_read_b32 v223, a29\n" \
  "v_mul_f32 v223, v223, v219\n" \
  "v_accvgpr_write_b32 a29, v223\n" \
  "v_accvgpr_read_b32 v224, a30\n" \
  "v_mul_f32 v224, v224, v219\n" \
  "v_accvgpr_write_b32 a30, v224\n" \
  "v_accvgpr_read_b32 v225, a31\n" \
  "v_mul_f32 v225, v225, v219\n" \
  "v_accvgpr_write_b32 a31, v225\n" \
  "v_accvgpr_read_b32 v222, a32\n" \
  "v_mul_f32 v222, v222, v216\n" \
  "v_accvgpr_write_b32 a32, v222\n" \
  "v_accvgpr_read_b32 v223, a33\n" \
  "v_mul_f32 v223, v223, v216\n" \
  "v_accvgpr_write_b32 a33, v223\n" \
  "v_accvgpr_read_b32 v224, a34\n" \
  "v_mul_f32 v224, v224, v216\n" \
  "v_accvgpr_write_b32 a34, v224\n" \
  "v_accvgpr_read_b32 v225, a35\n" \
  "v_mul_f32 v225, v225, v216\n" \
  "v_accvgpr_write_b32 a35, v225\n" \
  "v_accvgpr_read_b32 v222, a36\n" \
  "v_mul_f32 v222, v222, v217\n" \
  "v_accvgpr_write_b32 a36, v222\n" \
  "v_accvgpr_read_b32 v223, a37\n" \
  "v_mul_f32 v223, v223, v217\n" \
  "v_accvgpr_write_b32 a37, v223\n" \
  "v_accvgpr_read_b32 v224, a38\n" \
  "v_mul_f32 v224, v224, v217\n" \
  "v_accvgpr_write_b32 a38, v224\n" \
  "v_accvgpr_read_b32 v225, a39\n" \
  "v_mul_f32 v225, v225, v217\n" \
  "v_accvgpr_write_b32 a39, v225\n" \
  "v_accvgpr_read_b32 v222, a40\n" \
  "v_mul_f32 v222, v222, v218\n" \
  "v_accvgpr_write_b32 a40, v222\n" \
  "v_accvgpr_read_b32 v223, a41\n" \
  "v_mul_f32 v223, v223, v218\n" \
  "v_accvgpr_write_b32 a41, v223\n" \
  "v_accvgpr_read_b32 v224, a42\n" \
  "v_mul_f32 v224, v224, v218\n" \
  "v_accvgpr_write_b32 a42, v224\n" \
  "v_accvgpr_read_b32 v225, a43\n" \
  "v_mul_f32 v225, v225, v218\n" \
  "v_accvgpr_write_b32 a43, v225\n" \
  "v_accvgpr_read_b32 v222, a44\n" \
  "v_mul_f32 v222, v222, v219\n" \
  "v_accvgpr_write_b32 a44, v222\n" \
  "v_accvgpr_read_b32 v223, a45\n" \
  "v_mul_f32 v223, v223, v219\n" \
  "v_accvgpr_write_b32 a45, v223\n" \
  "v_accvgpr_read_b32 v224, a46\n" \
  "v_mul_f32 v224, v224, v219\n" \
  "v_accvgpr_write_b32 a46, v224\n" \
  "v_accvgpr_read_b32 v225, a47\n" \
  "v_mul_f32 v225, v225, v219\n" \
  "v_accvgpr_write_b32 a47, v225\n" \
  "v_accvgpr_read_b32 v222, a48\n" \
  "v_mul_f32 v222, v222, v216\n" \
  "v_accvgpr_write_b32 a48, v222\n" \
  "v_accvgpr_read_b32 v223, a49\n" \
  "v_mul_f32 v223, v223, v216\n" \
  "v_accvgpr_write_b32 a49, v223\n" \
  "v_accvgpr_read_b32 v224, a50\n" \
  "v_mul_f32 v224, v224, v216\n" \
  "v_accvgpr_write_b32 a50, v224\n" \
  "v_accvgpr_read_b32 v225, a51\n" \
  "v_mul_f32 v225, v225, v216\n" \
  "v_accvgpr_write_b32 a51, v225\n" \
  "v_accvgpr_read_b32 v222, a52\n" \
  "v_mul_f32 v222, v222, v217\n" \
  "v_accvgpr_write_b32 a52, v222\n" \
  "v_accvgpr_read_b32 v223, a53\n" \
  "v_mul_f32 v223, v223, v217\n" \
  "v_accvgpr_write_b32 a53, v223\n" \
  "v_accvgpr_read_b32 v224, a54\n" \
  "v_mul_f32 v224, v224, v217\n" \
  "v_accvgpr_write_b32 a54, v224\n" \
  "v_accvgpr_read_b32 v225, a55\n" \
  "v_mul_f32 v225, v225, v217\n" \
  "v_accvgpr_write_b32 a55, v225\n" \
  "v_accvgpr_read_b32 v222, a56\n" \
  "v_mul_f32 v222, v222, v218\n" \
  "v_accvgpr_write_b32 a56, v222\n" \
  "v_accvgpr_read_b32 v223, a57\n" \
  "v_mul_f32 v223, v223, v218\n" \
  "v_accvgpr_write_b32 a57, v223\n" \
  "v_accvgpr_read_b32 v224, a58\n" \
  "v_mul_f32 v224, v224, v218\n" \
  "v_accvgpr_write_b32 a58, v224\n" \
  "v_accvgpr_read_b32 v225, a59\n" \
  "v_mul_f32 v225, v225, v218\n" \
  "v_accvgpr_write_b32 a59, v225\n" \
  "v_accvgpr_read_b32 v222, a60\n" \
  "v_mul_f32 v222, v222, v219\n" \
  "v_accvgpr_write_b32 a60, v222\n" \
  "v_accvgpr_read_b32 v223, a61\n" \
  "v_mul_f32 v223, v223, v219\n" \
  "v_accvgpr_write_b32 a61, v223\n" \
  "v_accvgpr_read_b32 v224, a62\n" \
  "v_mul_f32 v224, v224, v219\n" \
  "v_accvgpr_write_b32 a62, v224\n" \
  "v_accvgpr_read_b32 v225, a63\n" \
  "v_mul_f32 v225, v225, v219\n" \
  "v_accvgpr_write_b32 a63, v225\n" \
  "v_accvgpr_read_b32 v222, a64\n" \
  "v_mul_f32 v222, v222, v216\n" \
  "v_accvgpr_write_b32 a64, v222\n" \
  "v_accvgpr_read_b32 v223, a65\n" \
  "v_mul_f32 v223, v223, v216\n" \
  "v_accvgpr_write_b32 a65, v223\n" \
  "v_accvgpr_read_b32 v224, a66\n" \
  "v_mul_f32 v224, v224, v216\n" \
  "v_accvgpr_write_b32 a66, v224\n" \
  "v_accvgpr_read_b32 v225, a67\n" \
  "v_mul_f32 v225, v225, v216\n" \
  "v_accvgpr_write_b32 a67, v225\n" \
  "v_accvgpr_read_b32 v222, a68\n" \
  "v_mul_f32 v222, v222, v217\n" \
  "v_accvgpr_write_b32 a68, v222\n" \
  "v_accvgpr_read_b32 v223, a69\n" \
  "v_mul_f32 v223, v223, v217\n" \
  "v_accvgpr_write_b32 a69, v223\n" \
  "v_accvgpr_read_b32 v224, a70\n" \
  "v_mul_f32 v224, v224, v217\n" \
  "v_accvgpr_write_b32 a70, v224\n" \
  "v_accvgpr_read_b32 v225, a71\n" \
  "v_mul_f32 v225, v225, v217\n" \
  "v_accvgpr_write_b32 a71, v225\n" \
  "v_accvgpr_read_b32 v222, a72\n" \
  "v_mul_f32 v222, v222, v218\n" \
  "v_accvgpr_write_b32 a72, v222\n" \
  "v_accvgpr_read_b32 v223, a73\n" \
  "v_mul_f32 v223, v223, v218\n" \
  "v_accvgpr_write_b32 a73, v223\n" \
  "v_accvgpr_read_b32 v224, a74\n" \
  "v_mul_f32 v224, v224, v218\n" \
  "v_accvgpr_write_b32 a74, v224\n" \
  "v_accvgpr_read_b32 v225, a75\n" \
  "v_mul_f32 v225, v225, v218\n" \
  "v_accvgpr_write_b32 a75, v225\n" \
  "v_accvgpr_read_b32 v222, a76\n" \
  "v_mul_f32 v222, v222, v219\n" \
  "v_accvgpr_write_b32 a76, v222\n" \
  "v_accvgpr_read_b32 v223, a77\n" \
  "v_mul_f32 v223, v223, v219\n" \
  "v_accvgpr_write_b32 a77, v223\n" \
  "v_accvgpr_read_b32 v224, a78\n" \
  "v_mul_f32 v224, v224, v219\n" \
  "v_accvgpr_write_b32 a78, v224\n" \
  "v_accvgpr_read_b32 v225, a79\n" \
  "v_mul_f32 v225, v225, v219\n" \
  "v_accvgpr_write_b32 a79, v225\n" \
  "v_accvgpr_read_b32 v222, a80\n" \
  "v_mul_f32 v222, v222, v216\n" \
  "v_accvgpr_write_b32 a80, v222\n" \
  "v_accvgpr_read_b32 v223, a81\n" \
  "v_mul_f32 v223, v223, v216\n" \
  "v_accvgpr_write_b32 a81, v223\n" \
  "v_accvgpr_read_b32 v224, a82\n" \
  "v_mul_f32 v224, v224, v216\n" \
  "v_accvgpr_write_b32 a82, v224\n" \
  "v_accvgpr_read_b32 v225, a83\n" \
  "v_mul_f32 v225, v225, v216\n" \
  "v_accvgpr_write_b32 a83, v225\n" \
  "v_accvgpr_read_b32 v222, a84\n" \
  "v_mul_f32 v222, v222, v217\n" \
  "v_accvgpr_write_b32 a84, v222\n" \
  "v_accvgpr_read_b32 v223, a85\n" \
  "v_mul_f32 v223, v223, v217\n" \
  "v_accvgpr_write_b32 a85, v223\n" \
  "v_accvgpr_read_b32 v224, a86\n" \
  "v_mul_f32 v224, v224, v217\n" \
  "v_accvgpr_write_b32 a86, v224\n" \
  "v_accvgpr_read_b32 v225, a87\n" \
  "v_mul_f32 v225, v225, v217\n" \
  "v_accvgpr_write_b32 a87, v225\n" \
  "v_accvgpr_read_b32 v222, a88\n" \
  "v_mul_f32 v222, v222, v218\n" \
  "v_accvgpr_write_b32 a88, v222\n" \
  "v_accvgpr_read_b32 v223, a89\n" \
  "v_mul_f32 v223, v223, v218\n" \
  "v_accvgpr_write_b32 a89, v223\n" \
  "v_accvgpr_read_b32 v224, a90\n" \
  "v_mul_f32 v224, v224, v218\n" \
  "v_accvgpr_write_b32 a90, v224\n" \
  "v_accvgpr_read_b32 v225, a91\n" \
  "v_mul_f32 v225, v225, v218\n" \
  "v_accvgpr_write_b32 a91, v225\n" \
  "v_accvgpr_read_b32 v222, a92\n" \
  "v_mul_f32 v222, v222, v219\n" \
  "v_accvgpr_write_b32 a92, v222\n" \
  "v_accvgpr_read_b32 v223, a93\n" \
  "v_mul_f32 v223, v223, v219\n" \
  "v_accvgpr_write_b32 a93, v223\n" \
  "v_accvgpr_read_b32 v224, a94\n" \
  "v_mul_f32 v224, v224, v219\n" \
  "v_accvgpr_write_b32 a94, v224\n" \
  "v_accvgpr_read_b32 v225, a95\n" \
  "v_mul_f32 v225, v225, v219\n" \
  "v_accvgpr_write_b32 a95, v225\n" \
  "v_accvgpr_read_b32 v222, a96\n" \
  "v_mul_f32 v222, v222, v216\n" \
  "v_accvgpr_write_b32 a96, v222\n" \
  "v_accvgpr_read_b32 v223, a97\n" \
  "v_mul_f32 v223, v223, v216\n" \
  "v_accvgpr_write_b32 a97, v223\n" \
  "v_accvgpr_read_b32 v224, a98\n" \
  "v_mul_f32 v224, v224, v216\n" \
  "v_accvgpr_write_b32 a98, v224\n" \
  "v_accvgpr_read_b32 v225, a99\n" \
  "v_mul_f32 v225, v225, v216\n" \
  "v_accvgpr_write_b32 a99, v225\n" \
  "v_accvgpr_read_b32 v222, a100\n" \
  "v_mul_f32 v222, v222, v217\n" \
  "v_accvgpr_write_b32 a100, v222\n" \
  "v_accvgpr_read_b32 v223, a101\n" \
  "v_mul_f32 v223, v223, v217\n" \
  "v_accvgpr_write_b32 a101, v223\n" \
  "v_accvgpr_read_b32 v224, a102\n" \
  "v_mul_f32 v224, v224, v217\n" \
  "v_accvgpr_write_b32 a102, v224\n" \
  "v_accvgpr_read_b32 v225, a103\n" \
  "v_mul_f32 v225, v225, v217\n" \
  "v_accvgpr_write_b32 a103, v225\n" \
  "v_accvgpr_read_b32 v222, a104\n" \
  "v_mul_f32 v222, v222, v218\n" \
  "v_accvgpr_write_b32 a104, v222\n" \
  "v_accvgpr_read_b32 v223, a105\n" \
  "v_mul_f32 v223, v223, v218\n" \
  "v_accvgpr_write_b32 a105, v223\n" \
  "v_accvgpr_read_b32 v224, a106\n" \
  "v_mul_f32 v224, v224, v218\n" \
  "v_accvgpr_write_b32 a106, v224\n" \
  "v_accvgpr_read_b32 v225, a107\n" \
  "v_mul_f32 v225, v225, v218\n" \
  "v_accvgpr_write_b32 a107, v225\n" \
  "v_accvgpr_read_b32 v222, a108\n" \
  "v_mul_f32 v222, v222, v219\n" \
  "v_accvgpr_write_b32 a108, v222\n" \
  "v_accvgpr_read_b32 v223, a109\n" \
  "v_mul_f32 v223, v223, v219\n" \
  "v_accvgpr_write_b32 a109, v223\n" \
  "v_accvgpr_read_b32 v224, a110\n" \
  "v_mul_f32 v224, v224, v219\n" \
  "v_accvgpr_write_b32 a110, v224\n" \
  "v_accvgpr_read_b32 v225, a111\n" \
  "v_mul_f32 v225, v225, v219\n" \
  "v_accvgpr_write_b32 a111, v225\n" \
  "v_accvgpr_read_b32 v222, a112\n" \
  "v_mul_f32 v222, v222, v216\n" \
  "v_accvgpr_write_b32 a112, v222\n" \
  "v_accvgpr_read_b32 v223, a113\n" \
  "v_mul_f32 v223, v223, v216\n" \
  "v_accvgpr_write_b32 a113, v223\n" \
  "v_accvgpr_read_b32 v224, a114\n" \
  "v_mul_f32 v224, v224, v216\n" \
  "v_accvgpr_write_b32 a114, v224\n" \
  "v_accvgpr_read_b32 v225, a115\n" \
  "v_mul_f32 v225, v225, v216\n" \
  "v_accvgpr_write_b32 a115, v225\n" \
  "v_accvgpr_read_b32 v222, a116\n" \
  "v_mul_f32 v222, v222, v217\n" \
  "v_accvgpr_write_b32 a116, v222\n" \
  "v_accvgpr_read_b32 v223, a117\n" \
  "v_mul_f32 v223, v223, v217\n" \
  "v_accvgpr_write_b32 a117, v223\n" \
  "v_accvgpr_read_b32 v224, a118\n" \
  "v_mul_f32 v224, v224, v217\n" \
  "v_accvgpr_write_b32 a118, v224\n" \
  "v_accvgpr_read_b32 v225, a119\n" \
  "v_mul_f32 v225, v225, v217\n" \
  "v_accvgpr_write_b32 a119, v225\n" \
  "v_accvgpr_read_b32 v222, a120\n" \
  "v_mul_f32 v222, v222, v218\n" \
  "v_accvgpr_write_b32 a120, v222\n" \
  "v_accvgpr_read_b32 v223, a121\n" \
  "v_mul_f32 v223, v223, v218\n" \
  "v_accvgpr_write_b32 a121, v223\n" \
  "v_accvgpr_read_b32 v224, a122\n" \
  "v_mul_f32 v224, v224, v218\n" \
  "v_accvgpr_write_b32 a122, v224\n" \
  "v_accvgpr_read_b32 v225, a123\n" \
  "v_mul_f32 v225, v225, v218\n" \
  "v_accvgpr_write_b32 a123, v225\n" \
  "v_accvgpr_read_b32 v222, a124\n" \
  "v_mul_f32 v222, v222, v219\n" \
  "v_accvgpr_write_b32 a124, v222\n" \
  "v_accvgpr_read_b32 v223, a125\n" \
  "v_mul_f32 v223, v223, v219\n" \
  "v_accvgpr_write_b32 a125, v223\n" \
  "v_accvgpr_read_b32 v224, a126\n" \
  "v_mul_f32 v224, v224, v219\n" \
  "v_accvgpr_write_b32 a126, v224\n" \
  "v_accvgpr_read_b32 v225, a127\n" \
  "v_mul_f32 v225, v225, v219\n" \
  "v_accvgpr_write_b32 a127, v225\n" \
  "v_sub_f32 v32, v32, v120\n" \
  "v_sub_f32 v33, v33, v120\n" \
  "v_sub_f32 v34, v34, v120\n" \
  "v_sub_f32 v35, v35, v120\n" \
  "v_sub_f32 v48, v48, v120\n" \
  "v_sub_f32 v49, v49, v120\n" \
  "v_sub_f32 v50, v50, v120\n" \
  "v_sub_f32 v51, v51, v120\n" \
  "v_exp_f32 v32, v32\n" \
  "v_exp_f32 v33, v33\n" \
  "v_exp_f32 v34, v34\n" \
  "v_exp_f32 v35, v35\n" \
  "v_exp_f32 v48, v48\n" \
  "v_exp_f32 v49, v49\n" \
  "v_exp_f32 v50, v50\n" \
  "v_exp_f32 v51, v51\n" \
  "s_nop 0\n" \
  "v_cvt_pk_bf16_f32 v80, v32, v33\n" \
  "v_cvt_pk_bf16_f32 v81, v34, v35\n" \
  "v_cvt_pk_bf16_f32 v82, v48, v49\n" \
  "v_cvt_pk_bf16_f32 v83, v50, v51\n" \
  "v_sub_f32 v36, v36, v121\n" \
  "v_sub_f32 v37, v37, v121\n" \
  "v_sub_f32 v38, v38, v121\n" \
  "v_sub_f32 v39, v39, v121\n" \
  "v_sub_f32 v52, v52, v121\n" \
  "v_sub_f32 v53, v53, v121\n" \
  "v_sub_f32 v54, v54, v121\n" \
  "v_sub_f32 v55, v55, v121\n" \
  "v_exp_f32 v36, v36\n" \
  "v_exp_f32 v37, v37\n" \
  "v_exp_f32 v38, v38\n" \
  "v_exp_f32 v39, v39\n" \
  "v_exp_f32 v52, v52\n" \
  "v_exp_f32 v53, v53\n" \
  "v_exp_f32 v54, v54\n" \
  "v_exp_f32 v55, v55\n" \
  "s_nop 0\n" \
  "v_cvt_pk_bf16_f32 v84, v36, v37\n" \
  "v_cvt_pk_bf16_f32 v85, v38, v39\n" \
  "v_cvt_pk_bf16_f32 v86, v52, v53\n" \
  "v_cvt_pk_bf16_f32 v87, v54, v55\n" \
  "v_sub_f32 v40, v40, v122\n" \
  "v_sub_f32 v41, v41, v122\n" \
  "v_sub_f32 v42, v42, v122\n" \
  "v_sub_f32 v43, v43, v122\n" \
  "v_sub_f32 v56, v56, v122\n" \
  "v_sub_f32 v57, v57, v122\n" \
  "v_sub_f32 v58, v58, v122\n" \
  "v_sub_f32 v59, v59, v122\n" \
  "v_exp_f32 v40, v40\n" \
  "v_exp_f32 v41, v41\n" \
  "v_exp_f32 v42, v42\n" \
  "v_exp_f32 v43, v43\n" \
  "v_exp_f32 v56, v56\n" \
  "v_exp_f32 v57, v57\n" \
  "v_exp_f32 v58, v58\n" \
  "v_exp_f32 v59, v59\n" \
  "s_nop 0\n" \
  "v_cvt_pk_bf16_f32 v88, v40, v41\n" \
  "v_cvt_pk_bf16_f32 v89, v42, v43\n" \
  "v_cvt_pk_bf16_f32 v90, v56, v57\n" \
  "v_cvt_pk_bf16_f32 v91, v58, v59\n" \
  "v_sub_f32 v44, v44, v123\n" \
  "v_sub_f32 v45, v45, v123\n" \
  "v_sub_f32 v46, v46, v123\n" \
  "v_sub_f32 v47, v47, v123\n" \
  "v_sub_f32 v60, v60, v123\n" \
  "v_sub_f32 v61, v61, v123\n" \
  "v_sub_f32 v62, v62, v123\n" \
  "v_sub_f32 v63, v63, v123\n" \
  "v_exp_f32 v44, v44\n" \
  "v_exp_f32 v45, v45\n" \
  "v_exp_f32 v46, v46\n" \
  "v_exp_f32 v47, v47\n" \
  "v_exp_f32 v60, v60\n" \
  "v_exp_f32 v61, v61\n" \
  "v_exp_f32 v62, v62\n" \
  "v_exp_f32 v63, v63\n" \
  "s_nop 0\n" \
  "v_cvt_pk_bf16_f32 v92, v44, v45\n" \
  "v_cvt_pk_bf16_f32 v93, v46, v47\n" \
  "v_cvt_pk_bf16_f32 v94, v60, v61\n" \
  "v_cvt_pk_bf16_f32 v95, v62, v63\n" \
  "s_nop 1\n" \
  "v_mfma_f32_16x16x32_bf16 v[240:243], v[124:127], v[80:83], 0\n" \
  "v_mfma_f32_16x16x32_bf16 v[244:247], v[124:127], v[84:87], 0\n" \
  "v_mfma_f32_16x16x32_bf16 v[248:251], v[124:127], v[88:91], 0\n" \
  "v_mfma_f32_16x16x32_bf16 v[252:255], v[124:127], v[92:95], 0\n" \
  "s_nop 7\n" \
  "s_nop 3\n" \
  "s_waitcnt lgkmcnt(0)\n" \
  "s_nop 1\n" \
  "v_mfma_f32_16x16x32_bf16 v[0:3], v[160:163], a[128:131], v[96:99]\n" \
  "v_mfma_f32_16x16x32_bf16 v[0:3], v[164:167], a[132:135], v[0:3]\n" \
  "v_mfma_f32_16x16x32_bf16 v[0:3], v[168:171], a[136:139], v[0:3]\n" \
  "v_mfma_f32_16x16x32_bf16 v[0:3], v[172:175], a[140:143], v[0:3]\n" \
  "v_mfma_f32_16x16x32_bf16 v[4:7], v[160:163], a[144:147], v[100:103]\n" \
  "v_mfma_f32_16x16x32_bf16 v[4:7], v[164:167], a[148:151], v[4:7]\n" \
  "v_mfma_f32_16x16x32_bf16 v[4:7], v[168:171], a[152:155], v[4:7]\n" \
  "v_mfma_f32_16x16x32_bf16 v[4:7], v[172:175], a[156:159], v[4:7]\n" \
  "v_mfma_f32_16x16x32_bf16 v[8:11], v[160:163], a[160:163], v[104:107]\n" \
  "v_mfma_f32_16x16x32_bf16 v[8:11], v[164:167], a[164:167], v[8:11]\n" \
  "v_mfma_f32_16x16x32_bf16 v[8:11], v[168:171], a[168:171], v[8:11]\n" \
  "v_mfma_f32_16x16x32_bf16 v[8:11], v[172:175], a[172:175], v[8:11]\n" \
  "v_mfma_f32_16x16x32_bf16 v[12:15], v[160:163], a[176:179], v[108:111]\n" \
  "v_mfma_f32_16x16x32_bf16 v[12:15], v[164:167], a[180:183], v[12:15]\n" \
  "v_mfma_f32_16x16x32_bf16 v[12:15], v[168:171], a[184:187], v[12:15]\n" \
  "v_mfma_f32_16x16x32_bf16 v[12:15], v[172:175], a[188:191], v[12:15]\n" \
  "v_mfma_f32_16x16x32_bf16 v[16:19], v[176:179], a[128:131], v[96:99]\n" \
  "v_mfma_f32_16x16x32_bf16 v[16:19], v[180:183], a[132:135], v[16:19]\n" \
  "v_mfma_f32_16x16x32_bf16 v[16:19], v[184:187], a[136:139], v[16:19]\n" \
  "v_mfma_f32_16x16x32_bf16 v[16:19], v[188:191], a[140:143], v[16:19]\n" \
  "v_mfma_f32_16x16x32_bf16 v[20:23], v[176:179], a[144:147], v[100:103]\n" \
  "v_mfma_f32_16x16x32_bf16 v[20:23], v[180:183], a[148:151], v[20:23]\n" \
  "v_mfma_f32_16x16x32_bf16 v[20:23], v[184:187], a[152:155], v[20:23]\n" \
  "v_mfma_f32_16x16x32_bf16 v[20:23], v[188:191], a[156:159], v[20:23]\n" \
  "v_mfma_f32_16x16x32_bf16 v[24:27], v[176:179], a[160:163], v[104:107]\n" \
  "v_mfma_f32_16x16x32_bf16 v[24:27], v[180:183], a[164:167], v[24:27]\n" \
  "v_mfma_f32_16x16x32_bf16 v[24:27], v[184:187], a[168:171], v[24:27]\n" \
  "v_mfma_f32_16x16x32_bf16 v[24:27], v[188:191], a[172:175], v[24:27]\n" \
  "v_mfma_f32_16x16x32_bf16 v[28:31], v[176:179], a[176:179], v[108:111]\n" \
  "v_mfma_f32_16x16x32_bf16 v[28:31], v[180:183], a[180:183], v[28:31]\n" \
  "v_mfma_f32_16x16x32_bf16 v[28:31], v[184:187], a[184:187], v[28:31]\n" \
  "v_mfma_f32_16x16x32_bf16 v[28:31], v[188:191], a[188:191], v[28:31]\n" \
  "s_nop 7\n" \
  "s_nop 7\n" \
  "s_add_u32 s60, s60, 4\n" \
  "s_addc_u32 s61, s61, 0\n" \
  "s_setpc_b64 s[60:61]\n" \
  "END%=:\n" \
  "CEND%=:\n" \
  ""

#define FIN_ASM \
  "CSTART%=:\n" \
  "s_getpc_b64 s[78:79]\n" \
  "v_mbcnt_lo_u32_b32 v172, -1, 0\n" \
  "v_mbcnt_hi_u32_b32 v172, -1, v172\n" \
  "s_lshl_b32 s77, %16, 6\n" \
  "v_add_u32 v172, s77, v172\n" \
  "v_lshlrev_b32 v172, 7, v172\n" \
  "s_mov_b32 s77, CEND%=-CSTART%=-8\n" \
  "v_min_u32 v173, s77, v172\n" \
  "global_load_dword v173, v173, s[78:79]\n" \
  "s_mov_b32 s40, %0\n" \
  "s_mov_b32 s41, %1\n" \
  "s_mov_b32 s42, 16777216\n" \
  "s_mov_b32 s43, 0x20000\n" \
  "s_mov_b32 s44, %2\n" \
  "s_mov_b32 s45, %3\n" \
  "s_mov_b32 s46, %4\n" \
  "s_mov_b32 s47, 0x20000\n" \
  "s_mov_b32 s48, %5\n" \
  "s_mov_b32 s49, %6\n" \
  "s_mov_b32 s50, 7340032\n" \
  "s_mov_b32 s51, 0x20000\n" \
  "s_mov_b32 s52, %7\n" \
  "s_mov_b32 s53, %8\n" \
  "s_mov_b32 s54, %9\n" \
  "s_mov_b32 s55, 0x20000\n" \
  "s_lshl_b32 s72, %16, 12\n" \
  "s_mov_b32 s74, %17\n" \
  "s_lshl_b32 s73, %16, 5\n" \
  "s_add_u32 s73, s73, %18\n" \
  "s_lshl_b32 s68, s73, 13\n" \
  "s_mul_i32 s70, s73, 3584\n" \
  "s_lshl_b32 s69, %16, 14\n" \
  "s_lshl_b32 s71, %16, 14\n" \
  "s_mul_i32 s73, s74, 47104\n" \
  "s_add_u32 s68, s68, s73\n" \
  "s_mul_i32 s73, s74, 1507328\n" \
  "s_add_u32 s69, s69, s73\n" \
  "v_mbcnt_lo_u32_b32 v160, -1, 0\n" \
  "v_mbcnt_hi_u32_b32 v160, -1, v160\n" \
  "v_and_b32 v161, 15, v160\n" \
  "v_lshrrev_b32 v162, 4, v160\n" \
  "v_lshlrev_b32 v132, 4, v160\n" \
  "v_add_u32 v133, 131072, v132\n" \
  "v_mov_b32 v134, v132\n" \
  "v_add_u32 v135, 57344, v132\n" \
  "v_bfe_u32 v163, v160, 2, 2\n" \
  "v_and_b32 v164, 3, v160\n" \
  "v_lshlrev_b32 v165, 10, v163\n" \
  "v_lshl_add_u32 v165, v162, 6, v165\n" \
  "v_xor_b32 v166, 0, v164\n" \
  "v_lshl_add_u32 v136, v166, 4, v165\n" \
  "v_add_u32 v136, 0, v136\n" \
  "v_xor_b32 v166, 0, v164\n" \
  "v_lshl_add_u32 v137, v166, 4, v165\n" \
  "v_add_u32 v137, 3072, v137\n" \
  "v_xor_b32 v166, 2, v164\n" \
  "v_lshl_add_u32 v138, v166, 4, v165\n" \
  "v_add_u32 v138, 6144, v138\n" \
  "v_xor_b32 v166, 2, v164\n" \
  "v_lshl_add_u32 v139, v166, 4, v165\n" \
  "v_add_u32 v139, 9216, v139\n" \
  "s_cmp_lg_u32 s74, 3\n" \
  "s_cbranch_scc1 NS3%=\n" \
  "s_mov_b32 s40, s48\n" \
  "s_mov_b32 s44, s52\n" \
  "s_mov_b32 s41, s49\n" \
  "s_mov_b32 s45, s53\n" \
  "s_mov_b32 s42, s50\n" \
  "s_mov_b32 s46, s54\n" \
  "s_mov_b32 s43, s51\n" \
  "s_mov_b32 s47, s55\n" \
  "s_add_u32 s68, s70, 10240\n" \
  "s_add_u32 s69, s71, 327680\n" \
  "v_mov_b32 v132, v134\n" \
  "v_mov_b32 v133, v135\n" \
  "NS3%=:\n" \
  "s_add_u32 m0, s72, 0\n" \
  "s_nop 0\n" \
  "buffer_load_dwordx4 v136, s[44:47], s69 offen lds\n" \
  "buffer_load_dwordx4 v137, s[44:47], s69 offen offset:1024 lds\n" \
  "buffer_load_dwordx4 v138, s[44:47], s69 offen offset:2048 lds\n" \
  "buffer_load_dwordx4 v139, s[44:47], s69 offen offset:3072 lds\n" \
  "s_add_u32 s69, s69, 0x10000\n" \
  "buffer_load_dwordx4 a[128:131], v132, s[40:43], s68 offen offset:0\n" \
  "buffer_load_dwordx4 a[136:139], v133, s[40:43], s68 offen offset:0\n" \
  "buffer_load_dwordx4 a[132:135], v132, s[40:43], s68 offen offset:1024\n" \
  "buffer_load_dwordx4 a[140:143], v133, s[40:43], s68 offen offset:1024\n" \
  "s_add_u32 s68, s68, 2048\n" \
  "s_add_u32 m0, s72, 16384\n" \
  "s_nop 0\n" \
  "buffer_load_dwordx4 v136, s[44:47], s69 offen lds\n" \
  "buffer_load_dwordx4 v137, s[44:47], s69 offen offset:1024 lds\n" \
  "buffer_load_dwordx4 v138, s[44:47], s69 offen offset:2048 lds\n" \
  "buffer_load_dwordx4 v139, s[44:47], s69 offen offset:3072 lds\n" \
  "s_add_u32 s69, s69, 0x10000\n" \
  "buffer_load_dwordx4 a[144:147], v132, s[40:43], s68 offen offset:0\n" \
  "buffer_load_dwordx4 a[152:155], v133, s[40:43], s68 offen offset:0\n" \
  "buffer_load_dwordx4 a[148:151], v132, s[40:43], s68 offen offset:1024\n" \
  "buffer_load_dwordx4 a[156:159], v133, s[40:43], s68 offen offset:1024\n" \
  "s_add_u32 s68, s68, 2048\n" \
  "s_add_u32 m0, s72, 32768\n" \
  "s_nop 0\n" \
  "buffer_load_dwordx4 v136, s[44:47], s69 offen lds\n" \
  "buffer_load_dwordx4 v137, s[44:47], s69 offen offset:1024 lds\n" \
  "buffer_load_dwordx4 v138, s[44:47], s69 offen offset:2048 lds\n" \
  "buffer_load_dwordx4 v139, s[44:47], s69 offen offset:3072 lds\n" \
  "s_add_u32 s69, s69, 0x10000\n" \
  "buffer_load_dwordx4 a[160:163], v132, s[40:43], s68 offen offset:0\n" \
  "buffer_load_dwordx4 a[168:171], v133, s[40:43], s68 offen offset:0\n" \
  "buffer_load_dwordx4 a[164:167], v132, s[40:43], s68 offen offset:1024\n" \
  "buffer_load_dwordx4 a[172:175], v133, s[40:43], s68 offen offset:1024\n" \
  "s_add_u32 s68, s68, 2048\n" \
  "s_mov_b32 s56, %10\n" \
  "s_mov_b32 s57, %11\n" \
  "s_mov_b32 s58, 163840\n" \
  "s_mov_b32 s59, 0x20000\n" \
  "s_mov_b32 s60, %12\n" \
  "s_mov_b32 s61, %13\n" \
  "s_mov_b32 s62, 2097152\n" \
  "s_mov_b32 s63, 0x20000\n" \
  "s_mov_b32 s64, %14\n" \
  "s_mov_b32 s65, %15\n" \
  "s_mov_b32 s66, 2097152\n" \
  "s_mov_b32 s67, 0x20000\n" \
  "v_lshlrev_b32 v165, 6, v163\n" \
  "v_lshl_add_u32 v165, v164, 3, v165\n" \
  "v_lshl_add_u32 v165, v162, 11, v165\n" \
  "v_and_b32 v166, 1, v162\n" \
  "v_xor_b32 v128, 0, v166\n" \
  "v_lshl_add_u32 v128, v128, 5, v165\n" \
  "v_add_u32 v130, 0x10000, v128\n" \
  "v_xor_b32 v129, 1, v166\n" \
  "v_lshl_add_u32 v129, v129, 5, v165\n" \
  "v_add_u32 v131, 0x10000, v129\n" \
  "s_waitcnt vmcnt(20) lgkmcnt(0)\n" \
  "s_barrier\n" \
  "ds_read_b64_tr_b16 v[0:1], v128 offset:0\n" \
  "ds_read_b64_tr_b16 v[2:3], v128 offset:1024\n" \
  "ds_read_b64_tr_b16 v[8:9], v129 offset:0\n" \
  "ds_read_b64_tr_b16 v[10:11], v129 offset:1024\n" \
  "ds_read_b64_tr_b16 v[16:17], v128 offset:256\n" \
  "ds_read_b64_tr_b16 v[18:19], v128 offset:1280\n" \
  "ds_read_b64_tr_b16 v[24:25], v129 offset:256\n" \
  "ds_read_b64_tr_b16 v[26:27], v129 offset:1280\n" \
  "ds_read_b64_tr_b16 v[32:33], v128 offset:512\n" \
  "ds_read_b64_tr_b16 v[34:35], v128 offset:1536\n" \
  "ds_read_b64_tr_b16 v[40:41], v129 offset:512\n" \
  "ds_read_b64_tr_b16 v[42:43], v129 offset:1536\n" \
  "ds_read_b64_tr_b16 v[48:49], v128 offset:768\n" \
  "ds_read_b64_tr_b16 v[50:51], v128 offset:1792\n" \
  "ds_read_b64_tr_b16 v[56:57], v129 offset:768\n" \
  "ds_read_b64_tr_b16 v[58:59], v129 offset:1792\n" \
  "ds_read_b64_tr_b16 v[4:5], v128 offset:8192\n" \
  "ds_read_b64_tr_b16 v[6:7], v128 offset:9216\n" \
  "ds_read_b64_tr_b16 v[12:13], v129 offset:8192\n" \
  "ds_read_b64_tr_b16 v[14:15], v129 offset:9216\n" \
  "ds_read_b64_tr_b16 v[20:21], v128 offset:8448\n" \
  "ds_read_b64_tr_b16 v[22:23], v128 offset:9472\n" \
  "ds_read_b64_tr_b16 v[28:29], v129 offset:8448\n" \
  "ds_read_b64_tr_b16 v[30:31], v129 offset:9472\n" \
  "ds_read_b64_tr_b16 v[36:37], v128 offset:8704\n" \
  "ds_read_b64_tr_b16 v[38:39], v128 offset:9728\n" \
  "ds_read_b64_tr_b16 v[44:45], v129 offset:8704\n" \
  "ds_read_b64_tr_b16 v[46:47], v129 offset:9728\n" \
  "ds_read_b64_tr_b16 v[52:53], v128 offset:8960\n" \
  "ds_read_b64_tr_b16 v[54:55], v128 offset:9984\n" \
  "ds_read_b64_tr_b16 v[60:61], v129 offset:8960\n" \
  "ds_read_b64_tr_b16 v[62:63], v129 offset:9984\n" \
  "s_add_u32 m0, s72, 49152\n" \
  "s_nop 0\n" \
  "buffer_load_dwordx4 v136, s[44:47], s69 offen lds\n" \
  "buffer_load_dwordx4 v137, s[44:47], s69 offen offset:1024 lds\n" \
  "buffer_load_dwordx4 v138, s[44:47], s69 offen offset:2048 lds\n" \
  "buffer_load_dwordx4 v139, s[44:47], s69 offen offset:3072 lds\n" \
  "s_add_u32 s69, s69, 0x10000\n" \
  "buffer_load_dwordx4 a[176:179], v132, s[40:43], s68 offen offset:0\n" \
  "buffer_load_dwordx4 a[184:187], v133, s[40:43], s68 offen offset:0\n" \
  "buffer_load_dwordx4 a[180:183], v132, s[40:43], s68 offen offset:1024\n" \
  "buffer_load_dwordx4 a[188:191], v133, s[40:43], s68 offen offset:1024\n" \
  "s_add_u32 s68, s68, 2048\n" \
  "s_add_u32 m0, s72, 65536\n" \
  "s_nop 0\n" \
  "buffer_load_dwordx4 v136, s[44:47], s69 offen lds\n" \
  "buffer_load_dwordx4 v137, s[44:47], s69 offen offset:1024 lds\n" \
  "buffer_load_dwordx4 v138, s[44:47], s69 offen offset:2048 lds\n" \
  "buffer_load_dwordx4 v139, s[44:47], s69 offen offset:3072 lds\n" \
  "s_add_u32 s69, s69, 0x10000\n" \
  "buffer_load_dwordx4 a[192:195], v132, s[40:43], s68 offen offset:0\n" \
  "buffer_load_dwordx4 a[200:203], v133, s[40:43], s68 offen offset:0\n" \
  "buffer_load_dwordx4 a[196:199], v132, s[40:43], s68 offen offset:1024\n" \
  "buffer_load_dwordx4 a[204:207], v133, s[40:43], s68 offen offset:1024\n" \
  "s_add_u32 s68, s68, 2048\n" \
  "s_waitcnt vmcnt(28) lgkmcnt(0)\n" \
  "s_barrier\n" \
  "v_mfma_f32_16x16x32_bf16 a[0:3], v[0:3], a[128:131], 0\n" \
  "ds_read_b64_tr_b16 v[64:65], v128 offset:16384\n" \
  "ds_read_b64_tr_b16 v[66:67], v128 offset:17408\n" \
  "v_mfma_f32_16x16x32_bf16 a[4:7], v[0:3], a[136:139], 0\n" \
  "ds_read_b64_tr_b16 v[72:73], v129 offset:16384\n" \
  "s_add_u32 m0, s72, 81920\n" \
  "s_nop 0\n" \
  "buffer_load_dwordx4 v136, s[44:47], s69 offen lds\n" \
  "v_mfma_f32_16x16x32_bf16 a[8:11], v[8:11], a[128:131], 0\n" \
  "ds_read_b64_tr_b16 v[74:75], v129 offset:17408\n" \
  "ds_read_b64_tr_b16 v[80:81], v128 offset:16640\n" \
  "v_mfma_f32_16x16x32_bf16 a[12:15], v[8:11], a[136:139], 0\n" \
  "ds_read_b64_tr_b16 v[82:83], v128 offset:17664\n" \
  "buffer_load_dwordx4 v137, s[44:47], s69 offen offset:1024 lds\n" \
  "v_mfma_f32_16x16x32_bf16 a[16:19], v[16:19], a[128:131], 0\n" \
  "ds_read_b64_tr_b16 v[88:89], v129 offset:16640\n" \
  "ds_read_b64_tr_b16 v[90:91], v129 offset:17664\n" \
  "v_mfma_f32_16x16x32_bf16 a[20:23], v[16:19], a[136:139], 0\n" \
  "ds_read_b64_tr_b16 v[96:97], v128 offset:16896\n" \
  "buffer_load_dwordx4 v138, s[44:47], s69 offen offset:2048 lds\n" \
  "v_mfma_f32_16x16x32_bf16 a[24:27], v[24:27], a[128:131], 0\n" \
  "ds_read_b64_tr_b16 v[98:99], v128 offset:17920\n" \
  "ds_read_b64_tr_b16 v[104:105], v129 offset:16896\n" \
  "v_mfma_f32_16x16x32_bf16 a[28:31], v[24:27], a[136:139], 0\n" \
  "ds_read_b64_tr_b16 v[106:107], v129 offset:17920\n" \
  "buffer_load_dwordx4 v139, s[44:47], s69 offen offset:3072 lds\n" \
  "s_add_u32 s69, s69, 0x10000\n" \
  "v_mfma_f32_16x16x32_bf16 a[32:35], v[32:35], a[128:131], 0\n" \
  "ds_read_b64_tr_b16 v[112:113], v128 offset:17152\n" \
  "ds_read_b64_tr_b16 v[114:115], v128 offset:18176\n" \
  "v_mfma_f32_16x16x32_bf16 a[36:39], v[32:35], a[136:139], 0\n" \
  "ds_read_b64_tr_b16 v[120:121], v129 offset:17152\n" \
  "buffer_load_dwordx4 a[208:211], v132, s[40:43], s68 offen offset:0\n" \
  "v_mfma_f32_16x16x32_bf16 a[40:43], v[40:43], a[128:131], 0\n" \
  "ds_read_b64_tr_b16 v[122:123], v129 offset:18176\n" \
  "ds_read_b64_tr_b16 v[68:69], v128 offset:24576\n" \
  "v_mfma_f32_16x16x32_bf16 a[44:47], v[40:43], a[136:139], 0\n" \
  "ds_read_b64_tr_b16 v[70:71], v128 offset:25600\n" \
  "buffer_load_dwordx4 a[216:219], v133, s[40:43], s68 offen offset:0\n" \
  "v_mfma_f32_16x16x32_bf16 a[48:51], v[48:51], a[128:131], 0\n" \
  "ds_read_b64_tr_b16 v[76:77], v129 offset:24576\n" \
  "ds_read_b64_tr_b16 v[78:79], v129 offset:25600\n" \
  "v_mfma_f32_16x16x32_bf16 a[52:55], v[48:51], a[136:139], 0\n" \
  "ds_read_b64_tr_b16 v[84:85], v128 offset:24832\n" \
  "buffer_load_dwordx4 a[212:215], v132, s[40:43], s68 offen offset:1024\n" \
  "v_mfma_f32_16x16x32_bf16 a[56:59], v[56:59], a[128:131], 0\n" \
  "ds_read_b64_tr_b16 v[86:87], v128 offset:25856\n" \
  "ds_read_b64_tr_b16 v[92:93], v129 offset:24832\n" \
  "v_mfma_f32_16x16x32_bf16 a[60:63], v[56:59], a[136:139], 0\n" \
  "ds_read_b64_tr_b16 v[94:95], v129 offset:25856\n" \
  "buffer_load_dwordx4 a[220:223], v133, s[40:43], s68 offen offset:1024\n" \
  "s_add_u32 s68, s68, 2048\n" \
  "v_mfma_f32_16x16x32_bf16 a[0:3], v[4:7], a[132:135], a[0:3]\n" \
  "ds_read_b64_tr_b16 v[100:101], v128 offset:25088\n" \
  "ds_read_b64_tr_b16 v[102:103], v128 offset:26112\n" \
  "v_mfma_f32_16x16x32_bf16 a[4:7], v[4:7], a[140:143], a[4:7]\n" \
  "ds_read_b64_tr_b16 v[108:109], v129 offset:25088\n" \
  "s_add_u32 m0, s72, 98304\n" \
  "s_nop 0\n" \
  "buffer_load_dwordx4 v136, s[44:47], s69 offen lds\n" \
  "v_mfma_f32_16x16x32_bf16 a[8:11], v[12:15], a[132:135], a[8:11]\n" \
  "ds_read_b64_tr_b16 v[110:111], v129 offset:26112\n" \
  "ds_read_b64_tr_b16 v[116:117], v128 offset:25344\n" \
  "v_mfma_f32_16x16x32_bf16 a[12:15], v[12:15], a[140:143], a[12:15]\n" \
  "ds_read_b64_tr_b16 v[118:119], v128 offset:26368\n" \
  "buffer_load_dwordx4 v137, s[44:47], s69 offen offset:1024 lds\n" \
  "v_mfma_f32_16x16x32_bf16 a[16:19], v[20:23], a[132:135], a[16:19]\n" \
  "ds_read_b64_tr_b16 v[124:125], v129 offset:25344\n" \
  "ds_read_b64_tr_b16 v[126:127], v129 offset:26368\n" \
  "v_mfma_f32_16x16x32_bf16 a[20:23], v[20:23], a[140:143], a[20:23]\n" \
  "buffer_load_dwordx4 v138, s[44:47], s69 offen offset:2048 lds\n" \
  "buffer_load_dwordx4 v139, s[44:47], s69 offen offset:3072 lds\n" \
  "s_add_u32 s69, s69, 0x10000\n" \
  "v_mfma_f32_16x16x32_bf16 a[24:27], v[28:31], a[132:135], a[24:27]\n" \
  "buffer_load_dwordx4 a[224:227], v132, s[40:43], s68 offen offset:0\n" \
  "buffer_load_dwordx4 a[232:235], v133, s[40:43], s68 offen offset:0\n" \
  "v_mfma_f32_16x16x32_bf16 a[28:31], v[28:31], a[140:143], a[28:31]\n" \
  "buffer_load_dwordx4 a[228:231], v132, s[40:43], s68 offen offset:1024\n" \
  "buffer_load_dwordx4 a[236:239], v133, s[40:43], s68 offen offset:1024\n" \
  "s_add_u32 s68, s68, 2048\n" \
  "v_mfma_f32_16x16x32_bf16 a[32:35], v[36:39], a[132:135], a[32:35]\n" \
  "v_mfma_f32_16x16x32_bf16 a[36:39], v[36:39], a[140:143], a[36:39]\n" \
  "v_mfma_f32_16x16x32_bf16 a[40:43], v[44:47], a[132:135], a[40:43]\n" \
  "v_mfma_f32_16x16x32_bf16 a[44:47], v[44:47], a[140:143], a[44:47]\n" \
  "v_mfma_f32_16x16x32_bf16 a[48:51], v[52:55], a[132:135], a[48:51]\n" \
  "v_mfma_f32_16x16x32_bf16 a[52:55], v[52:55], a[140:143], a[52:55]\n" \
  "v_mfma_f32_16x16x32_bf16 a[56:59], v[60:63], a[132:135], a[56:59]\n" \
  "v_mfma_f32_16x16x32_bf16 a[60:63], v[60:63], a[140:143], a[60:63]\n" \
  "s_waitcnt vmcnt(36) lgkmcnt(0)\n" \
  "s_barrier\n" \
  "v_mfma_f32_16x16x32_bf16 a[0:3], v[64:67], a[144:147], a[0:3]\n" \
  "ds_read_b64_tr_b16 v[0:1], v128 offset:32768\n" \
  "ds_read_b64_tr_b16 v[2:3], v128 offset:33792\n" \
  "v_mfma_f32_16x16x32_bf16 a[4:7], v[64:67], a[152:155], a[4:7]\n" \
  "ds_read_b64_tr_b16 v[8:9], v129 offset:32768\n" \
  "s_add_u32 m0, s72, 114688\n" \
  "s_nop 0\n" \
  "buffer_load_dwordx4 v136, s[44:47], s69 offen lds\n" \
  "v_mfma_f32_16x16x32_bf16 a[8:11], v[72:75], a[144:147], a[8:11]\n" \
  "ds_read_b64_tr_b16 v[10:11], v129 offset:33792\n" \
  "ds_read_b64_tr_b16 v[16:17], v128 offset:33024\n" \
  "v_mfma_f32_16x16x32_bf16 a[12:15], v[72:75], a[152:155], a[12:15]\n" \
  "ds_read_b64_tr_b16 v[18:19], v128 offset:34048\n" \
  "buffer_load_dwordx4 v137, s[44:47], s69 offen offset:1024 lds\n" \
  "v_mfma_f32_16x16x32_bf16 a[16:19], v[80:83], a[144:147], a[16:19]\n" \
  "ds_read_b64_tr_b16 v[24:25], v129 offset:33024\n" \
  "ds_read_b64_tr_b16 v[26:27], v129 offset:34048\n" \
  "v_mfma_f32_16x16x32_bf16 a[20:23], v[80:83], a[152:155], a[20:23]\n" \
  "ds_read_b64_tr_b16 v[32:33], v128 offset:33280\n" \
  "buffer_load_dwordx4 v138, s[44:47], s69 offen offset:2048 lds\n" \
  "v_mfma_f32_16x16x32_bf16 a[24:27], v[88:91], a[144:147], a[24:27]\n" \
  "ds_read_b64_tr_b16 v[34:35], v128 offset:34304\n" \
  "ds_read_b64_tr_b16 v[40:41], v129 offset:33280\n" \
  "v_mfma_f32_16x16x32_bf16 a[28:31], v[88:91], a[152:155], a[28:31]\n" \
  "ds_read_b64_tr_b16 v[42:43], v129 offset:34304\n" \
  "buffer_load_dwordx4 v139, s[44:47], s69 offen offset:3072 lds\n" \
  "s_add_u32 s69, s69, 0x10000\n" \
  "v_mfma_f32_16x16x32_bf16 a[32:35], v[96:99], a[144:147], a[32:35]\n" \
  "ds_read_b64_tr_b16 v[48:49], v128 offset:33536\n" \
  "ds_read_b64_tr_b16 v[50:51], v128 offset:34560\n" \
  "v_mfma_f32_16x16x32_bf16 a[36:39], v[96:99], a[152:155], a[36:39]\n" \
  "ds_read_b64_tr_b16 v[56:57], v129 offset:33536\n" \
  "buffer_load_dwordx4 a[240:243], v132, s[40:43], s68 offen offset:0\n" \
  "v_mfma_f32_16x16x32_bf16 a[40:43], v[104:107], a[144:147], a[40:43]\n" \
  "ds_read_b64_tr_b16 v[58:59], v129 offset:34560\n" \
  "ds_read_b64_tr_b16 v[4:5], v128 offset:40960\n" \
  "v_mfma_f32_16x16x32_bf16 a[44:47], v[104:107], a[152:155], a[44:47]\n" \
  "ds_read_b64_tr_b16 v[6:7], v128 offset:41984\n" \
  "buffer_load_dwordx4 a[248:251], v133, s[40:43], s68 offen offset:0\n" \
  "v_mfma_f32_16x16x32_bf16 a[48:51], v[112:115], a[144:147], a[48:51]\n" \
  "ds_read_b64_tr_b16 v[12:13], v129 offset:40960\n" \
  "ds_read_b64_tr_b16 v[14:15], v129 offset:41984\n" \
  "v_mfma_f32_16x16x32_bf16 a[52:55], v[112:115], a[152:155], a[52:55]\n" \
  "ds_read_b64_tr_b16 v[20:21], v128 offset:41216\n" \
  "buffer_load_dwordx4 a[244:247], v132, s[40:43], s68 offen offset:1024\n" \
  "v_mfma_f32_16x16x32_bf16 a[56:59], v[120:123], a[144:147], a[56:59]\n" \
  "ds_read_b64_tr_b16 v[22:23], v128 offset:42240\n" \
  "ds_read_b64_tr_b16 v[28:29], v129 offset:41216\n" \
  "v_mfma_f32_16x16x32_bf16 a[60:63], v[120:123], a[152:155], a[60:63]\n" \
  "ds_read_b64_tr_b16 v[30:31], v129 offset:42240\n" \
  "buffer_load_dwordx4 a[252:255], v133, s[40:43], s68 offen offset:1024\n" \
  "s_add_u32 s68, s68, 2048\n" \
  "v_mfma_f32_16x16x32_bf16 a[0:3], v[68:71], a[148:151], a[0:3]\n" \
  "ds_read_b64_tr_b16 v[36:37], v128 offset:41472\n" \
  "ds_read_b64_tr_b16 v[38:39], v128 offset:42496\n" \
  "v_mfma_f32_16x16x32_bf16 a[4:7], v[68:71], a[156:159], a[4:7]\n" \
  "ds_read_b64_tr_b16 v[44:45], v129 offset:41472\n" \
  "s_add_u32 m0, s72, 0\n" \
  "s_nop 0\n" \
  "buffer_load_dwordx4 v136, s[44:47], s69 offen lds\n" \
  "v_mfma_f32_16x16x32_bf16 a[8:11], v[76:79], a[148:151], a[8:11]\n" \
  "ds_read_b64_tr_b16 v[46:47], v129 offset:42496\n" \
  "ds_read_b64_tr_b16 v[52:53], v128 offset:41728\n" \
  "v_mfma_f32_16x16x32_bf16 a[12:15], v[76:79], a[156:159], a[12:15]\n" \
  "ds_read_b64_tr_b16 v[54:55], v128 offset:42752\n" \
  "buffer_load_dwordx4 v137, s[44:47], s69 offen offset:1024 lds\n" \
  "v_mfma_f32_16x16x32_bf16 a[16:19], v[84:87], a[148:151], a[16:19]\n" \
  "ds_read_b64_tr_b16 v[60:61], v129 offset:41728\n" \
  "ds_read_b64_tr_b16 v[62:63], v129 offset:42752\n" \
  "v_mfma_f32_16x16x32_bf16 a[20:23], v[84:87], a[156:159], a[20:23]\n" \
  "buffer_load_dwordx4 v138, s[44:47], s69 offen offset:2048 lds\n" \
  "buffer_load_dwordx4 v139, s[44:47], s69 offen offset:3072 lds\n" \
  "s_add_u32 s69, s69, 0x10000\n" \
  "v_mfma_f32_16x16x32_bf16 a[24:27], v[92:95], a[148:151], a[24:27]\n" \
  "buffer_load_dwordx4 a[128:131], v132, s[40:43], s68 offen offset:0\n" \
  "buffer_load_dwordx4 a[136:139], v133, s[40:43], s68 offen offset:0\n" \
  "v_mfma_f32_16x16x32_bf16 a[28:31], v[92:95], a[156:159], a[28:31]\n" \
  "buffer_load_dwordx4 a[132:135], v132, s[40:43], s68 offen offset:1024\n" \
  "buffer_load_dwordx4 a[140:143], v133, s[40:43], s68 offen offset:1024\n" \
  "s_add_u32 s68, s68, 2048\n" \
  "v_mfma_f32_16x16x32_bf16 a[32:35], v[100:103], a[148:151], a[32:35]\n" \
  "v_mfma_f32_16x16x32_bf16 a[36:39], v[100:103], a[156:159], a[36:39]\n" \
  "v_mfma_f32_16x16x32_bf16 a[40:43], v[108:111], a[148:151], a[40:43]\n" \
  "v_mfma_f32_16x16x32_bf16 a[44:47], v[108:111], a[156:159], a[44:47]\n" \
  "v_mfma_f32_16x16x32_bf16 a[48:51], v[116:119], a[148:151], a[48:51]\n" \
  "v_mfma_f32_16x16x32_bf16 a[52:55], v[116:119], a[156:159], a[52:55]\n" \
  "v_mfma_f32_16x16x32_bf16 a[56:59], v[124:127], a[148:151], a[56:59]\n" \
  "v_mfma_f32_16x16x32_bf16 a[60:63], v[124:127], a[156:159], a[60:63]\n" \
  "s_waitcnt vmcnt(44) lgkmcnt(0)\n" \
  "s_barrier\n" \
  "v_mfma_f32_16x16x32_bf16 a[0:3], v[0:3], a[160:163], a[0:3]\n" \
  "ds_read_b64_tr_b16 v[64:65], v128 offset:49152\n" \
  "ds_read_b64_tr_b16 v[66:67], v128 offset:50176\n" \
  "v_mfma_f32_16x16x32_bf16 a[4:7], v[0:3], a[168:171], a[4:7]\n" \
  "ds_read_b64_tr_b16 v[72:73], v129 offset:49152\n" \
  "s_add_u32 m0, s72, 16384\n" \
  "s_nop 0\n" \
  "buffer_load_dwordx4 v136, s[44:47], s69 offen lds\n" \
  "v_mfma_f32_16x16x32_bf16 a[8:11], v[8:11], a[160:163], a[8:11]\n" \
  "ds_read_b64_tr_b16 v[74:75], v129 offset:50176\n" \
  "ds_read_b64_tr_b16 v[80:81], v128 offset:49408\n" \
  "v_mfma_f32_16x16x32_bf16 a[12:15], v[8:11], a[168:171], a[12:15]\n" \
  "ds_read_b64_tr_b16 v[82:83], v128 offset:50432\n" \
  "buffer_load_dwordx4 v137, s[44:47], s69 offen offset:1024 lds\n" \
  "v_mfma_f32_16x16x32_bf16 a[16:19], v[16:19], a[160:163], a[16:19]\n" \
  "ds_read_b64_tr_b16 v[88:89], v129 offset:49408\n" \
  "ds_read_b64_tr_b16 v[90:91], v129 offset:50432\n" \
  "v_mfma_f32_16x16x32_bf16 a[20:23], v[16:19], a[168:171], a[20:23]\n" \
  "ds_read_b64_tr_b16 v[96:97], v128 offset:49664\n" \
  "buffer_load_dwordx4 v138, s[44:47], s69 offen offset:2048 lds\n" \
  "v_mfma_f32_16x16x32_bf16 a[24:27], v[24:27], a[160:163], a[24:27]\n" \
  "ds_read_b64_tr_b16 v[98:99], v128 offset:50688\n" \
  "ds_read_b64_tr_b16 v[104:105], v129 offset:49664\n" \
  "v_mfma_f32_16x16x32_bf16 a[28:31], v[24:27], a[168:171], a[28:31]\n" \
  "ds_read_b64_tr_b16 v[106:107], v129 offset:50688\n" \
  "buffer_load_dwordx4 v139, s[44:47], s69 offen offset:3072 lds\n" \
  "s_add_u32 s69, s69, 0x10000\n" \
  "v_mfma_f32_16x16x32_bf16 a[32:35], v[32:35], a[160:163], a[32:35]\n" \
  "ds_read_b64_tr_b16 v[112:113], v128 offset:49920\n" \
  "ds_read_b64_tr_b16 v[114:115], v128 offset:50944\n" \
  "v_mfma_f32_16x16x32_bf16 a[36:39], v[32:35], a[168:171], a[36:39]\n" \
  "ds_read_b64_tr_b16 v[120:121], v129 offset:49920\n" \
  "buffer_load_dwordx4 a[144:147], v132, s[40:43], s68 offen offset:0\n" \
  "v_mfma_f32_16x16x32_bf16 a[40:43], v[40:43], a[160:163], a[40:43]\n" \
  "ds_read_b64_tr_b16 v[122:123], v129 offset:50944\n" \
  "ds_read_b64_tr_b16 v[68:69], v128 offset:57344\n" \
  "v_mfma_f32_16x16x32_bf16 a[44:47], v[40:43], a[168:171], a[44:47]\n" \
  "ds_read_b64_tr_b16 v[70:71], v128 offset:58368\n" \
  "buffer_load_dwordx4 a[152:155], v133, s[40:43], s68 offen offset:0\n" \
  "v_mfma_f32_16x16x32_bf16 a[48:51], v[48:51], a[160:163], a[48:51]\n" \
  "ds_read_b64_tr_b16 v[76:77], v129 offset:57344\n" \
  "ds_read_b64_tr_b16 v[78:79], v129 offset:58368\n" \
  "v_mfma_f32_16x16x32_bf16 a[52:55], v[48:51], a[168:171], a[52:55]\n" \
  "ds_read_b64_tr_b16 v[84:85], v128 offset:57600\n" \
  "buffer_load_dwordx4 a[148:151], v132, s[40:43], s68 offen offset:1024\n" \
  "v_mfma_f32_16x16x32_bf16 a[56:59], v[56:59], a[160:163], a[56:59]\n" \
  "ds_read_b64_tr_b16 v[86:87], v128 offset:58624\n" \
  "ds_read_b64_tr_b16 v[92:93], v129 offset:57600\n" \
  "v_mfma_f32_16x16x32_bf16 a[60:63], v[56:59], a[168:171], a[60:63]\n" \
  "ds_read_b64_tr_b16 v[94:95], v129 offset:58624\n" \
  "buffer_load_dwordx4 a[156:159], v133, s[40:43], s68 offen offset:1024\n" \
  "s_add_u32 s68, s68, 2048\n" \
  "v_mfma_f32_16x16x32_bf16 a[0:3], v[4:7], a[164:167], a[0:3]\n" \
  "ds_read_b64_tr_b16 v[100:101], v128 offset:57856\n" \
  "ds_read_b64_tr_b16 v[102:103], v128 offset:58880\n" \
  "v_mfma_f32_16x16x32_bf16 a[4:7], v[4:7], a[172:175], a[4:7]\n" \
  "ds_read_b64_tr_b16 v[108:109], v129 offset:57856\n" \
  "ds_read_b64_tr_b16 v[110:111], v129 offset:58880\n" \
  "v_mfma_f32_16x16x32_bf16 a[8:11], v[12:15], a[164:167], a[8:11]\n" \
  "ds_read_b64_tr_b16 v[116:117], v128 offset:58112\n" \
  "ds_read_b64_tr_b16 v[118:119], v128 offset:59136\n" \
  "v_mfma_f32_16x16x32_bf16 a[12:15], v[12:15], a[172:175], a[12:15]\n" \
  "ds_read_b64_tr_b16 v[124:125], v129 offset:58112\n" \
  "ds_read_b64_tr_b16 v[126:127], v129 offset:59136\n" \
  "v_mfma_f32_16x16x32_bf16 a[16:19], v[20:23], a[164:167], a[16:19]\n" \
  "v_mfma_f32_16x16x32_bf16 a[20:23], v[20:23], a[172:175], a[20:23]\n" \
  "v_mfma_f32_16x16x32_bf16 a[24:27], v[28:31], a[164:167], a[24:27]\n" \
  "v_mfma_f32_16x16x32_bf16 a[28:31], v[28:31], a[172:175], a[28:31]\n" \
  "v_mfma_f32_16x16x32_bf16 a[32:35], v[36:39], a[164:167], a[32:35]\n" \
  "v_mfma_f32_16x16x32_bf16 a[36:39], v[36:39], a[172:175], a[36:39]\n" \
  "v_mfma_f32_16x16x32_bf16 a[40:43], v[44:47], a[164:167], a[40:43]\n" \
  "v_mfma_f32_16x16x32_bf16 a[44:47], v[44:47], a[172:175], a[44:47]\n" \
  "v_mfma_f32_16x16x32_bf16 a[48:51], v[52:55], a[164:167], a[48:51]\n" \
  "v_mfma_f32_16x16x32_bf16 a[52:55], v[52:55], a[172:175], a[52:55]\n" \
  "v_mfma_f32_16x16x32_bf16 a[56:59], v[60:63], a[164:167], a[56:59]\n" \
  "v_mfma_f32_16x16x32_bf16 a[60:63], v[60:63], a[172:175], a[60:63]\n" \
  "s_waitcnt vmcnt(44) lgkmcnt(0)\n" \
  "s_barrier\n" \
  "v_mfma_f32_16x16x32_bf16 a[0:3], v[64:67], a[176:179], a[0:3]\n" \
  "ds_read_b64_tr_b16 v[0:1], v130 offset:0\n" \
  "ds_read_b64_tr_b16 v[2:3], v130 offset:1024\n" \
  "v_mfma_f32_16x16x32_bf16 a[4:7], v[64:67], a[184:187], a[4:7]\n" \
  "ds_read_b64_tr_b16 v[8:9], v131 offset:0\n" \
  "s_add_u32 m0, s72, 32768\n" \
  "s_nop 0\n" \
  "buffer_load_dwordx4 v136, s[44:47], s69 offen lds\n" \
  "v_mfma_f32_16x16x32_bf16 a[8:11], v[72:75], a[176:179], a[8:11]\n" \
  "ds_read_b64_tr_b16 v[10:11], v131 offset:1024\n" \
  "ds_read_b64_tr_b16 v[16:17], v130 offset:256\n" \
  "v_mfma_f32_16x16x32_bf16 a[12:15], v[72:75], a[184:187], a[12:15]\n" \
  "ds_read_b64_tr_b16 v[18:19], v130 offset:1280\n" \
  "buffer_load_dwordx4 v137, s[44:47], s69 offen offset:1024 lds\n" \
  "v_mfma_f32_16x16x32_bf16 a[16:19], v[80:83], a[176:179], a[16:19]\n" \
  "ds_read_b64_tr_b16 v[24:25], v131 offset:256\n" \
  "ds_read_b64_tr_b16 v[26:27], v131 offset:1280\n" \
  "v_mfma_f32_16x16x32_bf16 a[20:23], v[80:83], a[184:187], a[20:23]\n" \
  "ds_read_b64_tr_b16 v[32:33], v130 offset:512\n" \
  "buffer_load_dwordx4 v138, s[44:47], s69 offen offset:2048 lds\n" \
  "v_mfma_f32_16x16x32_bf16 a[24:27], v[88:91], a[176:179], a[24:27]\n" \
  "ds_read_b64_tr_b16 v[34:35], v130 offset:1536\n" \
  "ds_read_b64_tr_b16 v[40:41], v131 offset:512\n" \
  "v_mfma_f32_16x16x32_bf16 a[28:31], v[88:91], a[184:187], a[28:31]\n" \
  "ds_read_b64_tr_b16 v[42:43], v131 offset:1536\n" \
  "buffer_load_dwordx4 v139, s[44:47], s69 offen offset:3072 lds\n" \
  "s_add_u32 s69, s69, 0x10000\n" \
  "v_mfma_f32_16x16x32_bf16 a[32:35], v[96:99], a[176:179], a[32:35]\n" \
  "ds_read_b64_tr_b16 v[48:49], v130 offset:768\n" \
  "ds_read_b64_tr_b16 v[50:51], v130 offset:1792\n" \
  "v_mfma_f32_16x16x32_bf16 a[36:39], v[96:99], a[184:187], a[36:39]\n" \
  "ds_read_b64_tr_b16 v[56:57], v131 offset:768\n" \
  "buffer_load_dwordx4 a[160:163], v132, s[40:43], s68 offen offset:0\n" \
  "v_mfma_f32_16x16x32_bf16 a[40:43], v[104:107], a[176:179], a[40:43]\n" \
  "ds_read_b64_tr_b16 v[58:59], v131 offset:1792\n" \
  "ds_read_b64_tr_b16 v[4:5], v130 offset:8192\n" \
  "v_mfma_f32_16x16x32_bf16 a[44:47], v[104:107], a[184:187], a[44:47]\n" \
  "ds_read_b64_tr_b16 v[6:7], v130 offset:9216\n" \
  "buffer_load_dwordx4 a[168:171], v133, s[40:43], s68 offen offset:0\n" \
  "v_mfma_f32_16x16x32_bf16 a[48:51], v[112:115], a[176:179], a[48:51]\n" \
  "ds_read_b64_tr_b16 v[12:13], v131 offset:8192\n" \
  "ds_read_b64_tr_b16 v[14:15], v131 offset:9216\n" \
  "v_mfma_f32_16x16x32_bf16 a[52:55], v[112:115], a[184:187], a[52:55]\n" \
  "ds_read_b64_tr_b16 v[20:21], v130 offset:8448\n" \
  "buffer_load_dwordx4 a[164:167], v132, s[40:43], s68 offen offset:1024\n" \
  "v_mfma_f32_16x16x32_bf16 a[56:59], v[120:123], a[176:179], a[56:59]\n" \
  "ds_read_b64_tr_b16 v[22:23], v130 offset:9472\n" \
  "ds_read_b64_tr_b16 v[28:29], v131 offset:8448\n" \
  "v_mfma_f32_16x16x32_bf16 a[60:63], v[120:123], a[184:187], a[60:63]\n" \
  "ds_read_b64_tr_b16 v[30:31], v131 offset:9472\n" \
  "buffer_load_dwordx4 a[172:175], v133, s[40:43], s68 offen offset:1024\n" \
  "s_add_u32 s68, s68, 2048\n" \
  "v_mfma_f32_16x16x32_bf16 a[0:3], v[68:71], a[180:183], a[0:3]\n" \
  "ds_read_b64_tr_b16 v[36:37], v130 offset:8704\n" \
  "ds_read_b64_tr_b16 v[38:39], v130 offset:9728\n" \
  "v_mfma_f32_16x16x32_bf16 a[4:7], v[68:71], a[188:191], a[4:7]\n" \
  "ds_read_b64_tr_b16 v[44:45], v131 offset:8704\n" \
  "ds_read_b64_tr_b16 v[46:47], v131 offset:9728\n" \
  "v_mfma_f32_16x16x32_bf16 a[8:11], v[76:79], a[180:183], a[8:11]\n" \
  "ds_read_b64_tr_b16 v[52:53], v130 offset:8960\n" \
  "ds_read_b64_tr_b16 v[54:55], v130 offset:9984\n" \
  "v_mfma_f32_16x16x32_bf16 a[12:15], v[76:79], a[188:191], a[12:15]\n" \
  "ds_read_b64_tr_b16 v[60:61], v131 offset:8960\n" \
  "ds_read_b64_tr_b16 v[62:63], v131 offset:9984\n" \
  "v_mfma_f32_16x16x32_bf16 a[16:19], v[84:87], a[180:183], a[16:19]\n" \
  "v_mfma_f32_16x16x32_bf16 a[20:23], v[84:87], a[188:191], a[20:23]\n" \
  "v_mfma_f32_16x16x32_bf16 a[24:27], v[92:95], a[180:183], a[24:27]\n" \
  "v_mfma_f32_16x16x32_bf16 a[28:31], v[92:95], a[188:191], a[28:31]\n" \
  "v_mfma_f32_16x16x32_bf16 a[32:35], v[100:103], a[180:183], a[32:35]\n" \
  "v_mfma_f32_16x16x32_bf16 a[36:39], v[100:103], a[188:191], a[36:39]\n" \
  "v_mfma_f32_16x16x32_bf16 a[40:43], v[108:111], a[180:183], a[40:43]\n" \
  "v_mfma_f32_16x16x32_bf16 a[44:47], v[108:111], a[188:191], a[44:47]\n" \
  "v_mfma_f32_16x16x32_bf16 a[48:51], v[116:119], a[180:183], a[48:51]\n" \
  "v_mfma_f32_16x16x32_bf16 a[52:55], v[116:119], a[188:191], a[52:55]\n" \
  "v_mfma_f32_16x16x32_bf16 a[56:59], v[124:127], a[180:183], a[56:59]\n" \
  "v_mfma_f32_16x16x32_bf16 a[60:63], v[124:127], a[188:191], a[60:63]\n" \
  "s_waitcnt vmcnt(44) lgkmcnt(0)\n" \
  "s_barrier\n" \
  "v_mfma_f32_16x16x32_bf16 a[0:3], v[0:3], a[192:195], a[0:3]\n" \
  "ds_read_b64_tr_b16 v[64:65], v130 offset:16384\n" \
  "ds_read_b64_tr_b16 v[66:67], v130 offset:17408\n" \
  "v_mfma_f32_16x16x32_bf16 a[4:7], v[0:3], a[200:203], a[4:7]\n" \
  "ds_read_b64_tr_b16 v[72:73], v131 offset:16384\n" \
  "s_add_u32 m0, s72, 49152\n" \
  "s_nop 0\n" \
  "buffer_load_dwordx4 v136, s[44:47], s69 offen lds\n" \
  "v_mfma_f32_16x16x32_bf16 a[8:11], v[8:11], a[192:195], a[8:11]\n" \
  "ds_read_b64_tr_b16 v[74:75], v131 offset:17408\n" \
  "ds_read_b64_tr_b16 v[80:81], v130 offset:16640\n" \
  "v_mfma_f32_16x16x32_bf16 a[12:15], v[8:11], a[200:203], a[12:15]\n" \
  "ds_read_b64_tr_b16 v[82:83], v130 offset:17664\n" \
  "buffer_load_dwordx4 v137, s[44:47], s69 offen offset:1024 lds\n" \
  "v_mfma_f32_16x16x32_bf16 a[16:19], v[16:19], a[192:195], a[16:19]\n" \
  "ds_read_b64_tr_b16 v[88:89], v131 offset:16640\n" \
  "ds_read_b64_tr_b16 v[90:91], v131 offset:17664\n" \
  "v_mfma_f32_16x16x32_bf16 a[20:23], v[16:19], a[200:203], a[20:23]\n" \
  "ds_read_b64_tr_b16 v[96:97], v130 offset:16896\n" \
  "buffer_load_dwordx4 v138, s[44:47], s69 offen offset:2048 lds\n" \
  "v_mfma_f32_16x16x32_bf16 a[24:27], v[24:27], a[192:195], a[24:27]\n" \
  "ds_read_b64_tr_b16 v[98:99], v130 offset:17920\n" \
  "ds_read_b64_tr_b16 v[104:105], v131 offset:16896\n" \
  "v_mfma_f32_16x16x32_bf16 a[28:31], v[24:27], a[200:203], a[28:31]\n" \
  "ds_read_b64_tr_b16 v[106:107], v131 offset:17920\n" \
  "buffer_load_dwordx4 v139, s[44:47], s69 offen offset:3072 lds\n" \
  "s_add_u32 s69, s69, 0x10000\n" \
  "v_mfma_f32_16x16x32_bf16 a[32:35], v[32:35], a[192:195], a[32:35]\n" \
  "ds_read_b64_tr_b16 v[112:113], v130 offset:17152\n" \
  "ds_read_b64_tr_b16 v[114:115], v130 offset:18176\n" \
  "v_mfma_f32_16x16x32_bf16 a[36:39], v[32:35], a[200:203], a[36:39]\n" \
  "ds_read_b64_tr_b16 v[120:121], v131 offset:17152\n" \
  "buffer_load_dwordx4 a[176:179], v132, s[40:43], s68 offen offset:0\n" \
  "v_mfma_f32_16x16x32_bf16 a[40:43], v[40:43], a[192:195], a[40:43]\n" \
  "ds_read_b64_tr_b16 v[122:123], v131 offset:18176\n" \
  "ds_read_b64_tr_b16 v[68:69], v130 offset:24576\n" \
  "v_mfma_f32_16x16x32_bf16 a[44:47], v[40:43], a[200:203], a[44:47]\n" \
  "ds_read_b64_tr_b16 v[70:71], v130 offset:25600\n" \
  "buffer_load_dwordx4 a[184:187], v133, s[40:43], s68 offen offset:0\n" \
  "v_mfma_f32_16x16x32_bf16 a[48:51], v[48:51], a[192:195], a[48:51]\n" \
  "ds_read_b64_tr_b16 v[76:77], v131 offset:24576\n" \
  "ds_read_b64_tr_b16 v[78:79], v131 offset:25600\n" \
  "v_mfma_f32_16x16x32_bf16 a[52:55], v[48:51], a[200:203], a[52:55]\n" \
  "ds_read_b64_tr_b16 v[84:85], v130 offset:24832\n" \
  "buffer_load_dwordx4 a[180:183], v132, s[40:43], s68 offen offset:1024\n" \
  "v_mfma_f32_16x16x32_bf16 a[56:59], v[56:59], a[192:195], a[56:59]\n" \
  "ds_read_b64_tr_b16 v[86:87], v130 offset:25856\n" \
  "ds_read_b64_tr_b16 v[92:93], v131 offset:24832\n" \
  "v_mfma_f32_16x16x32_bf16 a[60:63], v[56:59], a[200:203], a[60:63]\n" \
  "ds_read_b64_tr_b16 v[94:95], v131 offset:25856\n" \
  "buffer_load_dwordx4 a[188:191], v133, s[40:43], s68 offen offset:1024\n" \
  "s_add_u32 s68, s68, 2048\n" \
  "v_mfma_f32_16x16x32_bf16 a[0:3], v[4:7], a[196:199], a[0:3]\n" \
  "ds_read_b64_tr_b16 v[100:101], v130 offset:25088\n" \
  "ds_read_b64_tr_b16 v[102:103], v130 offset:26112\n" \
  "v_mfma_f32_16x16x32_bf16 a[4:7], v[4:7], a[204:207], a[4:7]\n" \
  "ds_read_b64_tr_b16 v[108:109], v131 offset:25088\n" \
  "ds_read_b64_tr_b16 v[110:111], v131 offset:26112\n" \
  "v_mfma_f32_16x16x32_bf16 a[8:11], v[12:15], a[196:199], a[8:11]\n" \
  "ds_read_b64_tr_b16 v[116:117], v130 offset:25344\n" \
  "ds_read_b64_tr_b16 v[118:119], v130 offset:26368\n" \
  "v_mfma_f32_16x16x32_bf16 a[12:15], v[12:15], a[204:207], a[12:15]\n" \
  "ds_read_b64_tr_b16 v[124:125], v131 offset:25344\n" \
  "ds_read_b64_tr_b16 v[126:127], v131 offset:26368\n" \
  "v_mfma_f32_16x16x32_bf16 a[16:19], v[20:23], a[196:199], a[16:19]\n" \
  "v_mfma_f32_16x16x32_bf16 a[20:23], v[20:23], a[204:207], a[20:23]\n" \
  "v_mfma_f32_16x16x32_bf16 a[24:27], v[28:31], a[196:199], a[24:27]\n" \
  "v_mfma_f32_16x16x32_bf16 a[28:31], v[28:31], a[204:207], a[28:31]\n" \
  "v_mfma_f32_16x16x32_bf16 a[32:35], v[36:39], a[196:199], a[32:35]\n" \
  "v_mfma_f32_16x16x32_bf16 a[36:39], v[36:39], a[204:207], a[36:39]\n" \
  "v_mfma_f32_16x16x32_bf16 a[40:43], v[44:47], a[196:199], a[40:43]\n" \
  "v_mfma_f32_16x16x32_bf16 a[44:47], v[44:47], a[204:207], a[44:47]\n" \
  "v_mfma_f32_16x16x32_bf16 a[48:51], v[52:55], a[196:199], a[48:51]\n" \
  "v_mfma_f32_16x16x32_bf16 a[52:55], v[52:55], a[204:207], a[52:55]\n" \
  "v_mfma_f32_16x16x32_bf16 a[56:59], v[60:63], a[196:199], a[56:59]\n" \
  "v_mfma_f32_16x16x32_bf16 a[60:63], v[60:63], a[204:207], a[60:63]\n" \
  "s_waitcnt vmcnt(44) lgkmcnt(0)\n" \
  "s_barrier\n" \
  "v_mfma_f32_16x16x32_bf16 a[0:3], v[64:67], a[208:211], a[0:3]\n" \
  "ds_read_b64_tr_b16 v[0:1], v130 offset:32768\n" \
  "ds_read_b64_tr_b16 v[2:3], v130 offset:33792\n" \
  "v_mfma_f32_16x16x32_bf16 a[4:7], v[64:67], a[216:219], a[4:7]\n" \
  "ds_read_b64_tr_b16 v[8:9], v131 offset:32768\n" \
  "s_add_u32 m0, s72, 65536\n" \
  "s_nop 0\n" \
  "buffer_load_dwordx4 v136, s[44:47], s69 offen lds\n" \
  "v_mfma_f32_16x16x32_bf16 a[8:11], v[72:75], a[208:211], a[8:11]\n" \
  "ds_read_b64_tr_b16 v[10:11], v131 offset:33792\n" \
  "ds_read_b64_tr_b16 v[16:17], v130 offset:33024\n" \
  "v_mfma_f32_16x16x32_bf16 a[12:15], v[72:75], a[216:219], a[12:15]\n" \
  "ds_read_b64_tr_b16 v[18:19], v130 offset:34048\n" \
  "buffer_load_dwordx4 v137, s[44:47], s69 offen offset:1024 lds\n" \
  "v_mfma_f32_16x16x32_bf16 a[16:19], v[80:83], a[208:211], a[16:19]\n" \
  "ds_read_b64_tr_b16 v[24:25], v131 offset:33024\n" \
  "ds_read_b64_tr_b16 v[26:27], v131 offset:34048\n" \
  "v_mfma_f32_16x16x32_bf16 a[20:23], v[80:83], a[216:219], a[20:23]\n" \
  "ds_read_b64_tr_b16 v[32:33], v130 offset:33280\n" \
  "buffer_load_dwordx4 v138, s[44:47], s69 offen offset:2048 lds\n" \
  "v_mfma_f32_16x16x32_bf16 a[24:27], v[88:91], a[208:211], a[24:27]\n" \
  "ds_read_b64_tr_b16 v[34:35], v130 offset:34304\n" \
  "ds_read_b64_tr_b16 v[40:41], v131 offset:33280\n" \
  "v_mfma_f32_16x16x32_bf16 a[28:31], v[88:91], a[216:219], a[28:31]\n" \
  "ds_read_b64_tr_b16 v[42:43], v131 offset:34304\n" \
  "buffer_load_dwordx4 v139, s[44:47], s69 offen offset:3072 lds\n" \
  "s_add_u32 s69, s69, 0x10000\n" \
  "v_mfma_f32_16x16x32_bf16 a[32:35], v[96:99], a[208:211], a[32:35]\n" \
  "ds_read_b64_tr_b16 v[48:49], v130 offset:33536\n" \
  "ds_read_b64_tr_b16 v[50:51], v130 offset:34560\n" \
  "v_mfma_f32_16x16x32_bf16 a[36:39], v[96:99], a[216:219], a[36:39]\n" \
  "ds_read_b64_tr_b16 v[56:57], v131 offset:33536\n" \
  "buffer_load_dwordx4 a[192:195], v132, s[40:43], s68 offen offset:0\n" \
  "v_mfma_f32_16x16x32_bf16 a[40:43], v[104:107], a[208:211], a[40:43]\n" \
  "ds_read_b64_tr_b16 v[58:59], v131 offset:34560\n" \
  "ds_read_b64_tr_b16 v[4:5], v130 offset:40960\n" \
  "v_mfma_f32_16x16x32_bf16 a[44:47], v[104:107], a[216:219], a[44:47]\n" \
  "ds_read_b64_tr_b16 v[6:7], v130 offset:41984\n" \
  "buffer_load_dwordx4 a[200:203], v133, s[40:43], s68 offen offset:0\n" \
  "v_mfma_f32_16x16x32_bf16 a[48:51], v[112:115], a[208:211], a[48:51]\n" \
  "ds_read_b64_tr_b16 v[12:13], v131 offset:40960\n" \
  "ds_read_b64_tr_b16 v[14:15], v131 offset:41984\n" \
  "v_mfma_f32_16x16x32_bf16 a[52:55], v[112:115], a[216:219], a[52:55]\n" \
  "ds_read_b64_tr_b16 v[20:21], v130 offset:41216\n" \
  "buffer_load_dwordx4 a[196:199], v132, s[40:43], s68 offen offset:1024\n" \
  "v_mfma_f32_16x16x32_bf16 a[56:59], v[120:123], a[208:211], a[56:59]\n" \
  "ds_read_b64_tr_b16 v[22:23], v130 offset:42240\n" \
  "ds_read_b64_tr_b16 v[28:29], v131 offset:41216\n" \
  "v_mfma_f32_16x16x32_bf16 a[60:63], v[120:123], a[216:219], a[60:63]\n" \
  "ds_read_b64_tr_b16 v[30:31], v131 offset:42240\n" \
  "buffer_load_dwordx4 a[204:207], v133, s[40:43], s68 offen offset:1024\n" \
  "s_add_u32 s68, s68, 2048\n" \
  "v_mfma_f32_16x16x32_bf16 a[0:3], v[68:71], a[212:215], a[0:3]\n" \
  "ds_read_b64_tr_b16 v[36:37], v130 offset:41472\n" \
  "ds_read_b64_tr_b16 v[38:39], v130 offset:42496\n" \
  "v_mfma_f32_16x16x32_bf16 a[4:7], v[68:71], a[220:223], a[4:7]\n" \
  "ds_read_b64_tr_b16 v[44:45], v131 offset:41472\n" \
  "ds_read_b64_tr_b16 v[46:47], v131 offset:42496\n" \
  "v_mfma_f32_16x16x32_bf16 a[8:11], v[76:79], a[212:215], a[8:11]\n" \
  "ds_read_b64_tr_b16 v[52:53], v130 offset:41728\n" \
  "ds_read_b64_tr_b16 v[54:55], v130 offset:42752\n" \
  "v_mfma_f32_16x16x32_bf16 a[12:15], v[76:79], a[220:223], a[12:15]\n" \
  "ds_read_b64_tr_b16 v[60:61], v131 offset:41728\n" \
  "ds_read_b64_tr_b16 v[62:63], v131 offset:42752\n" \
  "v_mfma_f32_16x16x32_bf16 a[16:19], v[84:87], a[212:215], a[16:19]\n" \
  "v_mfma_f32_16x16x32_bf16 a[20:23], v[84:87], a[220:223], a[20:23]\n" \
  "v_mfma_f32_16x16x32_bf16 a[24:27], v[92:95], a[212:215], a[24:27]\n" \
  "v_mfma_f32_16x16x32_bf16 a[28:31], v[92:95], a[220:223], a[28:31]\n" \
  "v_mfma_f32_16x16x32_bf16 a[32:35], v[100:103], a[212:215], a[32:35]\n" \
  "v_mfma_f32_16x16x32_bf16 a[36:39], v[100:103], a[220:223], a[36:39]\n" \
  "v_mfma_f32_16x16x32_bf16 a[40:43], v[108:111], a[212:215], a[40:43]\n" \
  "v_mfma_f32_16x16x32_bf16 a[44:47], v[108:111], a[220:223], a[44:47]\n" \
  "v_mfma_f32_16x16x32_bf16 a[48:51], v[116:119], a[212:215], a[48:51]\n" \
  "v_mfma_f32_16x16x32_bf16 a[52:55], v[116:119], a[220:223], a[52:55]\n" \
  "v_mfma_f32_16x16x32_bf16 a[56:59], v[124:127], a[212:215], a[56:59]\n" \
  "v_mfma_f32_16x16x32_bf16 a[60:63], v[124:127], a[220:223], a[60:63]\n" \
  "s_waitcnt vmcnt(44) lgkmcnt(0)\n" \
  "s_barrier\n" \
  "v_mfma_f32_16x16x32_bf16 a[0:3], v[0:3], a[224:227], a[0:3]\n" \
  "ds_read_b64_tr_b16 v[64:65], v130 offset:49152\n" \
  "ds_read_b64_tr_b16 v[66:67], v130 offset:50176\n" \
  "v_mfma_f32_16x16x32_bf16 a[4:7], v[0:3], a[232:235], a[4:7]\n" \
  "ds_read_b64_tr_b16 v[72:73], v131 offset:49152\n" \
  "s_add_u32 m0, s72, 81920\n" \
  "s_nop 0\n" \
  "buffer_load_dwordx4 v136, s[44:47], s69 offen lds\n" \
  "v_mfma_f32_16x16x32_bf16 a[8:11], v[8:11], a[224:227], a[8:11]\n" \
  "ds_read_b64_tr_b16 v[74:75], v131 offset:50176\n" \
  "ds_read_b64_tr_b16 v[80:81], v130 offset:49408\n" \
  "v_mfma_f32_16x16x32_bf16 a[12:15], v[8:11], a[232:235], a[12:15]\n" \
  "ds_read_b64_tr_b16 v[82:83], v130 offset:50432\n" \
  "buffer_load_dwordx4 v137, s[44:47], s69 offen offset:1024 lds\n" \
  "v_mfma_f32_16x16x32_bf16 a[16:19], v[16:19], a[224:227], a[16:19]\n" \
  "ds_read_b64_tr_b16 v[88:89], v131 offset:49408\n" \
  "ds_read_b64_tr_b16 v[90:91], v131 offset:50432\n" \
  "v_mfma_f32_16x16x32_bf16 a[20:23], v[16:19], a[232:235], a[20:23]\n" \
  "ds_read_b64_tr_b16 v[96:97], v130 offset:49664\n" \
  "buffer_load_dwordx4 v138, s[44:47], s69 offen offset:2048 lds\n" \
  "v_mfma_f32_16x16x32_bf16 a[24:27], v[24:27], a[224:227], a[24:27]\n" \
  "ds_read_b64_tr_b16 v[98:99], v130 offset:50688\n" \
  "ds_read_b64_tr_b16 v[104:105], v131 offset:49664\n" \
  "v_mfma_f32_16x16x32_bf16 a[28:31], v[24:27], a[232:235], a[28:31]\n" \
  "ds_read_b64_tr_b16 v[106:107], v131 offset:50688\n" \
  "buffer_load_dwordx4 v139, s[44:47], s69 offen offset:3072 lds\n" \
  "s_add_u32 s69, s69, 0x10000\n" \
  "v_mfma_f32_16x16x32_bf16 a[32:35], v[32:35], a[224:227], a[32:35]\n" \
  "ds_read_b64_tr_b16 v[112:113], v130 offset:49920\n" \
  "ds_read_b64_tr_b16 v[114:115], v130 offset:50944\n" \
  "v_mfma_f32_16x16x32_bf16 a[36:39], v[32:35], a[232:235], a[36:39]\n" \
  "ds_read_b64_tr_b16 v[120:121], v131 offset:49920\n" \
  "buffer_load_dwordx4 a[208:211], v132, s[40:43], s68 offen offset:0\n" \
  "v_mfma_f32_16x16x32_bf16 a[40:43], v[40:43], a[224:227], a[40:43]\n" \
  "ds_read_b64_tr_b16 v[122:123], v131 offset:50944\n" \
  "ds_read_b64_tr_b16 v[68:69], v130 offset:57344\n" \
  "v_mfma_f32_16x16x32_bf16 a[44:47], v[40:43], a[232:235], a[44:47]\n" \
  "ds_read_b64_tr_b16 v[70:71], v130 offset:58368\n" \
  "buffer_load_dwordx4 a[216:219], v133, s[40:43], s68 offen offset:0\n" \
  "v_mfma_f32_16x16x32_bf16 a[48:51], v[48:51], a[224:227], a[48:51]\n" \
  "ds_read_b64_tr_b16 v[76:77], v131 offset:57344\n" \
  "ds_read_b64_tr_b16 v[78:79], v131 offset:58368\n" \
  "v_mfma_f32_16x16x32_bf16 a[52:55], v[48:51], a[232:235], a[52:55]\n" \
  "ds_read_b64_tr_b16 v[84:85], v130 offset:57600\n" \
  "buffer_load_dwordx4 a[212:215], v132, s[40:43], s68 offen offset:1024\n" \
  "v_mfma_f32_16x16x32_bf16 a[56:59], v[56:59], a[224:227], a[56:59]\n" \
  "ds_read_b64_tr_b16 v[86:87], v130 offset:58624\n" \
  "ds_read_b64_tr_b16 v[92:93], v131 offset:57600\n" \
  "v_mfma_f32_16x16x32_bf16 a[60:63], v[56:59], a[232:235], a[60:63]\n" \
  "ds_read_b64_tr_b16 v[94:95], v131 offset:58624\n" \
  "buffer_load_dwordx4 a[220:223], v133, s[40:43], s68 offen offset:1024\n" \
  "s_add_u32 s68, s68, 2048\n" \
  "v_mfma_f32_16x16x32_bf16 a[0:3], v[4:7], a[228:231], a[0:3]\n" \
  "ds_read_b64_tr_b16 v[100:101], v130 offset:57856\n" \
  "ds_read_b64_tr_b16 v[102:103], v130 offset:58880\n" \
  "v_mfma_f32_16x16x32_bf16 a[4:7], v[4:7], a[236:239], a[4:7]\n" \
  "ds_read_b64_tr_b16 v[108:109], v131 offset:57856\n" \
  "ds_read_b64_tr_b16 v[110:111], v131 offset:58880\n" \
  "v_mfma_f32_16x16x32_bf16 a[8:11], v[12:15], a[228:231], a[8:11]\n" \
  "ds_read_b64_tr_b16 v[116:117], v130 offset:58112\n" \
  "ds_read_b64_tr_b16 v[118:119], v130 offset:59136\n" \
  "v_mfma_f32_16x16x32_bf16 a[12:15], v[12:15], a[236:239], a[12:15]\n" \
  "ds_read_b64_tr_b16 v[124:125], v131 offset:58112\n" \
  "ds_read_b64_tr_b16 v[126:127], v131 offset:59136\n" \
  "v_mfma_f32_16x16x32_bf16 a[16:19], v[20:23], a[228:231], a[16:19]\n" \
  "v_mfma_f32_16x16x32_bf16 a[20:23], v[20:23], a[236:239], a[20:23]\n" \
  "v_mfma_f32_16x16x32_bf16 a[24:27], v[28:31], a[228:231], a[24:27]\n" \
  "v_mfma_f32_16x16x32_bf16 a[28:31], v[28:31], a[236:239], a[28:31]\n" \
  "v_mfma_f32_16x16x32_bf16 a[32:35], v[36:39], a[228:231], a[32:35]\n" \
  "v_mfma_f32_16x16x32_bf16 a[36:39], v[36:39], a[236:239], a[36:39]\n" \
  "v_mfma_f32_16x16x32_bf16 a[40:43], v[44:47], a[228:231], a[40:43]\n" \
  "v_mfma_f32_16x16x32_bf16 a[44:47], v[44:47], a[236:239], a[44:47]\n" \
  "v_mfma_f32_16x16x32_bf16 a[48:51], v[52:55], a[228:231], a[48:51]\n" \
  "v_mfma_f32_16x16x32_bf16 a[52:55], v[52:55], a[236:239], a[52:55]\n" \
  "v_mfma_f32_16x16x32_bf16 a[56:59], v[60:63], a[228:231], a[56:59]\n" \
  "v_mfma_f32_16x16x32_bf16 a[60:63], v[60:63], a[236:239], a[60:63]\n" \
  "s_waitcnt vmcnt(44) lgkmcnt(0)\n" \
  "s_barrier\n" \
  "v_mfma_f32_16x16x32_bf16 a[0:3], v[64:67], a[240:243], a[0:3]\n" \
  "ds_read_b64_tr_b16 v[0:1], v128 offset:0\n" \
  "ds_read_b64_tr_b16 v[2:3], v128 offset:1024\n" \
  "v_mfma_f32_16x16x32_bf16 a[4:7], v[64:67], a[248:251], a[4:7]\n" \
  "ds_read_b64_tr_b16 v[8:9], v129 offset:0\n" \
  "s_add_u32 m0, s72, 98304\n" \
  "s_nop 0\n" \
  "buffer_load_dwordx4 v136, s[44:47], s69 offen lds\n" \
  "v_mfma_f32_16x16x32_bf16 a[8:11], v[72:75], a[240:243], a[8:11]\n" \
  "ds_read_b64_tr_b16 v[10:11], v129 offset:1024\n" \
  "ds_read_b64_tr_b16 v[16:17], v128 offset:256\n" \
  "v_mfma_f32_16x16x32_bf16 a[12:15], v[72:75], a[248:251], a[12:15]\n" \
  "ds_read_b64_tr_b16 v[18:19], v128 offset:1280\n" \
  "buffer_load_dwordx4 v137, s[44:47], s69 offen offset:1024 lds\n" \
  "v_mfma_f32_16x16x32_bf16 a[16:19], v[80:83], a[240:243], a[16:19]\n" \
  "ds_read_b64_tr_b16 v[24:25], v129 offset:256\n" \
  "ds_read_b64_tr_b16 v[26:27], v129 offset:1280\n" \
  "v_mfma_f32_16x16x32_bf16 a[20:23], v[80:83], a[248:251], a[20:23]\n" \
  "ds_read_b64_tr_b16 v[32:33], v128 offset:512\n" \
  "buffer_load_dwordx4 v138, s[44:47], s69 offen offset:2048 lds\n" \
  "v_mfma_f32_16x16x32_bf16 a[24:27], v[88:91], a[240:243], a[24:27]\n" \
  "ds_read_b64_tr_b16 v[34:35], v128 offset:1536\n" \
  "ds_read_b64_tr_b16 v[40:41], v129 offset:512\n" \
  "v_mfma_f32_16x16x32_bf16 a[28:31], v[88:91], a[248:251], a[28:31]\n" \
  "ds_read_b64_tr_b16 v[42:43], v129 offset:1536\n" \
  "buffer_load_dwordx4 v139, s[44:47], s69 offen offset:3072 lds\n" \
  "s_add_u32 s69, s69, 0x10000\n" \
  "v_mfma_f32_16x16x32_bf16 a[32:35], v[96:99], a[240:243], a[32:35]\n" \
  "ds_read_b64_tr_b16 v[48:49], v128 offset:768\n" \
  "ds_read_b64_tr_b16 v[50:51], v128 offset:1792\n" \
  "v_mfma_f32_16x16x32_bf16 a[36:39], v[96:99], a[248:251], a[36:39]\n" \
  "ds_read_b64_tr_b16 v[56:57], v129 offset:768\n" \
  "buffer_load_dwordx4 a[224:227], v132, s[40:43], s68 offen offset:0\n" \
  "v_mfma_f32_16x16x32_bf16 a[40:43], v[104:107], a[240:243], a[40:43]\n" \
  "ds_read_b64_tr_b16 v[58:59], v129 offset:1792\n" \
  "ds_read_b64_tr_b16 v[4:5], v128 offset:8192\n" \
  "v_mfma_f32_16x16x32_bf16 a[44:47], v[104:107], a[248:251], a[44:47]\n" \
  "ds_read_b64_tr_b16 v[6:7], v128 offset:9216\n" \
  "buffer_load_dwordx4 a[232:235], v133, s[40:43], s68 offen offset:0\n" \
  "v_mfma_f32_16x16x32_bf16 a[48:51], v[112:115], a[240:243], a[48:51]\n" \
  "ds_read_b64_tr_b16 v[12:13], v129 offset:8192\n" \
  "ds_read_b64_tr_b16 v[14:15], v129 offset:9216\n" \
  "v_mfma_f32_16x16x32_bf16 a[52:55], v[112:115], a[248:251], a[52:55]\n" \
  "ds_read_b64_tr_b16 v[20:21], v128 offset:8448\n" \
  "buffer_load_dwordx4 a[228:231], v132, s[40:43], s68 offen offset:1024\n" \
  "v_mfma_f32_16x16x32_bf16 a[56:59], v[120:123], a[240:243], a[56:59]\n" \
  "ds_read_b64_tr_b16 v[22:23], v128 offset:9472\n" \
  "ds_read_b64_tr_b16 v[28:29], v129 offset:8448\n" \
  "v_mfma_f32_16x16x32_bf16 a[60:63], v[120:123], a[248:251], a[60:63]\n" \
  "ds_read_b64_tr_b16 v[30:31], v129 offset:9472\n" \
  "buffer_load_dwordx4 a[236:239], v133, s[40:43], s68 offen offset:1024\n" \
  "s_add_u32 s68, s68, 2048\n" \
  "v_mfma_f32_16x16x32_bf16 a[0:3], v[68:71], a[244:247], a[0:3]\n" \
  "ds_read_b64_tr_b16 v[36:37], v128 offset:8704\n" \
  "ds_read_b64_tr_b16 v[38:39], v128 offset:9728\n" \
  "v_mfma_f32_16x16x32_bf16 a[4:7], v[68:71], a[252:255], a[4:7]\n" \
  "ds_read_b64_tr_b16 v[44:45], v129 offset:8704\n" \
  "ds_read_b64_tr_b16 v[46:47], v129 offset:9728\n" \
  "v_mfma_f32_16x16x32_bf16 a[8:11], v[76:79], a[244:247], a[8:11]\n" \
  "ds_read_b64_tr_b16 v[52:53], v128 offset:8960\n" \
  "ds_read_b64_tr_b16 v[54:55], v128 offset:9984\n" \
  "v_mfma_f32_16x16x32_bf16 a[12:15], v[76:79], a[252:255], a[12:15]\n" \
  "ds_read_b64_tr_b16 v[60:61], v129 offset:8960\n" \
  "ds_read_b64_tr_b16 v[62:63], v129 offset:9984\n" \
  "v_mfma_f32_16x16x32_bf16 a[16:19], v[84:87], a[244:247], a[16:19]\n" \
  "v_mfma_f32_16x16x32_bf16 a[20:23], v[84:87], a[252:255], a[20:23]\n" \
  "v_mfma_f32_16x16x32_bf16 a[24:27], v[92:95], a[244:247], a[24:27]\n" \
  "v_mfma_f32_16x16x32_bf16 a[28:31], v[92:95], a[252:255], a[28:31]\n" \
  "v_mfma_f32_16x16x32_bf16 a[32:35], v[100:103], a[244:247], a[32:35]\n" \
  "v_mfma_f32_16x16x32_bf16 a[36:39], v[100:103], a[252:255], a[36:39]\n" \
  "v_mfma_f32_16x16x32_bf16 a[40:43], v[108:111], a[244:247], a[40:43]\n" \
  "v_mfma_f32_16x16x32_bf16 a[44:47], v[108:111], a[252:255], a[44:47]\n" \
  "v_mfma_f32_16x16x32_bf16 a[48:51], v[116:119], a[244:247], a[48:51]\n" \
  "v_mfma_f32_16x16x32_bf16 a[52:55], v[116:119], a[252:255], a[52:55]\n" \
  "v_mfma_f32_16x16x32_bf16 a[56:59], v[124:127], a[244:247], a[56:59]\n" \
  "v_mfma_f32_16x16x32_bf16 a[60:63], v[124:127], a[252:255], a[60:63]\n" \
  "s_waitcnt vmcnt(44) lgkmcnt(0)\n" \
  "s_barrier\n" \
  "v_mfma_f32_16x16x32_bf16 a[0:3], v[0:3], a[128:131], a[0:3]\n" \
  "ds_read_b64_tr_b16 v[64:65], v128 offset:16384\n" \
  "ds_read_b64_tr_b16 v[66:67], v128 offset:17408\n" \
  "v_mfma_f32_16x16x32_bf16 a[4:7], v[0:3], a[136:139], a[4:7]\n" \
  "ds_read_b64_tr_b16 v[72:73], v129 offset:16384\n" \
  "s_add_u32 m0, s72, 114688\n" \
  "s_nop 0\n" \
  "buffer_load_dwordx4 v136, s[44:47], s69 offen lds\n" \
  "v_mfma_f32_16x16x32_bf16 a[8:11], v[8:11], a[128:131], a[8:11]\n" \
  "ds_read_b64_tr_b16 v[74:75], v129 offset:17408\n" \
  "ds_read_b64_tr_b16 v[80:81], v128 offset:16640\n" \
  "v_mfma_f32_16x16x32_bf16 a[12:15], v[8:11], a[136:139], a[12:15]\n" \
  "ds_read_b64_tr_b16 v[82:83], v128 offset:17664\n" \
  "buffer_load_dwordx4 v137, s[44:47], s69 offen offset:1024 lds\n" \
  "v_mfma_f32_16x16x32_bf16 a[16:19], v[16:19], a[128:131], a[16:19]\n" \
  "ds_read_b64_tr_b16 v[88:89], v129 offset:16640\n" \
  "ds_read_b64_tr_b16 v[90:91], v129 offset:17664\n" \
  "v_mfma_f32_16x16x32_bf16 a[20:23], v[16:19], a[136:139], a[20:23]\n" \
  "ds_read_b64_tr_b16 v[96:97], v128 offset:16896\n" \
  "buffer_load_dwordx4 v138, s[44:47], s69 offen offset:2048 lds\n" \
  "v_mfma_f32_16x16x32_bf16 a[24:27], v[24:27], a[128:131], a[24:27]\n" \
  "ds_read_b64_tr_b16 v[98:99], v128 offset:17920\n" \
  "ds_read_b64_tr_b16 v[104:105], v129 offset:16896\n" \
  "v_mfma_f32_16x16x32_bf16 a[28:31], v[24:27], a[136:139], a[28:31]\n" \
  "ds_read_b64_tr_b16 v[106:107], v129 offset:17920\n" \
  "buffer_load_dwordx4 v139, s[44:47], s69 offen offset:3072 lds\n" \
  "s_add_u32 s69, s69, 0x10000\n" \
  "v_mfma_f32_16x16x32_bf16 a[32:35], v[32:35], a[128:131], a[32:35]\n" \
  "ds_read_b64_tr_b16 v[112:113], v128 offset:17152\n" \
  "ds_read_b64_tr_b16 v[114:115], v128 offset:18176\n" \
  "v_mfma_f32_16x16x32_bf16 a[36:39], v[32:35], a[136:139], a[36:39]\n" \
  "ds_read_b64_tr_b16 v[120:121], v129 offset:17152\n" \
  "buffer_load_dwordx4 a[240:243], v132, s[40:43], s68 offen offset:0\n" \
  "v_mfma_f32_16x16x32_bf16 a[40:43], v[40:43], a[128:131], a[40:43]\n" \
  "ds_read_b64_tr_b16 v[122:123], v129 offset:18176\n" \
  "ds_read_b64_tr_b16 v[68:69], v128 offset:24576\n" \
  "v_mfma_f32_16x16x32_bf16 a[44:47], v[40:43], a[136:139], a[44:47]\n" \
  "ds_read_b64_tr_b16 v[70:71], v128 offset:25600\n" \
  "buffer_load_dwordx4 a[248:251], v133, s[40:43], s68 offen offset:0\n" \
  "v_mfma_f32_16x16x32_bf16 a[48:51], v[48:51], a[128:131], a[48:51]\n" \
  "ds_read_b64_tr_b16 v[76:77], v129 offset:24576\n" \
  "ds_read_b64_tr_b16 v[78:79], v129 offset:25600\n" \
  "v_mfma_f32_16x16x32_bf16 a[52:55], v[48:51], a[136:139], a[52:55]\n" \
  "ds_read_b64_tr_b16 v[84:85], v128 offset:24832\n" \
  "buffer_load_dwordx4 a[244:247], v132, s[40:43], s68 offen offset:1024\n" \
  "v_mfma_f32_16x16x32_bf16 a[56:59], v[56:59], a[128:131], a[56:59]\n" \
  "ds_read_b64_tr_b16 v[86:87], v128 offset:25856\n" \
  "ds_read_b64_tr_b16 v[92:93], v129 offset:24832\n" \
  "v_mfma_f32_16x16x32_bf16 a[60:63], v[56:59], a[136:139], a[60:63]\n" \
  "ds_read_b64_tr_b16 v[94:95], v129 offset:25856\n" \
  "buffer_load_dwordx4 a[252:255], v133, s[40:43], s68 offen offset:1024\n" \
  "s_add_u32 s68, s68, 2048\n" \
  "v_mfma_f32_16x16x32_bf16 a[0:3], v[4:7], a[132:135], a[0:3]\n" \
  "ds_read_b64_tr_b16 v[100:101], v128 offset:25088\n" \
  "ds_read_b64_tr_b16 v[102:103], v128 offset:26112\n" \
  "v_mfma_f32_16x16x32_bf16 a[4:7], v[4:7], a[140:143], a[4:7]\n" \
  "ds_read_b64_tr_b16 v[108:109], v129 offset:25088\n" \
  "ds_read_b64_tr_b16 v[110:111], v129 offset:26112\n" \
  "v_mfma_f32_16x16x32_bf16 a[8:11], v[12:15], a[132:135], a[8:11]\n" \
  "ds_read_b64_tr_b16 v[116:117], v128 offset:25344\n" \
  "ds_read_b64_tr_b16 v[118:119], v128 offset:26368\n" \
  "v_mfma_f32_16x16x32_bf16 a[12:15], v[12:15], a[140:143], a[12:15]\n" \
  "ds_read_b64_tr_b16 v[124:125], v129 offset:25344\n" \
  "ds_read_b64_tr_b16 v[126:127], v129 offset:26368\n" \
  "v_mfma_f32_16x16x32_bf16 a[16:19], v[20:23], a[132:135], a[16:19]\n" \
  "v_mfma_f32_16x16x32_bf16 a[20:23], v[20:23], a[140:143], a[20:23]\n" \
  "v_mfma_f32_16x16x32_bf16 a[24:27], v[28:31], a[132:135], a[24:27]\n" \
  "v_mfma_f32_16x16x32_bf16 a[28:31], v[28:31], a[140:143], a[28:31]\n" \
  "v_mfma_f32_16x16x32_bf16 a[32:35], v[36:39], a[132:135], a[32:35]\n" \
  "v_mfma_f32_16x16x32_bf16 a[36:39], v[36:39], a[140:143], a[36:39]\n" \
  "v_mfma_f32_16x16x32_bf16 a[40:43], v[44:47], a[132:135], a[40:43]\n" \
  "v_mfma_f32_16x16x32_bf16 a[44:47], v[44:47], a[140:143], a[44:47]\n" \
  "v_mfma_f32_16x16x32_bf16 a[48:51], v[52:55], a[132:135], a[48:51]\n" \
  "v_mfma_f32_16x16x32_bf16 a[52:55], v[52:55], a[140:143], a[52:55]\n" \
  "v_mfma_f32_16x16x32_bf16 a[56:59], v[60:63], a[132:135], a[56:59]\n" \
  "v_mfma_f32_16x16x32_bf16 a[60:63], v[60:63], a[140:143], a[60:63]\n" \
  "s_waitcnt vmcnt(44) lgkmcnt(0)\n" \
  "s_barrier\n" \
  "v_mfma_f32_16x16x32_bf16 a[0:3], v[64:67], a[144:147], a[0:3]\n" \
  "ds_read_b64_tr_b16 v[0:1], v128 offset:32768\n" \
  "ds_read_b64_tr_b16 v[2:3], v128 offset:33792\n" \
  "v_mfma_f32_16x16x32_bf16 a[4:7], v[64:67], a[152:155], a[4:7]\n" \
  "ds_read_b64_tr_b16 v[8:9], v129 offset:32768\n" \
  "s_add_u32 m0, s72, 0\n" \
  "s_nop 0\n" \
  "buffer_load_dwordx4 v136, s[44:47], s69 offen lds\n" \
  "v_mfma_f32_16x16x32_bf16 a[8:11], v[72:75], a[144:147], a[8:11]\n" \
  "ds_read_b64_tr_b16 v[10:11], v129 offset:33792\n" \
  "ds_read_b64_tr_b16 v[16:17], v128 offset:33024\n" \
  "v_mfma_f32_16x16x32_bf16 a[12:15], v[72:75], a[152:155], a[12:15]\n" \
  "ds_read_b64_tr_b16 v[18:19], v128 offset:34048\n" \
  "buffer_load_dwordx4 v137, s[44:47], s69 offen offset:1024 lds\n" \
  "v_mfma_f32_16x16x32_bf16 a[16:19], v[80:83], a[144:147], a[16:19]\n" \
  "ds_read_b64_tr_b16 v[24:25], v129 offset:33024\n" \
  "ds_read_b64_tr_b16 v[26:27], v129 offset:34048\n" \
  "v_mfma_f32_16x16x32_bf16 a[20:23], v[80:83], a[152:155], a[20:23]\n" \
  "ds_read_b64_tr_b16 v[32:33], v128 offset:33280\n" \
  "buffer_load_dwordx4 v138, s[44:47], s69 offen offset:2048 lds\n" \
  "v_mfma_f32_16x16x32_bf16 a[24:27], v[88:91], a[144:147], a[24:27]\n" \
  "ds_read_b64_tr_b16 v[34:35], v128 offset:34304\n" \
  "ds_read_b64_tr_b16 v[40:41], v129 offset:33280\n" \
  "v_mfma_f32_16x16x32_bf16 a[28:31], v[88:91], a[152:155], a[28:31]\n" \
  "ds_read_b64_tr_b16 v[42:43], v129 offset:34304\n" \
  "buffer_load_dwordx4 v139, s[44:47], s69 offen offset:3072 lds\n" \
  "s_add_u32 s69, s69, 0x10000\n" \
  "v_mfma_f32_16x16x32_bf16 a[32:35], v[96:99], a[144:147], a[32:35]\n" \
  "ds_read_b64_tr_b16 v[48:49], v128 offset:33536\n" \
  "ds_read_b64_tr_b16 v[50:51], v128 offset:34560\n" \
  "v_mfma_f32_16x16x32_bf16 a[36:39], v[96:99], a[152:155], a[36:39]\n" \
  "ds_read_b64_tr_b16 v[56:57], v129 offset:33536\n" \
  "buffer_load_dwordx4 a[128:131], v132, s[40:43], s68 offen offset:0\n" \
  "v_mfma_f32_16x16x32_bf16 a[40:43], v[104:107], a[144:147], a[40:43]\n" \
  "ds_read_b64_tr_b16 v[58:59], v129 offset:34560\n" \
  "ds_read_b64_tr_b16 v[4:5], v128 offset:40960\n" \
  "v_mfma_f32_16x16x32_bf16 a[44:47], v[104:107], a[152:155], a[44:47]\n" \
  "ds_read_b64_tr_b16 v[6:7], v128 offset:41984\n" \
  "buffer_load_dwordx4 a[136:139], v133, s[40:43], s68 offen offset:0\n" \
  "v_mfma_f32_16x16x32_bf16 a[48:51], v[112:115], a[144:147], a[48:51]\n" \
  "ds_read_b64_tr_b16 v[12:13], v129 offset:40960\n" \
  "ds_read_b64_tr_b16 v[14:15], v129 offset:41984\n" \
  "v_mfma_f32_16x16x32_bf16 a[52:55], v[112:115], a[152:155], a[52:55]\n" \
  "ds_read_b64_tr_b16 v[20:21], v128 offset:41216\n" \
  "buffer_load_dwordx4 a[132:135], v132, s[40:43], s68 offen offset:1024\n" \
  "v_mfma_f32_16x16x32_bf16 a[56:59], v[120:123], a[144:147], a[56:59]\n" \
  "ds_read_b64_tr_b16 v[22:23], v128 offset:42240\n" \
  "ds_read_b64_tr_b16 v[28:29], v129 offset:41216\n" \
  "v_mfma_f32_16x16x32_bf16 a[60:63], v[120:123], a[152:155], a[60:63]\n" \
  "ds_read_b64_tr_b16 v[30:31], v129 offset:42240\n" \
  "buffer_load_dwordx4 a[140:143], v133, s[40:43], s68 offen offset:1024\n" \
  "s_add_u32 s68, s68, 2048\n" \
  "v_mfma_f32_16x16x32_bf16 a[0:3], v[68:71], a[148:151], a[0:3]\n" \
  "ds_read_b64_tr_b16 v[36:37], v128 offset:41472\n" \
  "ds_read_b64_tr_b16 v[38:39], v128 offset:42496\n" \
  "v_mfma_f32_16x16x32_bf16 a[4:7], v[68:71], a[156:159], a[4:7]\n" \
  "ds_read_b64_tr_b16 v[44:45], v129 offset:41472\n" \
  "ds_read_b64_tr_b16 v[46:47], v129 offset:42496\n" \
  "v_mfma_f32_16x16x32_bf16 a[8:11], v[76:79], a[148:151], a[8:11]\n" \
  "ds_read_b64_tr_b16 v[52:53], v128 offset:41728\n" \
  "ds_read_b64_tr_b16 v[54:55], v128 offset:42752\n" \
  "v_mfma_f32_16x16x32_bf16 a[12:15], v[76:79], a[156:159], a[12:15]\n" \
  "ds_read_b64_tr_b16 v[60:61], v129 offset:41728\n" \
  "ds_read_b64_tr_b16 v[62:63], v129 offset:42752\n" \
  "v_mfma_f32_16x16x32_bf16 a[16:19], v[84:87], a[148:151], a[16:19]\n" \
  "v_mfma_f32_16x16x32_bf16 a[20:23], v[84:87], a[156:159], a[20:23]\n" \
  "v_mfma_f32_16x16x32_bf16 a[24:27], v[92:95], a[148:151], a[24:27]\n" \
  "v_mfma_f32_16x16x32_bf16 a[28:31], v[92:95], a[156:159], a[28:31]\n" \
  "v_mfma_f32_16x16x32_bf16 a[32:35], v[100:103], a[148:151], a[32:35]\n" \
  "v_mfma_f32_16x16x32_bf16 a[36:39], v[100:103], a[156:159], a[36:39]\n" \
  "v_mfma_f32_16x16x32_bf16 a[40:43], v[108:111], a[148:151], a[40:43]\n" \
  "v_mfma_f32_16x16x32_bf16 a[44:47], v[108:111], a[156:159], a[44:47]\n" \
  "v_mfma_f32_16x16x32_bf16 a[48:51], v[116:119], a[148:151], a[48:51]\n" \
  "v_mfma_f32_16x16x32_bf16 a[52:55], v[116:119], a[156:159], a[52:55]\n" \
  "v_mfma_f32_16x16x32_bf16 a[56:59], v[124:127], a[148:151], a[56:59]\n" \
  "v_mfma_f32_16x16x32_bf16 a[60:63], v[124:127], a[156:159], a[60:63]\n" \
  "s_waitcnt vmcnt(44) lgkmcnt(0)\n" \
  "s_barrier\n" \
  "v_mfma_f32_16x16x32_bf16 a[0:3], v[0:3], a[160:163], a[0:3]\n" \
  "ds_read_b64_tr_b16 v[64:65], v128 offset:49152\n" \
  "ds_read_b64_tr_b16 v[66:67], v128 offset:50176\n" \
  "v_mfma_f32_16x16x32_bf16 a[4:7], v[0:3], a[168:171], a[4:7]\n" \
  "ds_read_b64_tr_b16 v[72:73], v129 offset:49152\n" \
  "s_add_u32 m0, s72, 16384\n" \
  "s_nop 0\n" \
  "buffer_load_dwordx4 v136, s[44:47], s69 offen lds\n" \
  "v_mfma_f32_16x16x32_bf16 a[8:11], v[8:11], a[160:163], a[8:11]\n" \
  "ds_read_b64_tr_b16 v[74:75], v129 offset:50176\n" \
  "ds_read_b64_tr_b16 v[80:81], v128 offset:49408\n" \
  "v_mfma_f32_16x16x32_bf16 a[12:15], v[8:11], a[168:171], a[12:15]\n" \
  "ds_read_b64_tr_b16 v[82:83], v128 offset:50432\n" \
  "buffer_load_dwordx4 v137, s[44:47], s69 offen offset:1024 lds\n" \
  "v_mfma_f32_16x16x32_bf16 a[16:19], v[16:19], a[160:163], a[16:19]\n" \
  "ds_read_b64_tr_b16 v[88:89], v129 offset:49408\n" \
  "ds_read_b64_tr_b16 v[90:91], v129 offset:50432\n" \
  "v_mfma_f32_16x16x32_bf16 a[20:23], v[16:19], a[168:171], a[20:23]\n" \
  "ds_read_b64_tr_b16 v[96:97], v128 offset:49664\n" \
  "buffer_load_dwordx4 v138, s[44:47], s69 offen offset:2048 lds\n" \
  "v_mfma_f32_16x16x32_bf16 a[24:27], v[24:27], a[160:163], a[24:27]\n" \
  "ds_read_b64_tr_b16 v[98:99], v128 offset:50688\n" \
  "ds_read_b64_tr_b16 v[104:105], v129 offset:49664\n" \
  "v_mfma_f32_16x16x32_bf16 a[28:31], v[24:27], a[168:171], a[28:31]\n" \
  "ds_read_b64_tr_b16 v[106:107], v129 offset:50688\n" \
  "buffer_load_dwordx4 v139, s[44:47], s69 offen offset:3072 lds\n" \
  "s_add_u32 s69, s69, 0x10000\n" \
  "v_mfma_f32_16x16x32_bf16 a[32:35], v[32:35], a[160:163], a[32:35]\n" \
  "ds_read_b64_tr_b16 v[112:113], v128 offset:49920\n" \
  "ds_read_b64_tr_b16 v[114:115], v128 offset:50944\n" \
  "v_mfma_f32_16x16x32_bf16 a[36:39], v[32:35], a[168:171], a[36:39]\n" \
  "ds_read_b64_tr_b16 v[120:121], v129 offset:49920\n" \
  "buffer_load_dwordx4 a[144:147], v132, s[40:43], s68 offen offset:0\n" \
  "v_mfma_f32_16x16x32_bf16 a[40:43], v[40:43], a[160:163], a[40:43]\n" \
  "ds_read_b64_tr_b16 v[122:123], v129 offset:50944\n" \
  "ds_read_b64_tr_b16 v[68:69], v128 offset:57344\n" \
  "v_mfma_f32_16x16x32_bf16 a[44:47], v[40:43], a[168:171], a[44:47]\n" \
  "ds_read_b64_tr_b16 v[70:71], v128 offset:58368\n" \
  "buffer_load_dwordx4 a[152:155], v133, s[40:43], s68 offen offset:0\n" \
  "v_mfma_f32_16x16x32_bf16 a[48:51], v[48:51], a[160:163], a[48:51]\n" \
  "ds_read_b64_tr_b16 v[76:77], v129 offset:57344\n" \
  "ds_read_b64_tr_b16 v[78:79], v129 offset:58368\n" \
  "v_mfma_f32_16x16x32_bf16 a[52:55], v[48:51], a[168:171], a[52:55]\n" \
  "ds_read_b64_tr_b16 v[84:85], v128 offset:57600\n" \
  "buffer_load_dwordx4 a[148:151], v132, s[40:43], s68 offen offset:1024\n" \
  "v_mfma_f32_16x16x32_bf16 a[56:59], v[56:59], a[160:163], a[56:59]\n" \
  "ds_read_b64_tr_b16 v[86:87], v128 offset:58624\n" \
  "ds_read_b64_tr_b16 v[92:93], v129 offset:57600\n" \
  "v_mfma_f32_16x16x32_bf16 a[60:63], v[56:59], a[168:171], a[60:63]\n" \
  "ds_read_b64_tr_b16 v[94:95], v129 offset:58624\n" \
  "buffer_load_dwordx4 a[156:159], v133, s[40:43], s68 offen offset:1024\n" \
  "s_add_u32 s68, s68, 2048\n" \
  "v_mfma_f32_16x16x32_bf16 a[0:3], v[4:7], a[164:167], a[0:3]\n" \
  "ds_read_b64_tr_b16 v[100:101], v128 offset:57856\n" \
  "ds_read_b64_tr_b16 v[102:103], v128 offset:58880\n" \
  "v_mfma_f32_16x16x32_bf16 a[4:7], v[4:7], a[172:175], a[4:7]\n" \
  "ds_read_b64_tr_b16 v[108:109], v129 offset:57856\n" \
  "ds_read_b64_tr_b16 v[110:111], v129 offset:58880\n" \
  "v_mfma_f32_16x16x32_bf16 a[8:11], v[12:15], a[164:167], a[8:11]\n" \
  "ds_read_b64_tr_b16 v[116:117], v128 offset:58112\n" \
  "ds_read_b64_tr_b16 v[118:119], v128 offset:59136\n" \
  "v_mfma_f32_16x16x32_bf16 a[12:15], v[12:15], a[172:175], a[12:15]\n" \
  "ds_read_b64_tr_b16 v[124:125], v129 offset:58112\n" \
  "ds_read_b64_tr_b16 v[126:127], v129 offset:59136\n" \
  "v_mfma_f32_16x16x32_bf16 a[16:19], v[20:23], a[164:167], a[16:19]\n" \
  "v_mfma_f32_16x16x32_bf16 a[20:23], v[20:23], a[172:175], a[20:23]\n" \
  "v_mfma_f32_16x16x32_bf16 a[24:27], v[28:31], a[164:167], a[24:27]\n" \
  "v_mfma_f32_16x16x32_bf16 a[28:31], v[28:31], a[172:175], a[28:31]\n" \
  "v_mfma_f32_16x16x32_bf16 a[32:35], v[36:39], a[164:167], a[32:35]\n" \
  "v_mfma_f32_16x16x32_bf16 a[36:39], v[36:39], a[172:175], a[36:39]\n" \
  "v_mfma_f32_16x16x32_bf16 a[40:43], v[44:47], a[164:167], a[40:43]\n" \
  "v_mfma_f32_16x16x32_bf16 a[44:47], v[44:47], a[172:175], a[44:47]\n" \
  "v_mfma_f32_16x16x32_bf16 a[48:51], v[52:55], a[164:167], a[48:51]\n" \
  "v_mfma_f32_16x16x32_bf16 a[52:55], v[52:55], a[172:175], a[52:55]\n" \
  "v_mfma_f32_16x16x32_bf16 a[56:59], v[60:63], a[164:167], a[56:59]\n" \
  "v_mfma_f32_16x16x32_bf16 a[60:63], v[60:63], a[172:175], a[60:63]\n" \
  "s_waitcnt vmcnt(44) lgkmcnt(0)\n" \
  "s_barrier\n" \
  "v_mfma_f32_16x16x32_bf16 a[0:3], v[64:67], a[176:179], a[0:3]\n" \
  "ds_read_b64_tr_b16 v[0:1], v130 offset:0\n" \
  "ds_read_b64_tr_b16 v[2:3], v130 offset:1024\n" \
  "v_mfma_f32_16x16x32_bf16 a[4:7], v[64:67], a[184:187], a[4:7]\n" \
  "ds_read_b64_tr_b16 v[8:9], v131 offset:0\n" \
  "s_cmp_lg_u32 s74, 2\n" \
  "s_cbranch_scc1 SW%=\n" \
  "s_mov_b32 s40, s48\n" \
  "s_mov_b32 s44, s52\n" \
  "s_mov_b32 s41, s49\n" \
  "s_mov_b32 s45, s53\n" \
  "s_mov_b32 s42, s50\n" \
  "s_mov_b32 s46, s54\n" \
  "s_mov_b32 s43, s51\n" \
  "s_mov_b32 s47, s55\n" \
  "s_mov_b32 s68, s70\n" \
  "s_mov_b32 s69, s71\n" \
  "v_mov_b32 v132, v134\n" \
  "v_mov_b32 v133, v135\n" \
  "SW%=:\n" \
  "v_mfma_f32_16x16x32_bf16 a[8:11], v[72:75], a[176:179], a[8:11]\n" \
  "ds_read_b64_tr_b16 v[10:11], v131 offset:1024\n" \
  "ds_read_b64_tr_b16 v[16:17], v130 offset:256\n" \
  "v_mfma_f32_16x16x32_bf16 a[12:15], v[72:75], a[184:187], a[12:15]\n" \
  "ds_read_b64_tr_b16 v[18:19], v130 offset:1280\n" \
  "s_add_u32 m0, s72, 32768\n" \
  "s_nop 0\n" \
  "buffer_load_dwordx4 v136, s[44:47], s69 offen lds\n" \
  "v_mfma_f32_16x16x32_bf16 a[16:19], v[80:83], a[176:179], a[16:19]\n" \
  "ds_read_b64_tr_b16 v[24:25], v131 offset:256\n" \
  "ds_read_b64_tr_b16 v[26:27], v131 offset:1280\n" \
  "v_mfma_f32_16x16x32_bf16 a[20:23], v[80:83], a[184:187], a[20:23]\n" \
  "ds_read_b64_tr_b16 v[32:33], v130 offset:512\n" \
  "buffer_load_dwordx4 v137, s[44:47], s69 offen offset:1024 lds\n" \
  "v_mfma_f32_16x16x32_bf16 a[24:27], v[88:91], a[176:179], a[24:27]\n" \
  "ds_read_b64_tr_b16 v[34:35], v130 offset:1536\n" \
  "ds_read_b64_tr_b16 v[40:41], v131 offset:512\n" \
  "v_mfma_f32_16x16x32_bf16 a[28:31], v[88:91], a[184:187], a[28:31]\n" \
  "ds_read_b64_tr_b16 v[42:43], v131 offset:1536\n" \
  "buffer_load_dwordx4 v138, s[44:47], s69 offen offset:2048 lds\n" \
  "v_mfma_f32_16x16x32_bf16 a[32:35], v[96:99], a[176:179], a[32:35]\n" \
  "ds_read_b64_tr_b16 v[48:49], v130 offset:768\n" \
  "ds_read_b64_tr_b16 v[50:51], v130 offset:1792\n" \
  "v_mfma_f32_16x16x32_bf16 a[36:39], v[96:99], a[184:187], a[36:39]\n" \
  "ds_read_b64_tr_b16 v[56:57], v131 offset:768\n" \
  "buffer_load_dwordx4 v139, s[44:47], s69 offen offset:3072 lds\n" \
  "s_add_u32 s69, s69, 0x10000\n" \
  "v_mfma_f32_16x16x32_bf16 a[40:43], v[104:107], a[176:179], a[40:43]\n" \
  "ds_read_b64_tr_b16 v[58:59], v131 offset:1792\n" \
  "ds_read_b64_tr_b16 v[4:5], v130 offset:8192\n" \
  "v_mfma_f32_16x16x32_bf16 a[44:47], v[104:107], a[184:187], a[44:47]\n" \
  "ds_read_b64_tr_b16 v[6:7], v130 offset:9216\n" \
  "buffer_load_dwordx4 a[160:163], v132, s[40:43], s68 offen offset:0\n" \
  "v_mfma_f32_16x16x32_bf16 a[48:51], v[112:115], a[176:179], a[48:51]\n" \
  "ds_read_b64_tr_b16 v[12:13], v131 offset:8192\n" \
  "ds_read_b64_tr_b16 v[14:15], v131 offset:9216\n" \
  "v_mfma_f32_16x16x32_bf16 a[52:55], v[112:115], a[184:187], a[52:55]\n" \
  "ds_read_b64_tr_b16 v[20:21], v130 offset:8448\n" \
  "buffer_load_dwordx4 a[168:171], v133, s[40:43], s68 offen offset:0\n" \
  "v_mfma_f32_16x16x32_bf16 a[56:59], v[120:123], a[176:179], a[56:59]\n" \
  "ds_read_b64_tr_b16 v[22:23], v130 offset:9472\n" \
  "ds_read_b64_tr_b16 v[28:29], v131 offset:8448\n" \
  "v_mfma_f32_16x16x32_bf16 a[60:63], v[120:123], a[184:187], a[60:63]\n" \
  "ds_read_b64_tr_b16 v[30:31], v131 offset:9472\n" \
  "buffer_load_dwordx4 a[164:167], v132, s[40:43], s68 offen offset:1024\n" \
  "v_mfma_f32_16x16x32_bf16 a[0:3], v[68:71], a[180:183], a[0:3]\n" \
  "ds_read_b64_tr_b16 v[36:37], v130 offset:8704\n" \
  "ds_read_b64_tr_b16 v[38:39], v130 offset:9728\n" \
  "v_mfma_f32_16x16x32_bf16 a[4:7], v[68:71], a[188:191], a[4:7]\n" \
  "ds_read_b64_tr_b16 v[44:45], v131 offset:8704\n" \
  "buffer_load_dwordx4 a[172:175], v133, s[40:43], s68 offen offset:1024\n" \
  "s_add_u32 s68, s68, 2048\n" \
  "v_mfma_f32_16x16x32_bf16 a[8:11], v[76:79], a[180:183], a[8:11]\n" \
  "ds_read_b64_tr_b16 v[46:47], v131 offset:9728\n" \
  "ds_read_b64_tr_b16 v[52:53], v130 offset:8960\n" \
  "v_mfma_f32_16x16x32_bf16 a[12:15], v[76:79], a[188:191], a[12:15]\n" \
  "ds_read_b64_tr_b16 v[54:55], v130 offset:9984\n" \
  "ds_read_b64_tr_b16 v[60:61], v131 offset:8960\n" \
  "v_mfma_f32_16x16x32_bf16 a[16:19], v[84:87], a[180:183], a[16:19]\n" \
  "ds_read_b64_tr_b16 v[62:63], v131 offset:9984\n" \
  "v_mfma_f32_16x16x32_bf16 a[20:23], v[84:87], a[188:191], a[20:23]\n" \
  "v_mfma_f32_16x16x32_bf16 a[24:27], v[92:95], a[180:183], a[24:27]\n" \
  "v_mfma_f32_16x16x32_bf16 a[28:31], v[92:95], a[188:191], a[28:31]\n" \
  "v_mfma_f32_16x16x32_bf16 a[32:35], v[100:103], a[180:183], a[32:35]\n" \
  "v_mfma_f32_16x16x32_bf16 a[36:39], v[100:103], a[188:191], a[36:39]\n" \
  "v_mfma_f32_16x16x32_bf16 a[40:43], v[108:111], a[180:183], a[40:43]\n" \
  "v_mfma_f32_16x16x32_bf16 a[44:47], v[108:111], a[188:191], a[44:47]\n" \
  "v_mfma_f32_16x16x32_bf16 a[48:51], v[116:119], a[180:183], a[48:51]\n" \
  "v_mfma_f32_16x16x32_bf16 a[52:55], v[116:119], a[188:191], a[52:55]\n" \
  "v_mfma_f32_16x16x32_bf16 a[56:59], v[124:127], a[180:183], a[56:59]\n" \
  "v_mfma_f32_16x16x32_bf16 a[60:63], v[124:127], a[188:191], a[60:63]\n" \
  "s_waitcnt vmcnt(44) lgkmcnt(0)\n" \
  "s_barrier\n" \
  "v_mfma_f32_16x16x32_bf16 a[0:3], v[0:3], a[192:195], a[0:3]\n" \
  "ds_read_b64_tr_b16 v[64:65], v130 offset:16384\n" \
  "ds_read_b64_tr_b16 v[66:67], v130 offset:17408\n" \
  "v_mfma_f32_16x16x32_bf16 a[4:7], v[0:3], a[200:203], a[4:7]\n" \
  "ds_read_b64_tr_b16 v[72:73], v131 offset:16384\n" \
  "s_add_u32 m0, s72, 49152\n" \
  "s_nop 0\n" \
  "buffer_load_dwordx4 v136, s[44:47], s69 offen lds\n" \
  "v_mfma_f32_16x16x32_bf16 a[8:11], v[8:11], a[192:195], a[8:11]\n" \
  "ds_read_b64_tr_b16 v[74:75], v131 offset:17408\n" \
  "ds_read_b64_tr_b16 v[80:81], v130 offset:16640\n" \
  "v_mfma_f32_16x16x32_bf16 a[12:15], v[8:11], a[200:203], a[12:15]\n" \
  "ds_read_b64_tr_b16 v[82:83], v130 offset:17664\n" \
  "buffer_load_dwordx4 v137, s[44:47], s69 offen offset:1024 lds\n" \
  "v_mfma_f32_16x16x32_bf16 a[16:19], v[16:19], a[192:195], a[16:19]\n" \
  "ds_read_b64_tr_b16 v[88:89], v131 offset:16640\n" \
  "ds_read_b64_tr_b16 v[90:91], v131 offset:17664\n" \
  "v_mfma_f32_16x16x32_bf16 a[20:23], v[16:19], a[200:203], a[20:23]\n" \
  "ds_read_b64_tr_b16 v[96:97], v130 offset:16896\n" \
  "buffer_load_dwordx4 v138, s[44:47], s69 offen offset:2048 lds\n" \
  "v_mfma_f32_16x16x32_bf16 a[24:27], v[24:27], a[192:195], a[24:27]\n" \
  "ds_read_b64_tr_b16 v[98:99], v130 offset:17920\n" \
  "ds_read_b64_tr_b16 v[104:105], v131 offset:16896\n" \
  "v_mfma_f32_16x16x32_bf16 a[28:31], v[24:27], a[200:203], a[28:31]\n" \
  "ds_read_b64_tr_b16 v[106:107], v131 offset:17920\n" \
  "buffer_load_dwordx4 v139, s[44:47], s69 offen offset:3072 lds\n" \
  "s_add_u32 s69, s69, 0x10000\n" \
  "v_mfma_f32_16x16x32_bf16 a[32:35], v[32:35], a[192:195], a[32:35]\n" \
  "ds_read_b64_tr_b16 v[112:113], v130 offset:17152\n" \
  "ds_read_b64_tr_b16 v[114:115], v130 offset:18176\n" \
  "v_mfma_f32_16x16x32_bf16 a[36:39], v[32:35], a[200:203], a[36:39]\n" \
  "ds_read_b64_tr_b16 v[120:121], v131 offset:17152\n" \
  "buffer_load_dwordx4 a[176:179], v132, s[40:43], s68 offen offset:0\n" \
  "v_mfma_f32_16x16x32_bf16 a[40:43], v[40:43], a[192:195], a[40:43]\n" \
  "ds_read_b64_tr_b16 v[122:123], v131 offset:18176\n" \
  "ds_read_b64_tr_b16 v[68:69], v130 offset:24576\n" \
  "v_mfma_f32_16x16x32_bf16 a[44:47], v[40:43], a[200:203], a[44:47]\n" \
  "ds_read_b64_tr_b16 v[70:71], v130 offset:25600\n" \
  "buffer_load_dwordx4 a[184:187], v133, s[40:43], s68 offen offset:0\n" \
  "v_mfma_f32_16x16x32_bf16 a[48:51], v[48:51], a[192:195], a[48:51]\n" \
  "ds_read_b64_tr_b16 v[76:77], v131 offset:24576\n" \
  "ds_read_b64_tr_b16 v[78:79], v131 offset:25600\n" \
  "v_mfma_f32_16x16x32_bf16 a[52:55], v[48:51], a[200:203], a[52:55]\n" \
  "ds_read_b64_tr_b16 v[84:85], v130 offset:24832\n" \
  "buffer_load_dwordx4 a[180:183], v132, s[40:43], s68 offen offset:1024\n" \
  "v_mfma_f32_16x16x32_bf16 a[56:59], v[56:59], a[192:195], a[56:59]\n" \
  "ds_read_b64_tr_b16 v[86:87], v130 offset:25856\n" \
  "ds_read_b64_tr_b16 v[92:93], v131 offset:24832\n" \
  "v_mfma_f32_16x16x32_bf16 a[60:63], v[56:59], a[200:203], a[60:63]\n" \
  "ds_read_b64_tr_b16 v[94:95], v131 offset:25856\n" \
  "buffer_load_dwordx4 a[188:191], v133, s[40:43], s68 offen offset:1024\n" \
  "s_add_u32 s68, s68, 2048\n" \
  "v_mfma_f32_16x16x32_bf16 a[0:3], v[4:7], a[196:199], a[0:3]\n" \
  "ds_read_b64_tr_b16 v[100:101], v130 offset:25088\n" \
  "ds_read_b64_tr_b16 v[102:103], v130 offset:26112\n" \
  "v_mfma_f32_16x16x32_bf16 a[4:7], v[4:7], a[204:207], a[4:7]\n" \
  "ds_read_b64_tr_b16 v[108:109], v131 offset:25088\n" \
  "ds_read_b64_tr_b16 v[110:111], v131 offset:26112\n" \
  "v_mfma_f32_16x16x32_bf16 a[8:11], v[12:15], a[196:199], a[8:11]\n" \
  "ds_read_b64_tr_b16 v[116:117], v130 offset:25344\n" \
  "ds_read_b64_tr_b16 v[118:119], v130 offset:26368\n" \
  "v_mfma_f32_16x16x32_bf16 a[12:15], v[12:15], a[204:207], a[12:15]\n" \
  "ds_read_b64_tr_b16 v[124:125], v131 offset:25344\n" \
  "ds_read_b64_tr_b16 v[126:127], v131 offset:26368\n" \
  "v_mfma_f32_16x16x32_bf16 a[16:19], v[20:23], a[196:199], a[16:19]\n" \
  "v_mfma_f32_16x16x32_bf16 a[20:23], v[20:23], a[204:207], a[20:23]\n" \
  "v_mfma_f32_16x16x32_bf16 a[24:27], v[28:31], a[196:199], a[24:27]\n" \
  "v_mfma_f32_16x16x32_bf16 a[28:31], v[28:31], a[204:207], a[28:31]\n" \
  "v_mfma_f32_16x16x32_bf16 a[32:35], v[36:39], a[196:199], a[32:35]\n" \
  "v_mfma_f32_16x16x32_bf16 a[36:39], v[36:39], a[204:207], a[36:39]\n" \
  "v_mfma_f32_16x16x32_bf16 a[40:43], v[44:47], a[196:199], a[40:43]\n" \
  "v_mfma_f32_16x16x32_bf16 a[44:47], v[44:47], a[204:207], a[44:47]\n" \
  "v_mfma_f32_16x16x32_bf16 a[48:51], v[52:55], a[196:199], a[48:51]\n" \
  "v_mfma_f32_16x16x32_bf16 a[52:55], v[52:55], a[204:207], a[52:55]\n" \
  "v_mfma_f32_16x16x32_bf16 a[56:59], v[60:63], a[196:199], a[56:59]\n" \
  "v_mfma_f32_16x16x32_bf16 a[60:63], v[60:63], a[204:207], a[60:63]\n" \
  "s_waitcnt vmcnt(44) lgkmcnt(0)\n" \
  "s_barrier\n" \
  "v_mfma_f32_16x16x32_bf16 a[0:3], v[64:67], a[208:211], a[0:3]\n" \
  "ds_read_b64_tr_b16 v[0:1], v130 offset:32768\n" \
  "ds_read_b64_tr_b16 v[2:3], v130 offset:33792\n" \
  "v_mfma_f32_16x16x32_bf16 a[4:7], v[64:67], a[216:219], a[4:7]\n" \
  "ds_read_b64_tr_b16 v[8:9], v131 offset:32768\n" \
  "s_add_u32 m0, s72, 65536\n" \
  "s_nop 0\n" \
  "buffer_load_dwordx4 v136, s[44:47], s69 offen lds\n" \
  "v_mfma_f32_16x16x32_bf16 a[8:11], v[72:75], a[208:211], a[8:11]\n" \
  "ds_read_b64_tr_b16 v[10:11], v131 offset:33792\n" \
  "ds_read_b64_tr_b16 v[16:17], v130 offset:33024\n" \
  "v_mfma_f32_16x16x32_bf16 a[12:15], v[72:75], a[216:219], a[12:15]\n" \
  "ds_read_b64_tr_b16 v[18:19], v130 offset:34048\n" \
  "buffer_load_dwordx4 v137, s[44:47], s69 offen offset:1024 lds\n" \
  "v_mfma_f32_16x16x32_bf16 a[16:19], v[80:83], a[208:211], a[16:19]\n" \
  "ds_read_b64_tr_b16 v[24:25], v131 offset:33024\n" \
  "ds_read_b64_tr_b16 v[26:27], v131 offset:34048\n" \
  "v_mfma_f32_16x16x32_bf16 a[20:23], v[80:83], a[216:219], a[20:23]\n" \
  "ds_read_b64_tr_b16 v[32:33], v130 offset:33280\n" \
  "buffer_load_dwordx4 v138, s[44:47], s69 offen offset:2048 lds\n" \
  "v_mfma_f32_16x16x32_bf16 a[24:27], v[88:91], a[208:211], a[24:27]\n" \
  "ds_read_b64_tr_b16 v[34:35], v130 offset:34304\n" \
  "ds_read_b64_tr_b16 v[40:41], v131 offset:33280\n" \
  "v_mfma_f32_16x16x32_bf16 a[28:31], v[88:91], a[216:219], a[28:31]\n" \
  "ds_read_b64_tr_b16 v[42:43], v131 offset:34304\n" \
  "buffer_load_dwordx4 v139, s[44:47], s69 offen offset:3072 lds\n" \
  "s_add_u32 s69, s69, 0x10000\n" \
  "v_mfma_f32_16x16x32_bf16 a[32:35], v[96:99], a[208:211], a[32:35]\n" \
  "ds_read_b64_tr_b16 v[48:49], v130 offset:33536\n" \
  "ds_read_b64_tr_b16 v[50:51], v130 offset:34560\n" \
  "v_mfma_f32_16x16x32_bf16 a[36:39], v[96:99], a[216:219], a[36:39]\n" \
  "ds_read_b64_tr_b16 v[56:57], v131 offset:33536\n" \
  "buffer_load_dwordx4 a[192:195], v132, s[40:43], s68 offen offset:0\n" \
  "v_mfma_f32_16x16x32_bf16 a[40:43], v[104:107], a[208:211], a[40:43]\n" \
  "ds_read_b64_tr_b16 v[58:59], v131 offset:34560\n" \
  "ds_read_b64_tr_b16 v[4:5], v130 offset:40960\n" \
  "v_mfma_f32_16x16x32_bf16 a[44:47], v[104:107], a[216:219], a[44:47]\n" \
  "ds_read_b64_tr_b16 v[6:7], v130 offset:41984\n" \
  "buffer_load_dwordx4 a[200:203], v133, s[40:43], s68 offen offset:0\n" \
  "v_mfma_f32_16x16x32_bf16 a[48:51], v[112:115], a[208:211], a[48:51]\n" \
  "ds_read_b64_tr_b16 v[12:13], v131 offset:40960\n" \
  "ds_read_b64_tr_b16 v[14:15], v131 offset:41984\n" \
  "v_mfma_f32_16x16x32_bf16 a[52:55], v[112:115], a[216:219], a[52:55]\n" \
  "ds_read_b64_tr_b16 v[20:21], v130 offset:41216\n" \
  "buffer_load_dwordx4 a[196:199], v132, s[40:43], s68 offen offset:1024\n" \
  "v_mfma_f32_16x16x32_bf16 a[56:59], v[120:123], a[208:211], a[56:59]\n" \
  "ds_read_b64_tr_b16 v[22:23], v130 offset:42240\n" \
  "ds_read_b64_tr_b16 v[28:29], v131 offset:41216\n" \
  "v_mfma_f32_16x16x32_bf16 a[60:63], v[120:123], a[216:219], a[60:63]\n" \
  "ds_read_b64_tr_b16 v[30:31], v131 offset:42240\n" \
  "buffer_load_dwordx4 a[204:207], v133, s[40:43], s68 offen offset:1024\n" \
  "s_add_u32 s68, s68, 2048\n" \
  "v_mfma_f32_16x16x32_bf16 a[0:3], v[68:71], a[212:215], a[0:3]\n" \
  "ds_read_b64_tr_b16 v[36:37], v130 offset:41472\n" \
  "ds_read_b64_tr_b16 v[38:39], v130 offset:42496\n" \
  "v_mfma_f32_16x16x32_bf16 a[4:7], v[68:71], a[220:223], a[4:7]\n" \
  "ds_read_b64_tr_b16 v[44:45], v131 offset:41472\n" \
  "ds_read_b64_tr_b16 v[46:47], v131 offset:42496\n" \
  "v_mfma_f32_16x16x32_bf16 a[8:11], v[76:79], a[212:215], a[8:11]\n" \
  "ds_read_b64_tr_b16 v[52:53], v130 offset:41728\n" \
  "ds_read_b64_tr_b16 v[54:55], v130 offset:42752\n" \
  "v_mfma_f32_16x16x32_bf16 a[12:15], v[76:79], a[220:223], a[12:15]\n" \
  "ds_read_b64_tr_b16 v[60:61], v131 offset:41728\n" \
  "ds_read_b64_tr_b16 v[62:63], v131 offset:42752\n" \
  "v_mfma_f32_16x16x32_bf16 a[16:19], v[84:87], a[212:215], a[16:19]\n" \
  "v_mfma_f32_16x16x32_bf16 a[20:23], v[84:87], a[220:223], a[20:23]\n" \
  "v_mfma_f32_16x16x32_bf16 a[24:27], v[92:95], a[212:215], a[24:27]\n" \
  "v_mfma_f32_16x16x32_bf16 a[28:31], v[92:95], a[220:223], a[28:31]\n" \
  "v_mfma_f32_16x16x32_bf16 a[32:35], v[100:103], a[212:215], a[32:35]\n" \
  "v_mfma_f32_16x16x32_bf16 a[36:39], v[100:103], a[220:223], a[36:39]\n" \
  "v_mfma_f32_16x16x32_bf16 a[40:43], v[108:111], a[212:215], a[40:43]\n" \
  "v_mfma_f32_16x16x32_bf16 a[44:47], v[108:111], a[220:223], a[44:47]\n" \
  "v_mfma_f32_16x16x32_bf16 a[48:51], v[116:119], a[212:215], a[48:51]\n" \
  "v_mfma_f32_16x16x32_bf16 a[52:55], v[116:119], a[220:223], a[52:55]\n" \
  "v_mfma_f32_16x16x32_bf16 a[56:59], v[124:127], a[212:215], a[56:59]\n" \
  "v_mfma_f32_16x16x32_bf16 a[60:63], v[124:127], a[220:223], a[60:63]\n" \
  "s_waitcnt vmcnt(44) lgkmcnt(0)\n" \
  "s_barrier\n" \
  "v_mfma_f32_16x16x32_bf16 a[0:3], v[0:3], a[224:227], a[0:3]\n" \
  "ds_read_b64_tr_b16 v[64:65], v130 offset:49152\n" \
  "ds_read_b64_tr_b16 v[66:67], v130 offset:50176\n" \
  "v_mfma_f32_16x16x32_bf16 a[4:7], v[0:3], a[232:235], a[4:7]\n" \
  "ds_read_b64_tr_b16 v[72:73], v131 offset:49152\n" \
  "s_add_u32 m0, s72, 81920\n" \
  "s_nop 0\n" \
  "buffer_load_dwordx4 v136, s[44:47], s69 offen lds\n" \
  "v_mfma_f32_16x16x32_bf16 a[8:11], v[8:11], a[224:227], a[8:11]\n" \
  "ds_read_b64_tr_b16 v[74:75], v131 offset:50176\n" \
  "ds_read_b64_tr_b16 v[80:81], v130 offset:49408\n" \
  "v_mfma_f32_16x16x32_bf16 a[12:15], v[8:11], a[232:235], a[12:15]\n" \
  "ds_read_b64_tr_b16 v[82:83], v130 offset:50432\n" \
  "buffer_load_dwordx4 v137, s[44:47], s69 offen offset:1024 lds\n" \
  "v_mfma_f32_16x16x32_bf16 a[16:19], v[16:19], a[224:227], a[16:19]\n" \
  "ds_read_b64_tr_b16 v[88:89], v131 offset:49408\n" \
  "ds_read_b64_tr_b16 v[90:91], v131 offset:50432\n" \
  "v_mfma_f32_16x16x32_bf16 a[20:23], v[16:19], a[232:235], a[20:23]\n" \
  "ds_read_b64_tr_b16 v[96:97], v130 offset:49664\n" \
  "buffer_load_dwordx4 v138, s[44:47], s69 offen offset:2048 lds\n" \
  "v_mfma_f32_16x16x32_bf16 a[24:27], v[24:27], a[224:227], a[24:27]\n" \
  "ds_read_b64_tr_b16 v[98:99], v130 offset:50688\n" \
  "ds_read_b64_tr_b16 v[104:105], v131 offset:49664\n" \
  "v_mfma_f32_16x16x32_bf16 a[28:31], v[24:27], a[232:235], a[28:31]\n" \
  "ds_read_b64_tr_b16 v[106:107], v131 offset:50688\n" \
  "buffer_load_dwordx4 v139, s[44:47], s69 offen offset:3072 lds\n" \
  "s_add_u32 s69, s69, 0x10000\n" \
  "v_mfma_f32_16x16x32_bf16 a[32:35], v[32:35], a[224:227], a[32:35]\n" \
  "ds_read_b64_tr_b16 v[112:113], v130 offset:49920\n" \
  "ds_read_b64_tr_b16 v[114:115], v130 offset:50944\n" \
  "v_mfma_f32_16x16x32_bf16 a[36:39], v[32:35], a[232:235], a[36:39]\n" \
  "ds_read_b64_tr_b16 v[120:121], v131 offset:49920\n" \
  "buffer_load_dwordx4 a[208:211], v132, s[40:43], s68 offen offset:0\n" \
  "v_mfma_f32_16x16x32_bf16 a[40:43], v[40:43], a[224:227], a[40:43]\n" \
  "ds_read_b64_tr_b16 v[122:123], v131 offset:50944\n" \
  "ds_read_b64_tr_b16 v[68:69], v130 offset:57344\n" \
  "v_mfma_f32_16x16x32_bf16 a[44:47], v[40:43], a[232:235], a[44:47]\n" \
  "ds_read_b64_tr_b16 v[70:71], v130 offset:58368\n" \
  "buffer_load_dwordx4 a[216:219], v133, s[40:43], s68 offen offset:0\n" \
  "v_mfma_f32_16x16x32_bf16 a[48:51], v[48:51], a[224:227], a[48:51]\n" \
  "ds_read_b64_tr_b16 v[76:77], v131 offset:57344\n" \
  "ds_read_b64_tr_b16 v[78:79], v131 offset:58368\n" \
  "v_mfma_f32_16x16x32_bf16 a[52:55], v[48:51], a[232:235], a[52:55]\n" \
  "ds_read_b64_tr_b16 v[84:85], v130 offset:57600\n" \
  "buffer_load_dwordx4 a[212:215], v132, s[40:43], s68 offen offset:1024\n" \
  "v_mfma_f32_16x16x32_bf16 a[56:59], v[56:59], a[224:227], a[56:59]\n" \
  "ds_read_b64_tr_b16 v[86:87], v130 offset:58624\n" \
  "ds_read_b64_tr_b16 v[92:93], v131 offset:57600\n" \
  "v_mfma_f32_16x16x32_bf16 a[60:63], v[56:59], a[232:235], a[60:63]\n" \
  "ds_read_b64_tr_b16 v[94:95], v131 offset:58624\n" \
  "buffer_load_dwordx4 a[220:223], v133, s[40:43], s68 offen offset:1024\n" \
  "s_add_u32 s68, s68, 2048\n" \
  "v_mfma_f32_16x16x32_bf16 a[0:3], v[4:7], a[228:231], a[0:3]\n" \
  "ds_read_b64_tr_b16 v[100:101], v130 offset:57856\n" \
  "ds_read_b64_tr_b16 v[102:103], v130 offset:58880\n" \
  "v_mfma_f32_16x16x32_bf16 a[4:7], v[4:7], a[236:239], a[4:7]\n" \
  "ds_read_b64_tr_b16 v[108:109], v131 offset:57856\n" \
  "ds_read_b64_tr_b16 v[110:111], v131 offset:58880\n" \
  "v_mfma_f32_16x16x32_bf16 a[8:11], v[12:15], a[228:231], a[8:11]\n" \
  "ds_read_b64_tr_b16 v[116:117], v130 offset:58112\n" \
  "ds_read_b64_tr_b16 v[118:119], v130 offset:59136\n" \
  "v_mfma_f32_16x16x32_bf16 a[12:15], v[12:15], a[236:239], a[12:15]\n" \
  "ds_read_b64_tr_b16 v[124:125], v131 offset:58112\n" \
  "ds_read_b64_tr_b16 v[126:127], v131 offset:59136\n" \
  "v_mfma_f32_16x16x32_bf16 a[16:19], v[20:23], a[228:231], a[16:19]\n" \
  "v_mfma_f32_16x16x32_bf16 a[20:23], v[20:23], a[236:239], a[20:23]\n" \
  "v_mfma_f32_16x16x32_bf16 a[24:27], v[28:31], a[228:231], a[24:27]\n" \
  "v_mfma_f32_16x16x32_bf16 a[28:31], v[28:31], a[236:239], a[28:31]\n" \
  "v_mfma_f32_16x16x32_bf16 a[32:35], v[36:39], a[228:231], a[32:35]\n" \
  "v_mfma_f32_16x16x32_bf16 a[36:39], v[36:39], a[236:239], a[36:39]\n" \
  "v_mfma_f32_16x16x32_bf16 a[40:43], v[44:47], a[228:231], a[40:43]\n" \
  "v_mfma_f32_16x16x32_bf16 a[44:47], v[44:47], a[236:239], a[44:47]\n" \
  "v_mfma_f32_16x16x32_bf16 a[48:51], v[52:55], a[228:231], a[48:51]\n" \
  "v_mfma_f32_16x16x32_bf16 a[52:55], v[52:55], a[236:239], a[52:55]\n" \
  "v_mfma_f32_16x16x32_bf16 a[56:59], v[60:63], a[228:231], a[56:59]\n" \
  "v_mfma_f32_16x16x32_bf16 a[60:63], v[60:63], a[236:239], a[60:63]\n" \
  "s_waitcnt vmcnt(44) lgkmcnt(0)\n" \
  "s_barrier\n" \
  "v_mfma_f32_16x16x32_bf16 a[0:3], v[64:67], a[240:243], a[0:3]\n" \
  "ds_read_b64_tr_b16 v[0:1], v128 offset:0\n" \
  "ds_read_b64_tr_b16 v[2:3], v128 offset:1024\n" \
  "v_mfma_f32_16x16x32_bf16 a[4:7], v[64:67], a[248:251], a[4:7]\n" \
  "ds_read_b64_tr_b16 v[8:9], v129 offset:0\n" \
  "s_add_u32 m0, s72, 98304\n" \
  "s_nop 0\n" \
  "buffer_load_dwordx4 v136, s[44:47], s69 offen lds\n" \
  "v_mfma_f32_16x16x32_bf16 a[8:11], v[72:75], a[240:243], a[8:11]\n" \
  "ds_read_b64_tr_b16 v[10:11], v129 offset:1024\n" \
  "ds_read_b64_tr_b16 v[16:17], v128 offset:256\n" \
  "v_mfma_f32_16x16x32_bf16 a[12:15], v[72:75], a[248:251], a[12:15]\n" \
  "ds_read_b64_tr_b16 v[18:19], v128 offset:1280\n" \
  "buffer_load_dwordx4 v137, s[44:47], s69 offen offset:1024 lds\n" \
  "v_mfma_f32_16x16x32_bf16 a[16:19], v[80:83], a[240:243], a[16:19]\n" \
  "ds_read_b64_tr_b16 v[24:25], v129 offset:256\n" \
  "ds_read_b64_tr_b16 v[26:27], v129 offset:1280\n" \
  "v_mfma_f32_16x16x32_bf16 a[20:23], v[80:83], a[248:251], a[20:23]\n" \
  "ds_read_b64_tr_b16 v[32:33], v128 offset:512\n" \
  "buffer_load_dwordx4 v138, s[44:47], s69 offen offset:2048 lds\n" \
  "v_mfma_f32_16x16x32_bf16 a[24:27], v[88:91], a[240:243], a[24:27]\n" \
  "ds_read_b64_tr_b16 v[34:35], v128 offset:1536\n" \
  "ds_read_b64_tr_b16 v[40:41], v129 offset:512\n" \
  "v_mfma_f32_16x16x32_bf16 a[28:31], v[88:91], a[248:251], a[28:31]\n" \
  "ds_read_b64_tr_b16 v[42:43], v129 offset:1536\n" \
  "buffer_load_dwordx4 v139, s[44:47], s69 offen offset:3072 lds\n" \
  "s_add_u32 s69, s69, 0x10000\n" \
  "v_mfma_f32_16x16x32_bf16 a[32:35], v[96:99], a[240:243], a[32:35]\n" \
  "ds_read_b64_tr_b16 v[48:49], v128 offset:768\n" \
  "ds_read_b64_tr_b16 v[50:51], v128 offset:1792\n" \
  "v_mfma_f32_16x16x32_bf16 a[36:39], v[96:99], a[248:251], a[36:39]\n" \
  "ds_read_b64_tr_b16 v[56:57], v129 offset:768\n" \
  "buffer_load_dwordx4 a[224:227], v132, s[40:43], s68 offen offset:0\n" \
  "v_mfma_f32_16x16x32_bf16 a[40:43], v[104:107], a[240:243], a[40:43]\n" \
  "ds_read_b64_tr_b16 v[58:59], v129 offset:1792\n" \
  "ds_read_b64_tr_b16 v[4:5], v128 offset:8192\n" \
  "v_mfma_f32_16x16x32_bf16 a[44:47], v[104:107], a[248:251], a[44:47]\n" \
  "ds_read_b64_tr_b16 v[6:7], v128 offset:9216\n" \
  "buffer_load_dwordx4 a[232:235], v133, s[40:43], s68 offen offset:0\n" \
  "v_mfma_f32_16x16x32_bf16 a[48:51], v[112:115], a[240:243], a[48:51]\n" \
  "ds_read_b64_tr_b16 v[12:13], v129 offset:8192\n" \
  "ds_read_b64_tr_b16 v[14:15], v129 offset:9216\n" \
  "v_mfma_f32_16x16x32_bf16 a[52:55], v[112:115], a[248:251], a[52:55]\n" \
  "ds_read_b64_tr_b16 v[20:21], v128 offset:8448\n" \
  "buffer_load_dwordx4 a[228:231], v132, s[40:43], s68 offen offset:1024\n" \
  "v_mfma_f32_16x16x32_bf16 a[56:59], v[120:123], a[240:243], a[56:59]\n" \
  "ds_read_b64_tr_b16 v[22:23], v128 offset:9472\n" \
  "ds_read_b64_tr_b16 v[28:29], v129 offset:8448\n" \
  "v_mfma_f32_16x16x32_bf16 a[60:63], v[120:123], a[248:251], a[60:63]\n" \
  "ds_read_b64_tr_b16 v[30:31], v129 offset:9472\n" \
  "buffer_load_dwordx4 a[236:239], v133, s[40:43], s68 offen offset:1024\n" \
  "s_add_u32 s68, s68, 2048\n" \
  "v_mfma_f32_16x16x32_bf16 a[0:3], v[68:71], a[244:247], a[0:3]\n" \
  "ds_read_b64_tr_b16 v[36:37], v128 offset:8704\n" \
  "ds_read_b64_tr_b16 v[38:39], v128 offset:9728\n" \
  "v_mfma_f32_16x16x32_bf16 a[4:7], v[68:71], a[252:255], a[4:7]\n" \
  "ds_read_b64_tr_b16 v[44:45], v129 offset:8704\n" \
  "ds_read_b64_tr_b16 v[46:47], v129 offset:9728\n" \
  "v_mfma_f32_16x16x32_bf16 a[8:11], v[76:79], a[244:247], a[8:11]\n" \
  "ds_read_b64_tr_b16 v[52:53], v128 offset:8960\n" \
  "ds_read_b64_tr_b16 v[54:55], v128 offset:9984\n" \
  "v_mfma_f32_16x16x32_bf16 a[12:15], v[76:79], a[252:255], a[12:15]\n" \
  "ds_read_b64_tr_b16 v[60:61], v129 offset:8960\n" \
  "ds_read_b64_tr_b16 v[62:63], v129 offset:9984\n" \
  "v_mfma_f32_16x16x32_bf16 a[16:19], v[84:87], a[244:247], a[16:19]\n" \
  "v_mfma_f32_16x16x32_bf16 a[20:23], v[84:87], a[252:255], a[20:23]\n" \
  "v_mfma_f32_16x16x32_bf16 a[24:27], v[92:95], a[244:247], a[24:27]\n" \
  "v_mfma_f32_16x16x32_bf16 a[28:31], v[92:95], a[252:255], a[28:31]\n" \
  "v_mfma_f32_16x16x32_bf16 a[32:35], v[100:103], a[244:247], a[32:35]\n" \
  "v_mfma_f32_16x16x32_bf16 a[36:39], v[100:103], a[252:255], a[36:39]\n" \
  "v_mfma_f32_16x16x32_bf16 a[40:43], v[108:111], a[244:247], a[40:43]\n" \
  "v_mfma_f32_16x16x32_bf16 a[44:47], v[108:111], a[252:255], a[44:47]\n" \
  "v_mfma_f32_16x16x32_bf16 a[48:51], v[116:119], a[244:247], a[48:51]\n" \
  "v_mfma_f32_16x16x32_bf16 a[52:55], v[116:119], a[252:255], a[52:55]\n" \
  "v_mfma_f32_16x16x32_bf16 a[56:59], v[124:127], a[244:247], a[56:59]\n" \
  "v_mfma_f32_16x16x32_bf16 a[60:63], v[124:127], a[252:255], a[60:63]\n" \
  "s_waitcnt vmcnt(44) lgkmcnt(0)\n" \
  "s_barrier\n" \
  "v_mfma_f32_16x16x32_bf16 a[0:3], v[0:3], a[128:131], a[0:3]\n" \
  "ds_read_b64_tr_b16 v[64:65], v128 offset:16384\n" \
  "v_mfma_f32_16x16x32_bf16 a[4:7], v[0:3], a[136:139], a[4:7]\n" \
  "ds_read_b64_tr_b16 v[66:67], v128 offset:17408\n" \
  "v_mfma_f32_16x16x32_bf16 a[8:11], v[8:11], a[128:131], a[8:11]\n" \
  "ds_read_b64_tr_b16 v[72:73], v129 offset:16384\n" \
  "v_mfma_f32_16x16x32_bf16 a[12:15], v[8:11], a[136:139], a[12:15]\n" \
  "ds_read_b64_tr_b16 v[74:75], v129 offset:17408\n" \
  "v_mfma_f32_16x16x32_bf16 a[16:19], v[16:19], a[128:131], a[16:19]\n" \
  "ds_read_b64_tr_b16 v[80:81], v128 offset:16640\n" \
  "v_mfma_f32_16x16x32_bf16 a[20:23], v[16:19], a[136:139], a[20:23]\n" \
  "ds_read_b64_tr_b16 v[82:83], v128 offset:17664\n" \
  "v_mfma_f32_16x16x32_bf16 a[24:27], v[24:27], a[128:131], a[24:27]\n" \
  "ds_read_b64_tr_b16 v[88:89], v129 offset:16640\n" \
  "v_mfma_f32_16x16x32_bf16 a[28:31], v[24:27], a[136:139], a[28:31]\n" \
  "ds_read_b64_tr_b16 v[90:91], v129 offset:17664\n" \
  "v_mfma_f32_16x16x32_bf16 a[32:35], v[32:35], a[128:131], a[32:35]\n" \
  "ds_read_b64_tr_b16 v[96:97], v128 offset:16896\n" \
  "v_mfma_f32_16x16x32_bf16 a[36:39], v[32:35], a[136:139], a[36:39]\n" \
  "ds_read_b64_tr_b16 v[98:99], v128 offset:17920\n" \
  "v_mfma_f32_16x16x32_bf16 a[40:43], v[40:43], a[128:131], a[40:43]\n" \
  "ds_read_b64_tr_b16 v[104:105], v129 offset:16896\n" \
  "v_mfma_f32_16x16x32_bf16 a[44:47], v[40:43], a[136:139], a[44:47]\n" \
  "ds_read_b64_tr_b16 v[106:107], v129 offset:17920\n" \
  "v_mfma_f32_16x16x32_bf16 a[48:51], v[48:51], a[128:131], a[48:51]\n" \
  "ds_read_b64_tr_b16 v[112:113], v128 offset:17152\n" \
  "v_mfma_f32_16x16x32_bf16 a[52:55], v[48:51], a[136:139], a[52:55]\n" \
  "ds_read_b64_tr_b16 v[114:115], v128 offset:18176\n" \
  "v_mfma_f32_16x16x32_bf16 a[56:59], v[56:59], a[128:131], a[56:59]\n" \
  "ds_read_b64_tr_b16 v[120:121], v129 offset:17152\n" \
  "v_mfma_f32_16x16x32_bf16 a[60:63], v[56:59], a[136:139], a[60:63]\n" \
  "ds_read_b64_tr_b16 v[122:123], v129 offset:18176\n" \
  "v_mfma_f32_16x16x32_bf16 a[0:3], v[4:7], a[132:135], a[0:3]\n" \
  "ds_read_b64_tr_b16 v[68:69], v128 offset:24576\n" \
  "v_mfma_f32_16x16x32_bf16 a[4:7], v[4:7], a[140:143], a[4:7]\n" \
  "ds_read_b64_tr_b16 v[70:71], v128 offset:25600\n" \
  "v_mfma_f32_16x16x32_bf16 a[8:11], v[12:15], a[132:135], a[8:11]\n" \
  "ds_read_b64_tr_b16 v[76:77], v129 offset:24576\n" \
  "v_mfma_f32_16x16x32_bf16 a[12:15], v[12:15], a[140:143], a[12:15]\n" \
  "ds_read_b64_tr_b16 v[78:79], v129 offset:25600\n" \
  "v_mfma_f32_16x16x32_bf16 a[16:19], v[20:23], a[132:135], a[16:19]\n" \
  "ds_read_b64_tr_b16 v[84:85], v128 offset:24832\n" \
  "v_mfma_f32_16x16x32_bf16 a[20:23], v[20:23], a[140:143], a[20:23]\n" \
  "ds_read_b64_tr_b16 v[86:87], v128 offset:25856\n" \
  "v_mfma_f32_16x16x32_bf16 a[24:27], v[28:31], a[132:135], a[24:27]\n" \
  "ds_read_b64_tr_b16 v[92:93], v129 offset:24832\n" \
  "v_mfma_f32_16x16x32_bf16 a[28:31], v[28:31], a[140:143], a[28:31]\n" \
  "ds_read_b64_tr_b16 v[94:95], v129 offset:25856\n" \
  "v_mfma_f32_16x16x32_bf16 a[32:35], v[36:39], a[132:135], a[32:35]\n" \
  "ds_read_b64_tr_b16 v[100:101], v128 offset:25088\n" \
  "v_mfma_f32_16x16x32_bf16 a[36:39], v[36:39], a[140:143], a[36:39]\n" \
  "ds_read_b64_tr_b16 v[102:103], v128 offset:26112\n" \
  "v_mfma_f32_16x16x32_bf16 a[40:43], v[44:47], a[132:135], a[40:43]\n" \
  "ds_read_b64_tr_b16 v[108:109], v129 offset:25088\n" \
  "v_mfma_f32_16x16x32_bf16 a[44:47], v[44:47], a[140:143], a[44:47]\n" \
  "ds_read_b64_tr_b16 v[110:111], v129 offset:26112\n" \
  "v_mfma_f32_16x16x32_bf16 a[48:51], v[52:55], a[132:135], a[48:51]\n" \
  "ds_read_b64_tr_b16 v[116:117], v128 offset:25344\n" \
  "v_mfma_f32_16x16x32_bf16 a[52:55], v[52:55], a[140:143], a[52:55]\n" \
  "ds_read_b64_tr_b16 v[118:119], v128 offset:26368\n" \
  "v_mfma_f32_16x16x32_bf16 a[56:59], v[60:63], a[132:135], a[56:59]\n" \
  "ds_read_b64_tr_b16 v[124:125], v129 offset:25344\n" \
  "v_mfma_f32_16x16x32_bf16 a[60:63], v[60:63], a[140:143], a[60:63]\n" \
  "ds_read_b64_tr_b16 v[126:127], v129 offset:26368\n" \
  "s_waitcnt vmcnt(36) lgkmcnt(0)\n" \
  "s_barrier\n" \
  "v_mfma_f32_16x16x32_bf16 a[0:3], v[64:67], a[144:147], a[0:3]\n" \
  "ds_read_b64_tr_b16 v[0:1], v128 offset:32768\n" \
  "v_mfma_f32_16x16x32_bf16 a[4:7], v[64:67], a[152:155], a[4:7]\n" \
  "ds_read_b64_tr_b16 v[2:3], v128 offset:33792\n" \
  "v_mfma_f32_16x16x32_bf16 a[8:11], v[72:75], a[144:147], a[8:11]\n" \
  "ds_read_b64_tr_b16 v[8:9], v129 offset:32768\n" \
  "v_mfma_f32_16x16x32_bf16 a[12:15], v[72:75], a[152:155], a[12:15]\n" \
  "ds_read_b64_tr_b16 v[10:11], v129 offset:33792\n" \
  "v_mfma_f32_16x16x32_bf16 a[16:19], v[80:83], a[144:147], a[16:19]\n" \
  "ds_read_b64_tr_b16 v[16:17], v128 offset:33024\n" \
  "v_mfma_f32_16x16x32_bf16 a[20:23], v[80:83], a[152:155], a[20:23]\n" \
  "ds_read_b64_tr_b16 v[18:19], v128 offset:34048\n" \
  "v_mfma_f32_16x16x32_bf16 a[24:27], v[88:91], a[144:147], a[24:27]\n" \
  "ds_read_b64_tr_b16 v[24:25], v129 offset:33024\n" \
  "v_mfma_f32_16x16x32_bf16 a[28:31], v[88:91], a[152:155], a[28:31]\n" \
  "ds_read_b64_tr_b16 v[26:27], v129 offset:34048\n" \
  "v_mfma_f32_16x16x32_bf16 a[32:35], v[96:99], a[144:147], a[32:35]\n" \
  "ds_read_b64_tr_b16 v[32:33], v128 offset:33280\n" \
  "v_mfma_f32_16x16x32_bf16 a[36:39], v[96:99], a[152:155], a[36:39]\n" \
  "ds_read_b64_tr_b16 v[34:35], v128 offset:34304\n" \
  "v_mfma_f32_16x16x32_bf16 a[40:43], v[104:107], a[144:147], a[40:43]\n" \
  "ds_read_b64_tr_b16 v[40:41], v129 offset:33280\n" \
  "v_mfma_f32_16x16x32_bf16 a[44:47], v[104:107], a[152:155], a[44:47]\n" \
  "ds_read_b64_tr_b16 v[42:43], v129 offset:34304\n" \
  "v_mfma_f32_16x16x32_bf16 a[48:51], v[112:115], a[144:147], a[48:51]\n" \
  "ds_read_b64_tr_b16 v[48:49], v128 offset:33536\n" \
  "v_mfma_f32_16x16x32_bf16 a[52:55], v[112:115], a[152:155], a[52:55]\n" \
  "ds_read_b64_tr_b16 v[50:51], v128 offset:34560\n" \
  "v_mfma_f32_16x16x32_bf16 a[56:59], v[120:123], a[144:147], a[56:59]\n" \
  "ds_read_b64_tr_b16 v[56:57], v129 offset:33536\n" \
  "v_mfma_f32_16x16x32_bf16 a[60:63], v[120:123], a[152:155], a[60:63]\n" \
  "ds_read_b64_tr_b16 v[58:59], v129 offset:34560\n" \
  "v_mfma_f32_16x16x32_bf16 a[0:3], v[68:71], a[148:151], a[0:3]\n" \
  "ds_read_b64_tr_b16 v[4:5], v128 offset:40960\n" \
  "v_mfma_f32_16x16x32_bf16 a[4:7], v[68:71], a[156:159], a[4:7]\n" \
  "ds_read_b64_tr_b16 v[6:7], v128 offset:41984\n" \
  "v_mfma_f32_16x16x32_bf16 a[8:11], v[76:79], a[148:151], a[8:11]\n" \
  "ds_read_b64_tr_b16 v[12:13], v129 offset:40960\n" \
  "v_mfma_f32_16x16x32_bf16 a[12:15], v[76:79], a[156:159], a[12:15]\n" \
  "ds_read_b64_tr_b16 v[14:15], v129 offset:41984\n" \
  "v_mfma_f32_16x16x32_bf16 a[16:19], v[84:87], a[148:151], a[16:19]\n" \
  "ds_read_b64_tr_b16 v[20:21], v128 offset:41216\n" \
  "v_mfma_f32_16x16x32_bf16 a[20:23], v[84:87], a[156:159], a[20:23]\n" \
  "ds_read_b64_tr_b16 v[22:23], v128 offset:42240\n" \
  "v_mfma_f32_16x16x32_bf16 a[24:27], v[92:95], a[148:151], a[24:27]\n" \
  "ds_read_b64_tr_b16 v[28:29], v129 offset:41216\n" \
  "v_mfma_f32_16x16x32_bf16 a[28:31], v[92:95], a[156:159], a[28:31]\n" \
  "ds_read_b64_tr_b16 v[30:31], v129 offset:42240\n" \
  "v_mfma_f32_16x16x32_bf16 a[32:35], v[100:103], a[148:151], a[32:35]\n" \
  "ds_read_b64_tr_b16 v[36:37], v128 offset:41472\n" \
  "v_mfma_f32_16x16x32_bf16 a[36:39], v[100:103], a[156:159], a[36:39]\n" \
  "ds_read_b64_tr_b16 v[38:39], v128 offset:42496\n" \
  "v_mfma_f32_16x16x32_bf16 a[40:43], v[108:111], a[148:151], a[40:43]\n" \
  "ds_read_b64_tr_b16 v[44:45], v129 offset:41472\n" \
  "v_mfma_f32_16x16x32_bf16 a[44:47], v[108:111], a[156:159], a[44:47]\n" \
  "ds_read_b64_tr_b16 v[46:47], v129 offset:42496\n" \
  "v_mfma_f32_16x16x32_bf16 a[48:51], v[116:119], a[148:151], a[48:51]\n" \
  "ds_read_b64_tr_b16 v[52:53], v128 offset:41728\n" \
  "v_mfma_f32_16x16x32_bf16 a[52:55], v[116:119], a[156:159], a[52:55]\n" \
  "ds_read_b64_tr_b16 v[54:55], v128 offset:42752\n" \
  "v_mfma_f32_16x16x32_bf16 a[56:59], v[124:127], a[148:151], a[56:59]\n" \
  "ds_read_b64_tr_b16 v[60:61], v129 offset:41728\n" \
  "v_mfma_f32_16x16x32_bf16 a[60:63], v[124:127], a[156:159], a[60:63]\n" \
  "ds_read_b64_tr_b16 v[62:63], v129 offset:42752\n" \
  "s_waitcnt vmcnt(28) lgkmcnt(0)\n" \
  "s_barrier\n" \
  "v_mfma_f32_16x16x32_bf16 a[64:67], v[0:3], a[160:163], 0\n" \
  "ds_read_b64_tr_b16 v[64:65], v128 offset:49152\n" \
  "v_mfma_f32_16x16x32_bf16 a[68:71], v[0:3], a[168:171], 0\n" \
  "ds_read_b64_tr_b16 v[66:67], v128 offset:50176\n" \
  "v_mfma_f32_16x16x32_bf16 a[72:75], v[8:11], a[160:163], 0\n" \
  "ds_read_b64_tr_b16 v[72:73], v129 offset:49152\n" \
  "v_mfma_f32_16x16x32_bf16 a[76:79], v[8:11], a[168:171], 0\n" \
  "ds_read_b64_tr_b16 v[74:75], v129 offset:50176\n" \
  "v_mfma_f32_16x16x32_bf16 a[80:83], v[16:19], a[160:163], 0\n" \
  "ds_read_b64_tr_b16 v[80:81], v128 offset:49408\n" \
  "v_mfma_f32_16x16x32_bf16 a[84:87], v[16:19], a[168:171], 0\n" \
  "ds_read_b64_tr_b16 v[82:83], v128 offset:50432\n" \
  "v_mfma_f32_16x16x32_bf16 a[88:91], v[24:27], a[160:163], 0\n" \
  "ds_read_b64_tr_b16 v[88:89], v129 offset:49408\n" \
  "v_mfma_f32_16x16x32_bf16 a[92:95], v[24:27], a[168:171], 0\n" \
  "ds_read_b64_tr_b16 v[90:91], v129 offset:50432\n" \
  "v_mfma_f32_16x16x32_bf16 a[96:99], v[32:35], a[160:163], 0\n" \
  "ds_read_b64_tr_b16 v[96:97], v128 offset:49664\n" \
  "v_mfma_f32_16x16x32_bf16 a[100:103], v[32:35], a[168:171], 0\n" \
  "ds_read_b64_tr_b16 v[98:99], v128 offset:50688\n" \
  "v_mfma_f32_16x16x32_bf16 a[104:107], v[40:43], a[160:163], 0\n" \
  "ds_read_b64_tr_b16 v[104:105], v129 offset:49664\n" \
  "v_mfma_f32_16x16x32_bf16 a[108:111], v[40:43], a[168:171], 0\n" \
  "ds_read_b64_tr_b16 v[106:107], v129 offset:50688\n" \
  "v_mfma_f32_16x16x32_bf16 a[112:115], v[48:51], a[160:163], 0\n" \
  "ds_read_b64_tr_b16 v[112:113], v128 offset:49920\n" \
  "v_mfma_f32_16x16x32_bf16 a[116:119], v[48:51], a[168:171], 0\n" \
  "ds_read_b64_tr_b16 v[114:115], v128 offset:50944\n" \
  "v_mfma_f32_16x16x32_bf16 a[120:123], v[56:59], a[160:163], 0\n" \
  "ds_read_b64_tr_b16 v[120:121], v129 offset:49920\n" \
  "v_mfma_f32_16x16x32_bf16 a[124:127], v[56:59], a[168:171], 0\n" \
  "ds_read_b64_tr_b16 v[122:123], v129 offset:50944\n" \
  "v_mfma_f32_16x16x32_bf16 a[64:67], v[4:7], a[164:167], a[64:67]\n" \
  "ds_read_b64_tr_b16 v[68:69], v128 offset:57344\n" \
  "v_mfma_f32_16x16x32_bf16 a[68:71], v[4:7], a[172:175], a[68:71]\n" \
  "ds_read_b64_tr_b16 v[70:71], v128 offset:58368\n" \
  "v_mfma_f32_16x16x32_bf16 a[72:75], v[12:15], a[164:167], a[72:75]\n" \
  "ds_read_b64_tr_b16 v[76:77], v129 offset:57344\n" \
  "v_mfma_f32_16x16x32_bf16 a[76:79], v[12:15], a[172:175], a[76:79]\n" \
  "ds_read_b64_tr_b16 v[78:79], v129 offset:58368\n" \
  "v_mfma_f32_16x16x32_bf16 a[80:83], v[20:23], a[164:167], a[80:83]\n" \
  "ds_read_b64_tr_b16 v[84:85], v128 offset:57600\n" \
  "v_mfma_f32_16x16x32_bf16 a[84:87], v[20:23], a[172:175], a[84:87]\n" \
  "ds_read_b64_tr_b16 v[86:87], v128 offset:58624\n" \
  "v_mfma_f32_16x16x32_bf16 a[88:91], v[28:31], a[164:167], a[88:91]\n" \
  "ds_read_b64_tr_b16 v[92:93], v129 offset:57600\n" \
  "v_mfma_f32_16x16x32_bf16 a[92:95], v[28:31], a[172:175], a[92:95]\n" \
  "ds_read_b64_tr_b16 v[94:95], v129 offset:58624\n" \
  "v_mfma_f32_16x16x32_bf16 a[96:99], v[36:39], a[164:167], a[96:99]\n" \
  "ds_read_b64_tr_b16 v[100:101], v128 offset:57856\n" \
  "v_mfma_f32_16x16x32_bf16 a[100:103], v[36:39], a[172:175], a[100:103]\n" \
  "ds_read_b64_tr_b16 v[102:103], v128 offset:58880\n" \
  "v_mfma_f32_16x16x32_bf16 a[104:107], v[44:47], a[164:167], a[104:107]\n" \
  "ds_read_b64_tr_b16 v[108:109], v129 offset:57856\n" \
  "v_mfma_f32_16x16x32_bf16 a[108:111], v[44:47], a[172:175], a[108:111]\n" \
  "ds_read_b64_tr_b16 v[110:111], v129 offset:58880\n" \
  "v_mfma_f32_16x16x32_bf16 a[112:115], v[52:55], a[164:167], a[112:115]\n" \
  "ds_read_b64_tr_b16 v[116:117], v128 offset:58112\n" \
  "v_mfma_f32_16x16x32_bf16 a[116:119], v[52:55], a[172:175], a[116:119]\n" \
  "ds_read_b64_tr_b16 v[118:119], v128 offset:59136\n" \
  "v_mfma_f32_16x16x32_bf16 a[120:123], v[60:63], a[164:167], a[120:123]\n" \
  "ds_read_b64_tr_b16 v[124:125], v129 offset:58112\n" \
  "v_mfma_f32_16x16x32_bf16 a[124:127], v[60:63], a[172:175], a[124:127]\n" \
  "ds_read_b64_tr_b16 v[126:127], v129 offset:59136\n" \
  "s_waitcnt vmcnt(20) lgkmcnt(0)\n" \
  "s_barrier\n" \
  "v_mfma_f32_16x16x32_bf16 a[64:67], v[64:67], a[176:179], a[64:67]\n" \
  "ds_read_b64_tr_b16 v[0:1], v130 offset:0\n" \
  "v_mfma_f32_16x16x32_bf16 a[68:71], v[64:67], a[184:187], a[68:71]\n" \
  "ds_read_b64_tr_b16 v[2:3], v130 offset:1024\n" \
  "v_mfma_f32_16x16x32_bf16 a[72:75], v[72:75], a[176:179], a[72:75]\n" \
  "ds_read_b64_tr_b16 v[8:9], v131 offset:0\n" \
  "v_mfma_f32_16x16x32_bf16 a[76:79], v[72:75], a[184:187], a[76:79]\n" \
  "ds_read_b64_tr_b16 v[10:11], v131 offset:1024\n" \
  "v_mfma_f32_16x16x32_bf16 a[80:83], v[80:83], a[176:179], a[80:83]\n" \
  "ds_read_b64_tr_b16 v[16:17], v130 offset:256\n" \
  "v_mfma_f32_16x16x32_bf16 a[84:87], v[80:83], a[184:187], a[84:87]\n" \
  "ds_read_b64_tr_b16 v[18:19], v130 offset:1280\n" \
  "v_mfma_f32_16x16x32_bf16 a[88:91], v[88:91], a[176:179], a[88:91]\n" \
  "ds_read_b64_tr_b16 v[24:25], v131 offset:256\n" \
  "v_mfma_f32_16x16x32_bf16 a[92:95], v[88:91], a[184:187], a[92:95]\n" \
  "ds_read_b64_tr_b16 v[26:27], v131 offset:1280\n" \
  "v_mfma_f32_16x16x32_bf16 a[96:99], v[96:99], a[176:179], a[96:99]\n" \
  "ds_read_b64_tr_b16 v[32:33], v130 offset:512\n" \
  "v_mfma_f32_16x16x32_bf16 a[100:103], v[96:99], a[184:187], a[100:103]\n" \
  "ds_read_b64_tr_b16 v[34:35], v130 offset:1536\n" \
  "v_mfma_f32_16x16x32_bf16 a[104:107], v[104:107], a[176:179], a[104:107]\n" \
  "ds_read_b64_tr_b16 v[40:41], v131 offset:512\n" \
  "v_mfma_f32_16x16x32_bf16 a[108:111], v[104:107], a[184:187], a[108:111]\n" \
  "ds_read_b64_tr_b16 v[42:43], v131 offset:1536\n" \
  "v_mfma_f32_16x16x32_bf16 a[112:115], v[112:115], a[176:179], a[112:115]\n" \
  "ds_read_b64_tr_b16 v[48:49], v130 offset:768\n" \
  "v_mfma_f32_16x16x32_bf16 a[116:119], v[112:115], a[184:187], a[116:119]\n" \
  "ds_read_b64_tr_b16 v[50:51], v130 offset:1792\n" \
  "v_mfma_f32_16x16x32_bf16 a[120:123], v[120:123], a[176:179], a[120:123]\n" \
  "ds_read_b64_tr_b16 v[56:57], v131 offset:768\n" \
  "v_mfma_f32_16x16x32_bf16 a[124:127], v[120:123], a[184:187], a[124:127]\n" \
  "ds_read_b64_tr_b16 v[58:59], v131 offset:1792\n" \
  "v_mfma_f32_16x16x32_bf16 a[64:67], v[68:71], a[180:183], a[64:67]\n" \
  "ds_read_b64_tr_b16 v[4:5], v130 offset:8192\n" \
  "v_mfma_f32_16x16x32_bf16 a[68:71], v[68:71], a[188:191], a[68:71]\n" \
  "ds_read_b64_tr_b16 v[6:7], v130 offset:9216\n" \
  "v_mfma_f32_16x16x32_bf16 a[72:75], v[76:79], a[180:183], a[72:75]\n" \
  "ds_read_b64_tr_b16 v[12:13], v131 offset:8192\n" \
  "v_mfma_f32_16x16x32_bf16 a[76:79], v[76:79], a[188:191], a[76:79]\n" \
  "ds_read_b64_tr_b16 v[14:15], v131 offset:9216\n" \
  "v_mfma_f32_16x16x32_bf16 a[80:83], v[84:87], a[180:183], a[80:83]\n" \
  "ds_read_b64_tr_b16 v[20:21], v130 offset:8448\n" \
  "v_mfma_f32_16x16x32_bf16 a[84:87], v[84:87], a[188:191], a[84:87]\n" \
  "ds_read_b64_tr_b16 v[22:23], v130 offset:9472\n" \
  "v_mfma_f32_16x16x32_bf16 a[88:91], v[92:95], a[180:183], a[88:91]\n" \
  "ds_read_b64_tr_b16 v[28:29], v131 offset:8448\n" \
  "v_mfma_f32_16x16x32_bf16 a[92:95], v[92:95], a[188:191], a[92:95]\n" \
  "ds_read_b64_tr_b16 v[30:31], v131 offset:9472\n" \
  "v_mfma_f32_16x16x32_bf16 a[96:99], v[100:103], a[180:183], a[96:99]\n" \
  "ds_read_b64_tr_b16 v[36:37], v130 offset:8704\n" \
  "v_mfma_f32_16x16x32_bf16 a[100:103], v[100:103], a[188:191], a[100:103]\n" \
  "ds_read_b64_tr_b16 v[38:39], v130 offset:9728\n" \
  "v_mfma_f32_16x16x32_bf16 a[104:107], v[108:111], a[180:183], a[104:107]\n" \
  "ds_read_b64_tr_b16 v[44:45], v131 offset:8704\n" \
  "v_mfma_f32_16x16x32_bf16 a[108:111], v[108:111], a[188:191], a[108:111]\n" \
  "ds_read_b64_tr_b16 v[46:47], v131 offset:9728\n" \
  "v_mfma_f32_16x16x32_bf16 a[112:115], v[116:119], a[180:183], a[112:115]\n" \
  "ds_read_b64_tr_b16 v[52:53], v130 offset:8960\n" \
  "v_mfma_f32_16x16x32_bf16 a[116:119], v[116:119], a[188:191], a[116:119]\n" \
  "ds_read_b64_tr_b16 v[54:55], v130 offset:9984\n" \
  "v_mfma_f32_16x16x32_bf16 a[120:123], v[124:127], a[180:183], a[120:123]\n" \
  "ds_read_b64_tr_b16 v[60:61], v131 offset:8960\n" \
  "v_mfma_f32_16x16x32_bf16 a[124:127], v[124:127], a[188:191], a[124:127]\n" \
  "ds_read_b64_tr_b16 v[62:63], v131 offset:9984\n" \
  "s_waitcnt vmcnt(12) lgkmcnt(0)\n" \
  "s_barrier\n" \
  "v_mfma_f32_16x16x32_bf16 a[64:67], v[0:3], a[192:195], a[64:67]\n" \
  "ds_read_b64_tr_b16 v[64:65], v130 offset:16384\n" \
  "v_mfma_f32_16x16x32_bf16 a[68:71], v[0:3], a[200:203], a[68:71]\n" \
  "ds_read_b64_tr_b16 v[66:67], v130 offset:17408\n" \
  "v_mfma_f32_16x16x32_bf16 a[72:75], v[8:11], a[192:195], a[72:75]\n" \
  "ds_read_b64_tr_b16 v[72:73], v131 offset:16384\n" \
  "v_mfma_f32_16x16x32_bf16 a[76:79], v[8:11], a[200:203], a[76:79]\n" \
  "ds_read_b64_tr_b16 v[74:75], v131 offset:17408\n" \
  "v_mfma_f32_16x16x32_bf16 a[80:83], v[16:19], a[192:195], a[80:83]\n" \
  "ds_read_b64_tr_b16 v[80:81], v130 offset:16640\n" \
  "v_mfma_f32_16x16x32_bf16 a[84:87], v[16:19], a[200:203], a[84:87]\n" \
  "ds_read_b64_tr_b16 v[82:83], v130 offset:17664\n" \
  "v_mfma_f32_16x16x32_bf16 a[88:91], v[24:27], a[192:195], a[88:91]\n" \
  "ds_read_b64_tr_b16 v[88:89], v131 offset:16640\n" \
  "v_mfma_f32_16x16x32_bf16 a[92:95], v[24:27], a[200:203], a[92:95]\n" \
  "ds_read_b64_tr_b16 v[90:91], v131 offset:17664\n" \
  "v_mfma_f32_16x16x32_bf16 a[96:99], v[32:35], a[192:195], a[96:99]\n" \
  "ds_read_b64_tr_b16 v[96:97], v130 offset:16896\n" \
  "v_mfma_f32_16x16x32_bf16 a[100:103], v[32:35], a[200:203], a[100:103]\n" \
  "ds_read_b64_tr_b16 v[98:99], v130 offset:17920\n" \
  "v_mfma_f32_16x16x32_bf16 a[104:107], v[40:43], a[192:195], a[104:107]\n" \
  "ds_read_b64_tr_b16 v[104:105], v131 offset:16896\n" \
  "v_mfma_f32_16x16x32_bf16 a[108:111], v[40:43], a[200:203], a[108:111]\n" \
  "ds_read_b64_tr_b16 v[106:107], v131 offset:17920\n" \
  "v_mfma_f32_16x16x32_bf16 a[112:115], v[48:51], a[192:195], a[112:115]\n" \
  "ds_read_b64_tr_b16 v[112:113], v130 offset:17152\n" \
  "v_mfma_f32_16x16x32_bf16 a[116:119], v[48:51], a[200:203], a[116:119]\n" \
  "ds_read_b64_tr_b16 v[114:115], v130 offset:18176\n" \
  "v_mfma_f32_16x16x32_bf16 a[120:123], v[56:59], a[192:195], a[120:123]\n" \
  "ds_read_b64_tr_b16 v[120:121], v131 offset:17152\n" \
  "v_mfma_f32_16x16x32_bf16 a[124:127], v[56:59], a[200:203], a[124:127]\n" \
  "ds_read_b64_tr_b16 v[122:123], v131 offset:18176\n" \
  "v_mfma_f32_16x16x32_bf16 a[64:67], v[4:7], a[196:199], a[64:67]\n" \
  "ds_read_b64_tr_b16 v[68:69], v130 offset:24576\n" \
  "v_mfma_f32_16x16x32_bf16 a[68:71], v[4:7], a[204:207], a[68:71]\n" \
  "ds_read_b64_tr_b16 v[70:71], v130 offset:25600\n" \
  "v_mfma_f32_16x16x32_bf16 a[72:75], v[12:15], a[196:199], a[72:75]\n" \
  "ds_read_b64_tr_b16 v[76:77], v131 offset:24576\n" \
  "v_mfma_f32_16x16x32_bf16 a[76:79], v[12:15], a[204:207], a[76:79]\n" \
  "ds_read_b64_tr_b16 v[78:79], v131 offset:25600\n" \
  "v_mfma_f32_16x16x32_bf16 a[80:83], v[20:23], a[196:199], a[80:83]\n" \
  "ds_read_b64_tr_b16 v[84:85], v130 offset:24832\n" \
  "v_mfma_f32_16x16x32_bf16 a[84:87], v[20:23], a[204:207], a[84:87]\n" \
  "ds_read_b64_tr_b16 v[86:87], v130 offset:25856\n" \
  "v_mfma_f32_16x16x32_bf16 a[88:91], v[28:31], a[196:199], a[88:91]\n" \
  "ds_read_b64_tr_b16 v[92:93], v131 offset:24832\n" \
  "v_mfma_f32_16x16x32_bf16 a[92:95], v[28:31], a[204:207], a[92:95]\n" \
  "ds_read_b64_tr_b16 v[94:95], v131 offset:25856\n" \
  "v_mfma_f32_16x16x32_bf16 a[96:99], v[36:39], a[196:199], a[96:99]\n" \
  "ds_read_b64_tr_b16 v[100:101], v130 offset:25088\n" \
  "v_mfma_f32_16x16x32_bf16 a[100:103], v[36:39], a[204:207], a[100:103]\n" \
  "ds_read_b64_tr_b16 v[102:103], v130 offset:26112\n" \
  "v_mfma_f32_16x16x32_bf16 a[104:107], v[44:47], a[196:199], a[104:107]\n" \
  "ds_read_b64_tr_b16 v[108:109], v131 offset:25088\n" \
  "v_mfma_f32_16x16x32_bf16 a[108:111], v[44:47], a[204:207], a[108:111]\n" \
  "ds_read_b64_tr_b16 v[110:111], v131 offset:26112\n" \
  "v_mfma_f32_16x16x32_bf16 a[112:115], v[52:55], a[196:199], a[112:115]\n" \
  "ds_read_b64_tr_b16 v[116:117], v130 offset:25344\n" \
  "v_mfma_f32_16x16x32_bf16 a[116:119], v[52:55], a[204:207], a[116:119]\n" \
  "ds_read_b64_tr_b16 v[118:119], v130 offset:26368\n" \
  "v_mfma_f32_16x16x32_bf16 a[120:123], v[60:63], a[196:199], a[120:123]\n" \
  "ds_read_b64_tr_b16 v[124:125], v131 offset:25344\n" \
  "v_mfma_f32_16x16x32_bf16 a[124:127], v[60:63], a[204:207], a[124:127]\n" \
  "ds_read_b64_tr_b16 v[126:127], v131 offset:26368\n" \
  "s_waitcnt vmcnt(4) lgkmcnt(0)\n" \
  "s_barrier\n" \
  "v_mfma_f32_16x16x32_bf16 a[64:67], v[64:67], a[208:211], a[64:67]\n" \
  "ds_read_b64_tr_b16 v[0:1], v130 offset:32768\n" \
  "v_mfma_f32_16x16x32_bf16 a[68:71], v[64:67], a[216:219], a[68:71]\n" \
  "ds_read_b64_tr_b16 v[2:3], v130 offset:33792\n" \
  "v_mfma_f32_16x16x32_bf16 a[72:75], v[72:75], a[208:211], a[72:75]\n" \
  "ds_read_b64_tr_b16 v[8:9], v131 offset:32768\n" \
  "v_mfma_f32_16x16x32_bf16 a[76:79], v[72:75], a[216:219], a[76:79]\n" \
  "ds_read_b64_tr_b16 v[10:11], v131 offset:33792\n" \
  "v_mfma_f32_16x16x32_bf16 a[80:83], v[80:83], a[208:211], a[80:83]\n" \
  "ds_read_b64_tr_b16 v[16:17], v130 offset:33024\n" \
  "v_mfma_f32_16x16x32_bf16 a[84:87], v[80:83], a[216:219], a[84:87]\n" \
  "ds_read_b64_tr_b16 v[18:19], v130 offset:34048\n" \
  "v_mfma_f32_16x16x32_bf16 a[88:91], v[88:91], a[208:211], a[88:91]\n" \
  "ds_read_b64_tr_b16 v[24:25], v131 offset:33024\n" \
  "v_mfma_f32_16x16x32_bf16 a[92:95], v[88:91], a[216:219], a[92:95]\n" \
  "ds_read_b64_tr_b16 v[26:27], v131 offset:34048\n" \
  "v_mfma_f32_16x16x32_bf16 a[96:99], v[96:99], a[208:211], a[96:99]\n" \
  "ds_read_b64_tr_b16 v[32:33], v130 offset:33280\n" \
  "v_mfma_f32_16x16x32_bf16 a[100:103], v[96:99], a[216:219], a[100:103]\n" \
  "ds_read_b64_tr_b16 v[34:35], v130 offset:34304\n" \
  "v_mfma_f32_16x16x32_bf16 a[104:107], v[104:107], a[208:211], a[104:107]\n" \
  "ds_read_b64_tr_b16 v[40:41], v131 offset:33280\n" \
  "v_mfma_f32_16x16x32_bf16 a[108:111], v[104:107], a[216:219], a[108:111]\n" \
  "ds_read_b64_tr_b16 v[42:43], v131 offset:34304\n" \
  "v_mfma_f32_16x16x32_bf16 a[112:115], v[112:115], a[208:211], a[112:115]\n" \
  "ds_read_b64_tr_b16 v[48:49], v130 offset:33536\n" \
  "v_mfma_f32_16x16x32_bf16 a[116:119], v[112:115], a[216:219], a[116:119]\n" \
  "ds_read_b64_tr_b16 v[50:51], v130 offset:34560\n" \
  "v_mfma_f32_16x16x32_bf16 a[120:123], v[120:123], a[208:211], a[120:123]\n" \
  "ds_read_b64_tr_b16 v[56:57], v131 offset:33536\n" \
  "v_mfma_f32_16x16x32_bf16 a[124:127], v[120:123], a[216:219], a[124:127]\n" \
  "ds_read_b64_tr_b16 v[58:59], v131 offset:34560\n" \
  "v_mfma_f32_16x16x32_bf16 a[64:67], v[68:71], a[212:215], a[64:67]\n" \
  "ds_read_b64_tr_b16 v[4:5], v130 offset:40960\n" \
  "v_mfma_f32_16x16x32_bf16 a[68:71], v[68:71], a[220:223], a[68:71]\n" \
  "ds_read_b64_tr_b16 v[6:7], v130 offset:41984\n" \
  "v_mfma_f32_16x16x32_bf16 a[72:75], v[76:79], a[212:215], a[72:75]\n" \
  "ds_read_b64_tr_b16 v[12:13], v131 offset:40960\n" \
  "v_mfma_f32_16x16x32_bf16 a[76:79], v[76:79], a[220:223], a[76:79]\n" \
  "ds_read_b64_tr_b16 v[14:15], v131 offset:41984\n" \
  "v_mfma_f32_16x16x32_bf16 a[80:83], v[84:87], a[212:215], a[80:83]\n" \
  "ds_read_b64_tr_b16 v[20:21], v130 offset:41216\n" \
  "v_mfma_f32_16x16x32_bf16 a[84:87], v[84:87], a[220:223], a[84:87]\n" \
  "ds_read_b64_tr_b16 v[22:23], v130 offset:42240\n" \
  "v_mfma_f32_16x16x32_bf16 a[88:91], v[92:95], a[212:215], a[88:91]\n" \
  "ds_read_b64_tr_b16 v[28:29], v131 offset:41216\n" \
  "v_mfma_f32_16x16x32_bf16 a[92:95], v[92:95], a[220:223], a[92:95]\n" \
  "ds_read_b64_tr_b16 v[30:31], v131 offset:42240\n" \
  "v_mfma_f32_16x16x32_bf16 a[96:99], v[100:103], a[212:215], a[96:99]\n" \
  "ds_read_b64_tr_b16 v[36:37], v130 offset:41472\n" \
  "v_mfma_f32_16x16x32_bf16 a[100:103], v[100:103], a[220:223], a[100:103]\n" \
  "ds_read_b64_tr_b16 v[38:39], v130 offset:42496\n" \
  "v_mfma_f32_16x16x32_bf16 a[104:107], v[108:111], a[212:215], a[104:107]\n" \
  "ds_read_b64_tr_b16 v[44:45], v131 offset:41472\n" \
  "v_mfma_f32_16x16x32_bf16 a[108:111], v[108:111], a[220:223], a[108:111]\n" \
  "ds_read_b64_tr_b16 v[46:47], v131 offset:42496\n" \
  "v_mfma_f32_16x16x32_bf16 a[112:115], v[116:119], a[212:215], a[112:115]\n" \
  "ds_read_b64_tr_b16 v[52:53], v130 offset:41728\n" \
  "v_mfma_f32_16x16x32_bf16 a[116:119], v[116:119], a[220:223], a[116:119]\n" \
  "ds_read_b64_tr_b16 v[54:55], v130 offset:42752\n" \
  "v_mfma_f32_16x16x32_bf16 a[120:123], v[124:127], a[212:215], a[120:123]\n" \
  "ds_read_b64_tr_b16 v[60:61], v131 offset:41728\n" \
  "v_mfma_f32_16x16x32_bf16 a[124:127], v[124:127], a[220:223], a[124:127]\n" \
  "ds_read_b64_tr_b16 v[62:63], v131 offset:42752\n" \
  "s_waitcnt vmcnt(0) lgkmcnt(0)\n" \
  "v_mfma_f32_16x16x32_bf16 a[64:67], v[0:3], a[224:227], a[64:67]\n" \
  "v_mfma_f32_16x16x32_bf16 a[68:71], v[0:3], a[232:235], a[68:71]\n" \
  "v_mfma_f32_16x16x32_bf16 a[72:75], v[8:11], a[224:227], a[72:75]\n" \
  "v_mfma_f32_16x16x32_bf16 a[76:79], v[8:11], a[232:235], a[76:79]\n" \
  "v_mfma_f32_16x16x32_bf16 a[80:83], v[16:19], a[224:227], a[80:83]\n" \
  "v_mfma_f32_16x16x32_bf16 a[84:87], v[16:19], a[232:235], a[84:87]\n" \
  "v_mfma_f32_16x16x32_bf16 a[88:91], v[24:27], a[224:227], a[88:91]\n" \
  "v_mfma_f32_16x16x32_bf16 a[92:95], v[24:27], a[232:235], a[92:95]\n" \
  "v_mfma_f32_16x16x32_bf16 a[96:99], v[32:35], a[224:227], a[96:99]\n" \
  "v_mfma_f32_16x16x32_bf16 a[100:103], v[32:35], a[232:235], a[100:103]\n" \
  "v_mfma_f32_16x16x32_bf16 a[104:107], v[40:43], a[224:227], a[104:107]\n" \
  "v_mfma_f32_16x16x32_bf16 a[108:111], v[40:43], a[232:235], a[108:111]\n" \
  "v_mfma_f32_16x16x32_bf16 a[112:115], v[48:51], a[224:227], a[112:115]\n" \
  "v_mfma_f32_16x16x32_bf16 a[116:119], v[48:51], a[232:235], a[116:119]\n" \
  "v_mfma_f32_16x16x32_bf16 a[120:123], v[56:59], a[224:227], a[120:123]\n" \
  "v_mfma_f32_16x16x32_bf16 a[124:127], v[56:59], a[232:235], a[124:127]\n" \
  "v_mfma_f32_16x16x32_bf16 a[64:67], v[4:7], a[228:231], a[64:67]\n" \
  "v_mfma_f32_16x16x32_bf16 a[68:71], v[4:7], a[236:239], a[68:71]\n" \
  "v_mfma_f32_16x16x32_bf16 a[72:75], v[12:15], a[228:231], a[72:75]\n" \
  "v_mfma_f32_16x16x32_bf16 a[76:79], v[12:15], a[236:239], a[76:79]\n" \
  "v_mfma_f32_16x16x32_bf16 a[80:83], v[20:23], a[228:231], a[80:83]\n" \
  "v_mfma_f32_16x16x32_bf16 a[84:87], v[20:23], a[236:239], a[84:87]\n" \
  "v_mfma_f32_16x16x32_bf16 a[88:91], v[28:31], a[228:231], a[88:91]\n" \
  "v_mfma_f32_16x16x32_bf16 a[92:95], v[28:31], a[236:239], a[92:95]\n" \
  "v_mfma_f32_16x16x32_bf16 a[96:99], v[36:39], a[228:231], a[96:99]\n" \
  "v_mfma_f32_16x16x32_bf16 a[100:103], v[36:39], a[236:239], a[100:103]\n" \
  "v_mfma_f32_16x16x32_bf16 a[104:107], v[44:47], a[228:231], a[104:107]\n" \
  "v_mfma_f32_16x16x32_bf16 a[108:111], v[44:47], a[236:239], a[108:111]\n" \
  "v_mfma_f32_16x16x32_bf16 a[112:115], v[52:55], a[228:231], a[112:115]\n" \
  "v_mfma_f32_16x16x32_bf16 a[116:119], v[52:55], a[236:239], a[116:119]\n" \
  "v_mfma_f32_16x16x32_bf16 a[120:123], v[60:63], a[228:231], a[120:123]\n" \
  "v_mfma_f32_16x16x32_bf16 a[124:127], v[60:63], a[236:239], a[124:127]\n" \
  "s_nop 7\n" \
  "s_nop 7\n" \
  "v_lshlrev_b32 v168, 4, v160\n" \
  "s_lshl_b32 s75, %16, 13\n" \
  "s_cmp_eq_u32 s74, 3\n" \
  "s_cselect_b32 s73, 4, s74\n" \
  "s_lshl_b32 s73, s73, 15\n" \
  "s_add_u32 s75, s75, s73\n" \
  "s_cmp_eq_u32 s74, 2\n" \
  "s_cbranch_scc1 PUB2%=\n" \
  "v_accvgpr_read_b32 v0, a0\n" \
  "v_accvgpr_read_b32 v1, a1\n" \
  "v_accvgpr_read_b32 v2, a2\n" \
  "v_accvgpr_read_b32 v3, a3\n" \
  "v_accvgpr_read_b32 v4, a8\n" \
  "v_accvgpr_read_b32 v5, a9\n" \
  "v_accvgpr_read_b32 v6, a10\n" \
  "v_accvgpr_read_b32 v7, a11\n" \
  "v_accvgpr_read_b32 v8, a64\n" \
  "v_accvgpr_read_b32 v9, a65\n" \
  "v_accvgpr_read_b32 v10, a66\n" \
  "v_accvgpr_read_b32 v11, a67\n" \
  "v_accvgpr_read_b32 v12, a72\n" \
  "v_accvgpr_read_b32 v13, a73\n" \
  "v_accvgpr_read_b32 v14, a74\n" \
  "v_accvgpr_read_b32 v15, a75\n" \
  "v_pk_add_f32 v[0:1], v[0:1], v[8:9]\n" \
  "v_pk_add_f32 v[2:3], v[2:3], v[10:11]\n" \
  "v_pk_add_f32 v[4:5], v[4:5], v[12:13]\n" \
  "v_pk_add_f32 v[6:7], v[6:7], v[14:15]\n" \
  "v_cvt_pk_bf16_f32 v16, v0, v1\n" \
  "v_cvt_pk_bf16_f32 v17, v2, v3\n" \
  "v_cvt_pk_bf16_f32 v18, v4, v5\n" \
  "v_cvt_pk_bf16_f32 v19, v6, v7\n" \
  "buffer_store_dwordx4 v[16:19], v168, s[56:59], s75 offen offset:0 sc1\n" \
  "v_accvgpr_read_b32 v32, a4\n" \
  "v_accvgpr_read_b32 v33, a5\n" \
  "v_accvgpr_read_b32 v34, a6\n" \
  "v_accvgpr_read_b32 v35, a7\n" \
  "v_accvgpr_read_b32 v36, a12\n" \
  "v_accvgpr_read_b32 v37, a13\n" \
  "v_accvgpr_read_b32 v38, a14\n" \
  "v_accvgpr_read_b32 v39, a15\n" \
  "v_accvgpr_read_b32 v40, a68\n" \
  "v_accvgpr_read_b32 v41, a69\n" \
  "v_accvgpr_read_b32 v42, a70\n" \
  "v_accvgpr_read_b32 v43, a71\n" \
  "v_accvgpr_read_b32 v44, a76\n" \
  "v_accvgpr_read_b32 v45, a77\n" \
  "v_accvgpr_read_b32 v46, a78\n" \
  "v_accvgpr_read_b32 v47, a79\n" \
  "v_pk_add_f32 v[32:33], v[32:33], v[40:41]\n" \
  "v_pk_add_f32 v[34:35], v[34:35], v[42:43]\n" \
  "v_pk_add_f32 v[36:37], v[36:37], v[44:45]\n" \
  "v_pk_add_f32 v[38:39], v[38:39], v[46:47]\n" \
  "v_cvt_pk_bf16_f32 v48, v32, v33\n" \
  "v_cvt_pk_bf16_f32 v49, v34, v35\n" \
  "v_cvt_pk_bf16_f32 v50, v36, v37\n" \
  "v_cvt_pk_bf16_f32 v51, v38, v39\n" \
  "buffer_store_dwordx4 v[48:51], v168, s[56:59], s75 offen offset:1024 sc1\n" \
  "v_accvgpr_read_b32 v64, a16\n" \
  "v_accvgpr_read_b32 v65, a17\n" \
  "v_accvgpr_read_b32 v66, a18\n" \
  "v_accvgpr_read_b32 v67, a19\n" \
  "v_accvgpr_read_b32 v68, a24\n" \
  "v_accvgpr_read_b32 v69, a25\n" \
  "v_accvgpr_read_b32 v70, a26\n" \
  "v_accvgpr_read_b32 v71, a27\n" \
  "v_accvgpr_read_b32 v72, a80\n" \
  "v_accvgpr_read_b32 v73, a81\n" \
  "v_accvgpr_read_b32 v74, a82\n" \
  "v_accvgpr_read_b32 v75, a83\n" \
  "v_accvgpr_read_b32 v76, a88\n" \
  "v_accvgpr_read_b32 v77, a89\n" \
  "v_accvgpr_read_b32 v78, a90\n" \
  "v_accvgpr_read_b32 v79, a91\n" \
  "v_pk_add_f32 v[64:65], v[64:65], v[72:73]\n" \
  "v_pk_add_f32 v[66:67], v[66:67], v[74:75]\n" \
  "v_pk_add_f32 v[68:69], v[68:69], v[76:77]\n" \
  "v_pk_add_f32 v[70:71], v[70:71], v[78:79]\n" \
  "v_cvt_pk_bf16_f32 v80, v64, v65\n" \
  "v_cvt_pk_bf16_f32 v81, v66, v67\n" \
  "v_cvt_pk_bf16_f32 v82, v68, v69\n" \
  "v_cvt_pk_bf16_f32 v83, v70, v71\n" \
  "buffer_store_dwordx4 v[80:83], v168, s[56:59], s75 offen offset:2048 sc1\n" \
  "v_accvgpr_read_b32 v96, a20\n" \
  "v_accvgpr_read_b32 v97, a21\n" \
  "v_accvgpr_read_b32 v98, a22\n" \
  "v_accvgpr_read_b32 v99, a23\n" \
  "v_accvgpr_read_b32 v100, a28\n" \
  "v_accvgpr_read_b32 v101, a29\n" \
  "v_accvgpr_read_b32 v102, a30\n" \
  "v_accvgpr_read_b32 v103, a31\n" \
  "v_accvgpr_read_b32 v104, a84\n" \
  "v_accvgpr_read_b32 v105, a85\n" \
  "v_accvgpr_read_b32 v106, a86\n" \
  "v_accvgpr_read_b32 v107, a87\n" \
  "v_accvgpr_read_b32 v108, a92\n" \
  "v_accvgpr_read_b32 v109, a93\n" \
  "v_accvgpr_read_b32 v110, a94\n" \
  "v_accvgpr_read_b32 v111, a95\n" \
  "v_pk_add_f32 v[96:97], v[96:97], v[104:105]\n" \
  "v_pk_add_f32 v[98:99], v[98:99], v[106:107]\n" \
  "v_pk_add_f32 v[100:101], v[100:101], v[108:109]\n" \
  "v_pk_add_f32 v[102:103], v[102:103], v[110:111]\n" \
  "v_cvt_pk_bf16_f32 v112, v96, v97\n" \
  "v_cvt_pk_bf16_f32 v113, v98, v99\n" \
  "v_cvt_pk_bf16_f32 v114, v100, v101\n" \
  "v_cvt_pk_bf16_f32 v115, v102, v103\n" \
  "buffer_store_dwordx4 v[112:115], v168, s[56:59], s75 offen offset:3072 sc1\n" \
  "s_add_u32 s75, s75, 4096\n" \
  "v_accvgpr_read_b32 v0, a32\n" \
  "v_accvgpr_read_b32 v1, a33\n" \
  "v_accvgpr_read_b32 v2, a34\n" \
  "v_accvgpr_read_b32 v3, a35\n" \
  "v_accvgpr_read_b32 v4, a40\n" \
  "v_accvgpr_read_b32 v5, a41\n" \
  "v_accvgpr_read_b32 v6, a42\n" \
  "v_accvgpr_read_b32 v7, a43\n" \
  "v_accvgpr_read_b32 v8, a96\n" \
  "v_accvgpr_read_b32 v9, a97\n" \
  "v_accvgpr_read_b32 v10, a98\n" \
  "v_accvgpr_read_b32 v11, a99\n" \
  "v_accvgpr_read_b32 v12, a104\n" \
  "v_accvgpr_read_b32 v13, a105\n" \
  "v_accvgpr_read_b32 v14, a106\n" \
  "v_accvgpr_read_b32 v15, a107\n" \
  "v_pk_add_f32 v[0:1], v[0:1], v[8:9]\n" \
  "v_pk_add_f32 v[2:3], v[2:3], v[10:11]\n" \
  "v_pk_add_f32 v[4:5], v[4:5], v[12:13]\n" \
  "v_pk_add_f32 v[6:7], v[6:7], v[14:15]\n" \
  "v_cvt_pk_bf16_f32 v16, v0, v1\n" \
  "v_cvt_pk_bf16_f32 v17, v2, v3\n" \
  "v_cvt_pk_bf16_f32 v18, v4, v5\n" \
  "v_cvt_pk_bf16_f32 v19, v6, v7\n" \
  "buffer_store_dwordx4 v[16:19], v168, s[56:59], s75 offen offset:0 sc1\n" \
  "v_accvgpr_read_b32 v32, a36\n" \
  "v_accvgpr_read_b32 v33, a37\n" \
  "v_accvgpr_read_b32 v34, a38\n" \
  "v_accvgpr_read_b32 v35, a39\n" \
  "v_accvgpr_read_b32 v36, a44\n" \
  "v_accvgpr_read_b32 v37, a45\n" \
  "v_accvgpr_read_b32 v38, a46\n" \
  "v_accvgpr_read_b32 v39, a47\n" \
  "v_accvgpr_read_b32 v40, a100\n" \
  "v_accvgpr_read_b32 v41, a101\n" \
  "v_accvgpr_read_b32 v42, a102\n" \
  "v_accvgpr_read_b32 v43, a103\n" \
  "v_accvgpr_read_b32 v44, a108\n" \
  "v_accvgpr_read_b32 v45, a109\n" \
  "v_accvgpr_read_b32 v46, a110\n" \
  "v_accvgpr_read_b32 v47, a111\n" \
  "v_pk_add_f32 v[32:33], v[32:33], v[40:41]\n" \
  "v_pk_add_f32 v[34:35], v[34:35], v[42:43]\n" \
  "v_pk_add_f32 v[36:37], v[36:37], v[44:45]\n" \
  "v_pk_add_f32 v[38:39], v[38:39], v[46:47]\n" \
  "v_cvt_pk_bf16_f32 v48, v32, v33\n" \
  "v_cvt_pk_bf16_f32 v49, v34, v35\n" \
  "v_cvt_pk_bf16_f32 v50, v36, v37\n" \
  "v_cvt_pk_bf16_f32 v51, v38, v39\n" \
  "buffer_store_dwordx4 v[48:51], v168, s[56:59], s75 offen offset:1024 sc1\n" \
  "v_accvgpr_read_b32 v64, a48\n" \
  "v_accvgpr_read_b32 v65, a49\n" \
  "v_accvgpr_read_b32 v66, a50\n" \
  "v_accvgpr_read_b32 v67, a51\n" \
  "v_accvgpr_read_b32 v68, a56\n" \
  "v_accvgpr_read_b32 v69, a57\n" \
  "v_accvgpr_read_b32 v70, a58\n" \
  "v_accvgpr_read_b32 v71, a59\n" \
  "v_accvgpr_read_b32 v72, a112\n" \
  "v_accvgpr_read_b32 v73, a113\n" \
  "v_accvgpr_read_b32 v74, a114\n" \
  "v_accvgpr_read_b32 v75, a115\n" \
  "v_accvgpr_read_b32 v76, a120\n" \
  "v_accvgpr_read_b32 v77, a121\n" \
  "v_accvgpr_read_b32 v78, a122\n" \
  "v_accvgpr_read_b32 v79, a123\n" \
  "v_pk_add_f32 v[64:65], v[64:65], v[72:73]\n" \
  "v_pk_add_f32 v[66:67], v[66:67], v[74:75]\n" \
  "v_pk_add_f32 v[68:69], v[68:69], v[76:77]\n" \
  "v_pk_add_f32 v[70:71], v[70:71], v[78:79]\n" \
  "v_cvt_pk_bf16_f32 v80, v64, v65\n" \
  "v_cvt_pk_bf16_f32 v81, v66, v67\n" \
  "v_cvt_pk_bf16_f32 v82, v68, v69\n" \
  "v_cvt_pk_bf16_f32 v83, v70, v71\n" \
  "buffer_store_dwordx4 v[80:83], v168, s[56:59], s75 offen offset:2048 sc1\n" \
  "v_accvgpr_read_b32 v96, a52\n" \
  "v_accvgpr_read_b32 v97, a53\n" \
  "v_accvgpr_read_b32 v98, a54\n" \
  "v_accvgpr_read_b32 v99, a55\n" \
  "v_accvgpr_read_b32 v100, a60\n" \
  "v_accvgpr_read_b32 v101, a61\n" \
  "v_accvgpr_read_b32 v102, a62\n" \
  "v_accvgpr_read_b32 v103, a63\n" \
  "v_accvgpr_read_b32 v104, a116\n" \
  "v_accvgpr_read_b32 v105, a117\n" \
  "v_accvgpr_read_b32 v106, a118\n" \
  "v_accvgpr_read_b32 v107, a119\n" \
  "v_accvgpr_read_b32 v108, a124\n" \
  "v_accvgpr_read_b32 v109, a125\n" \
  "v_accvgpr_read_b32 v110, a126\n" \
  "v_accvgpr_read_b32 v111, a127\n" \
  "v_pk_add_f32 v[96:97], v[96:97], v[104:105]\n" \
  "v_pk_add_f32 v[98:99], v[98:99], v[106:107]\n" \
  "v_pk_add_f32 v[100:101], v[100:101], v[108:109]\n" \
  "v_pk_add_f32 v[102:103], v[102:103], v[110:111]\n" \
  "v_cvt_pk_bf16_f32 v112, v96, v97\n" \
  "v_cvt_pk_bf16_f32 v113, v98, v99\n" \
  "v_cvt_pk_bf16_f32 v114, v100, v101\n" \
  "v_cvt_pk_bf16_f32 v115, v102, v103\n" \
  "buffer_store_dwordx4 v[112:115], v168, s[56:59], s75 offen offset:3072 sc1\n" \
  "s_add_u32 s75, s75, 4096\n" \
  "s_branch PUBD%=\n" \
  "PUB2%=:\n" \
  "s_add_u32 s76, s75, 0x8000\n" \
  "v_accvgpr_read_b32 v0, a0\n" \
  "v_accvgpr_read_b32 v1, a1\n" \
  "v_accvgpr_read_b32 v2, a2\n" \
  "v_accvgpr_read_b32 v3, a3\n" \
  "v_accvgpr_read_b32 v4, a8\n" \
  "v_accvgpr_read_b32 v5, a9\n" \
  "v_accvgpr_read_b32 v6, a10\n" \
  "v_accvgpr_read_b32 v7, a11\n" \
  "v_accvgpr_read_b32 v8, a64\n" \
  "v_accvgpr_read_b32 v9, a65\n" \
  "v_accvgpr_read_b32 v10, a66\n" \
  "v_accvgpr_read_b32 v11, a67\n" \
  "v_accvgpr_read_b32 v12, a72\n" \
  "v_accvgpr_read_b32 v13, a73\n" \
  "v_accvgpr_read_b32 v14, a74\n" \
  "v_accvgpr_read_b32 v15, a75\n" \
  "v_cvt_pk_bf16_f32 v16, v0, v1\n" \
  "v_cvt_pk_bf16_f32 v17, v2, v3\n" \
  "v_cvt_pk_bf16_f32 v18, v4, v5\n" \
  "v_cvt_pk_bf16_f32 v19, v6, v7\n" \
  "buffer_store_dwordx4 v[16:19], v168, s[56:59], s75 offen offset:0 sc1\n" \
  "v_cvt_pk_bf16_f32 v20, v8, v9\n" \
  "v_cvt_pk_bf16_f32 v21, v10, v11\n" \
  "v_cvt_pk_bf16_f32 v22, v12, v13\n" \
  "v_cvt_pk_bf16_f32 v23, v14, v15\n" \
  "buffer_store_dwordx4 v[20:23], v168, s[56:59], s76 offen offset:0 sc1\n" \
  "v_accvgpr_read_b32 v32, a4\n" \
  "v_accvgpr_read_b32 v33, a5\n" \
  "v_accvgpr_read_b32 v34, a6\n" \
  "v_accvgpr_read_b32 v35, a7\n" \
  "v_accvgpr_read_b32 v36, a12\n" \
  "v_accvgpr_read_b32 v37, a13\n" \
  "v_accvgpr_read_b32 v38, a14\n" \
  "v_accvgpr_read_b32 v39, a15\n" \
  "v_accvgpr_read_b32 v40, a68\n" \
  "v_accvgpr_read_b32 v41, a69\n" \
  "v_accvgpr_read_b32 v42, a70\n" \
  "v_accvgpr_read_b32 v43, a71\n" \
  "v_accvgpr_read_b32 v44, a76\n" \
  "v_accvgpr_read_b32 v45, a77\n" \
  "v_accvgpr_read_b32 v46, a78\n" \
  "v_accvgpr_read_b32 v47, a79\n" \
  "v_cvt_pk_bf16_f32 v48, v32, v33\n" \
  "v_cvt_pk_bf16_f32 v49, v34, v35\n" \
  "v_cvt_pk_bf16_f32 v50, v36, v37\n" \
  "v_cvt_pk_bf16_f32 v51, v38, v39\n" \
  "buffer_store_dwordx4 v[48:51], v168, s[56:59], s75 offen offset:1024 sc1\n" \
  "v_cvt_pk_bf16_f32 v52, v40, v41\n" \
  "v_cvt_pk_bf16_f32 v53, v42, v43\n" \
  "v_cvt_pk_bf16_f32 v54, v44, v45\n" \
  "v_cvt_pk_bf16_f32 v55, v46, v47\n" \
  "buffer_store_dwordx4 v[52:55], v168, s[56:59], s76 offen offset:1024 sc1\n" \
  "v_accvgpr_read_b32 v64, a16\n" \
  "v_accvgpr_read_b32 v65, a17\n" \
  "v_accvgpr_read_b32 v66, a18\n" \
  "v_accvgpr_read_b32 v67, a19\n" \
  "v_accvgpr_read_b32 v68, a24\n" \
  "v_accvgpr_read_b32 v69, a25\n" \
  "v_accvgpr_read_b32 v70, a26\n" \
  "v_accvgpr_read_b32 v71, a27\n" \
  "v_accvgpr_read_b32 v72, a80\n" \
  "v_accvgpr_read_b32 v73, a81\n" \
  "v_accvgpr_read_b32 v74, a82\n" \
  "v_accvgpr_read_b32 v75, a83\n" \
  "v_accvgpr_read_b32 v76, a88\n" \
  "v_accvgpr_read_b32 v77, a89\n" \
  "v_accvgpr_read_b32 v78, a90\n" \
  "v_accvgpr_read_b32 v79, a91\n" \
  "v_cvt_pk_bf16_f32 v80, v64, v65\n" \
  "v_cvt_pk_bf16_f32 v81, v66, v67\n" \
  "v_cvt_pk_bf16_f32 v82, v68, v69\n" \
  "v_cvt_pk_bf16_f32 v83, v70, v71\n" \
  "buffer_store_dwordx4 v[80:83], v168, s[56:59], s75 offen offset:2048 sc1\n" \
  "v_cvt_pk_bf16_f32 v84, v72, v73\n" \
  "v_cvt_pk_bf16_f32 v85, v74, v75\n" \
  "v_cvt_pk_bf16_f32 v86, v76, v77\n" \
  "v_cvt_pk_bf16_f32 v87, v78, v79\n" \
  "buffer_store_dwordx4 v[84:87], v168, s[56:59], s76 offen offset:2048 sc1\n" \
  "v_accvgpr_read_b32 v96, a20\n" \
  "v_accvgpr_read_b32 v97, a21\n" \
  "v_accvgpr_read_b32 v98, a22\n" \
  "v_accvgpr_read_b32 v99, a23\n" \
  "v_accvgpr_read_b32 v100, a28\n" \
  "v_accvgpr_read_b32 v101, a29\n" \
  "v_accvgpr_read_b32 v102, a30\n" \
  "v_accvgpr_read_b32 v103, a31\n" \
  "v_accvgpr_read_b32 v104, a84\n" \
  "v_accvgpr_read_b32 v105, a85\n" \
  "v_accvgpr_read_b32 v106, a86\n" \
  "v_accvgpr_read_b32 v107, a87\n" \
  "v_accvgpr_read_b32 v108, a92\n" \
  "v_accvgpr_read_b32 v109, a93\n" \
  "v_accvgpr_read_b32 v110, a94\n" \
  "v_accvgpr_read_b32 v111, a95\n" \
  "v_cvt_pk_bf16_f32 v112, v96, v97\n" \
  "v_cvt_pk_bf16_f32 v113, v98, v99\n" \
  "v_cvt_pk_bf16_f32 v114, v100, v101\n" \
  "v_cvt_pk_bf16_f32 v115, v102, v103\n" \
  "buffer_store_dwordx4 v[112:115], v168, s[56:59], s75 offen offset:3072 sc1\n" \
  "v_cvt_pk_bf16_f32 v116, v104, v105\n" \
  "v_cvt_pk_bf16_f32 v117, v106, v107\n" \
  "v_cvt_pk_bf16_f32 v118, v108, v109\n" \
  "v_cvt_pk_bf16_f32 v119, v110, v111\n" \
  "buffer_store_dwordx4 v[116:119], v168, s[56:59], s76 offen offset:3072 sc1\n" \
  "s_add_u32 s75, s75, 4096\n" \
  "s_add_u32 s76, s76, 4096\n" \
  "v_accvgpr_read_b32 v0, a32\n" \
  "v_accvgpr_read_b32 v1, a33\n" \
  "v_accvgpr_read_b32 v2, a34\n" \
  "v_accvgpr_read_b32 v3, a35\n" \
  "v_accvgpr_read_b32 v4, a40\n" \
  "v_accvgpr_read_b32 v5, a41\n" \
  "v_accvgpr_read_b32 v6, a42\n" \
  "v_accvgpr_read_b32 v7, a43\n" \
  "v_accvgpr_read_b32 v8, a96\n" \
  "v_accvgpr_read_b32 v9, a97\n" \
  "v_accvgpr_read_b32 v10, a98\n" \
  "v_accvgpr_read_b32 v11, a99\n" \
  "v_accvgpr_read_b32 v12, a104\n" \
  "v_accvgpr_read_b32 v13, a105\n" \
  "v_accvgpr_read_b32 v14, a106\n" \
  "v_accvgpr_read_b32 v15, a107\n" \
  "v_cvt_pk_bf16_f32 v16, v0, v1\n" \
  "v_cvt_pk_bf16_f32 v17, v2, v3\n" \
  "v_cvt_pk_bf16_f32 v18, v4, v5\n" \
  "v_cvt_pk_bf16_f32 v19, v6, v7\n" \
  "buffer_store_dwordx4 v[16:19], v168, s[56:59], s75 offen offset:0 sc1\n" \
  "v_cvt_pk_bf16_f32 v20, v8, v9\n" \
  "v_cvt_pk_bf16_f32 v21, v10, v11\n" \
  "v_cvt_pk_bf16_f32 v22, v12, v13\n" \
  "v_cvt_pk_bf16_f32 v23, v14, v15\n" \
  "buffer_store_dwordx4 v[20:23], v168, s[56:59], s76 offen offset:0 sc1\n" \
  "v_accvgpr_read_b32 v32, a36\n" \
  "v_accvgpr_read_b32 v33, a37\n" \
  "v_accvgpr_read_b32 v34, a38\n" \
  "v_accvgpr_read_b32 v35, a39\n" \
  "v_accvgpr_read_b32 v36, a44\n" \
  "v_accvgpr_read_b32 v37, a45\n" \
  "v_accvgpr_read_b32 v38, a46\n" \
  "v_accvgpr_read_b32 v39, a47\n" \
  "v_accvgpr_read_b32 v40, a100\n" \
  "v_accvgpr_read_b32 v41, a101\n" \
  "v_accvgpr_read_b32 v42, a102\n" \
  "v_accvgpr_read_b32 v43, a103\n" \
  "v_accvgpr_read_b32 v44, a108\n" \
  "v_accvgpr_read_b32 v45, a109\n" \
  "v_accvgpr_read_b32 v46, a110\n" \
  "v_accvgpr_read_b32 v47, a111\n" \
  "v_cvt_pk_bf16_f32 v48, v32, v33\n" \
  "v_cvt_pk_bf16_f32 v49, v34, v35\n" \
  "v_cvt_pk_bf16_f32 v50, v36, v37\n" \
  "v_cvt_pk_bf16_f32 v51, v38, v39\n" \
  "buffer_store_dwordx4 v[48:51], v168, s[56:59], s75 offen offset:1024 sc1\n" \
  "v_cvt_pk_bf16_f32 v52, v40, v41\n" \
  "v_cvt_pk_bf16_f32 v53, v42, v43\n" \
  "v_cvt_pk_bf16_f32 v54, v44, v45\n" \
  "v_cvt_pk_bf16_f32 v55, v46, v47\n" \
  "buffer_store_dwordx4 v[52:55], v168, s[56:59], s76 offen offset:1024 sc1\n" \
  "v_accvgpr_read_b32 v64, a48\n" \
  "v_accvgpr_read_b32 v65, a49\n" \
  "v_accvgpr_read_b32 v66, a50\n" \
  "v_accvgpr_read_b32 v67, a51\n" \
  "v_accvgpr_read_b32 v68, a56\n" \
  "v_accvgpr_read_b32 v69, a57\n" \
  "v_accvgpr_read_b32 v70, a58\n" \
  "v_accvgpr_read_b32 v71, a59\n" \
  "v_accvgpr_read_b32 v72, a112\n" \
  "v_accvgpr_read_b32 v73, a113\n" \
  "v_accvgpr_read_b32 v74, a114\n" \
  "v_accvgpr_read_b32 v75, a115\n" \
  "v_accvgpr_read_b32 v76, a120\n" \
  "v_accvgpr_read_b32 v77, a121\n" \
  "v_accvgpr_read_b32 v78, a122\n" \
  "v_accvgpr_read_b32 v79, a123\n" \
  "v_cvt_pk_bf16_f32 v80, v64, v65\n" \
  "v_cvt_pk_bf16_f32 v81, v66, v67\n" \
  "v_cvt_pk_bf16_f32 v82, v68, v69\n" \
  "v_cvt_pk_bf16_f32 v83, v70, v71\n" \
  "buffer_store_dwordx4 v[80:83], v168, s[56:59], s75 offen offset:2048 sc1\n" \
  "v_cvt_pk_bf16_f32 v84, v72, v73\n" \
  "v_cvt_pk_bf16_f32 v85, v74, v75\n" \
  "v_cvt_pk_bf16_f32 v86, v76, v77\n" \
  "v_cvt_pk_bf16_f32 v87, v78, v79\n" \
  "buffer_store_dwordx4 v[84:87], v168, s[56:59], s76 offen offset:2048 sc1\n" \
  "v_accvgpr_read_b32 v96, a52\n" \
  "v_accvgpr_read_b32 v97, a53\n" \
  "v_accvgpr_read_b32 v98, a54\n" \
  "v_accvgpr_read_b32 v99, a55\n" \
  "v_accvgpr_read_b32 v100, a60\n" \
  "v_accvgpr_read_b32 v101, a61\n" \
  "v_accvgpr_read_b32 v102, a62\n" \
  "v_accvgpr_read_b32 v103, a63\n" \
  "v_accvgpr_read_b32 v104, a116\n" \
  "v_accvgpr_read_b32 v105, a117\n" \
  "v_accvgpr_read_b32 v106, a118\n" \
  "v_accvgpr_read_b32 v107, a119\n" \
  "v_accvgpr_read_b32 v108, a124\n" \
  "v_accvgpr_read_b32 v109, a125\n" \
  "v_accvgpr_read_b32 v110, a126\n" \
  "v_accvgpr_read_b32 v111, a127\n" \
  "v_cvt_pk_bf16_f32 v112, v96, v97\n" \
  "v_cvt_pk_bf16_f32 v113, v98, v99\n" \
  "v_cvt_pk_bf16_f32 v114, v100, v101\n" \
  "v_cvt_pk_bf16_f32 v115, v102, v103\n" \
  "buffer_store_dwordx4 v[112:115], v168, s[56:59], s75 offen offset:3072 sc1\n" \
  "v_cvt_pk_bf16_f32 v116, v104, v105\n" \
  "v_cvt_pk_bf16_f32 v117, v106, v107\n" \
  "v_cvt_pk_bf16_f32 v118, v108, v109\n" \
  "v_cvt_pk_bf16_f32 v119, v110, v111\n" \
  "buffer_store_dwordx4 v[116:119], v168, s[56:59], s76 offen offset:3072 sc1\n" \
  "s_add_u32 s75, s75, 4096\n" \
  "s_add_u32 s76, s76, 4096\n" \
  "PUBD%=:\n" \
  "s_waitcnt vmcnt(0)\n" \
  "s_barrier\n" \
  "s_lshl_b32 s73, s74, 5\n" \
  "s_add_u32 s73, s73, %18\n" \
  "s_lshl_b32 s73, s73, 10\n" \
  "s_add_u32 s73, s73, %19\n" \
  "s_lshl_b32 s76, %16, 6\n" \
  "s_add_u32 s73, s73, s76\n" \
  "v_lshlrev_b32 v169, 10, v161\n" \
  "v_lshl_add_u32 v169, v162, 3, v169\n" \
  "v_add_u32 v169, s73, v169\n" \
  "v_add_u32 v170, 0x4000, v169\n" \
  "buffer_load_dwordx2 v[144:145], v169, s[60:63], 0 offen offset:0\n" \
  "buffer_load_dwordx2 v[146:147], v169, s[60:63], 0 offen offset:32\n" \
  "buffer_load_dwordx2 v[148:149], v170, s[60:63], 0 offen offset:0\n" \
  "buffer_load_dwordx2 v[150:151], v170, s[60:63], 0 offen offset:32\n" \
  "s_cmp_lg_u32 %16, 0\n" \
  "s_cbranch_scc1 NOPOLL%=\n" \
  "v_mov_b32 v172, %20\n" \
  "v_mov_b32 v173, %21\n" \
  "v_mov_b32 v165, 1\n" \
  "s_mov_b64 s[78:79], exec\n" \
  "s_mov_b64 exec, 1\n" \
  "global_atomic_add v[172:173], v165, off sc1\n" \
  "s_mov_b32 s76, 0\n" \
  "POLL%=:\n" \
  "global_load_dword v165, v[172:173], off sc1\n" \
  "s_waitcnt vmcnt(0)\n" \
  "v_readfirstlane_b32 s77, v165\n" \
  "s_add_u32 s76, s76, 1\n" \
  "s_cmp_ge_u32 s77, 4\n" \
  "s_cbranch_scc1 POLLD%=\n" \
  "s_sleep 1\n" \
  "s_cmp_lt_u32 s76, 0x200000\n" \
  "s_cbranch_scc1 POLL%=\n" \
  "POLLD%=:\n" \
  "s_mov_b64 exec, s[78:79]\n" \
  "NOPOLL%=:\n" \
  "s_barrier\n" \
  "s_lshl_b32 s73, s74, 13\n" \
  "s_lshl_b32 s76, %16, 11\n" \
  "s_add_u32 s73, s73, s76\n" \
  "buffer_load_dwordx4 v[100:103], v168, s[56:59], s73 offen offset:0 sc1\n" \
  "buffer_load_dwordx4 v[104:107], v168, s[56:59], s73 offen offset:1024 sc1\n" \
  "s_add_u32 s73, s73, 0x8000\n" \
  "buffer_load_dwordx4 v[108:111], v168, s[56:59], s73 offen offset:0 sc1\n" \
  "buffer_load_dwordx4 v[112:115], v168, s[56:59], s73 offen offset:1024 sc1\n" \
  "s_add_u32 s73, s73, 0x8000\n" \
  "buffer_load_dwordx4 v[116:119], v168, s[56:59], s73 offen offset:0 sc1\n" \
  "buffer_load_dwordx4 v[120:123], v168, s[56:59], s73 offen offset:1024 sc1\n" \
  "s_add_u32 s73, s73, 0x8000\n" \
  "buffer_load_dwordx4 v[124:127], v168, s[56:59], s73 offen offset:0 sc1\n" \
  "buffer_load_dwordx4 v[128:131], v168, s[56:59], s73 offen offset:1024 sc1\n" \
  "s_add_u32 s73, s73, 0x8000\n" \
  "buffer_load_dwordx4 v[132:135], v168, s[56:59], s73 offen offset:0 sc1\n" \
  "buffer_load_dwordx4 v[136:139], v168, s[56:59], s73 offen offset:1024 sc1\n" \
  "s_mov_b32 s76, 0xbfb8aa3b\n" \
  "s_waitcnt vmcnt(0)\n" \
  "v_lshlrev_b32 v0, 16, v100\n" \
  "v_and_b32 v1, 0xffff0000, v100\n" \
  "v_lshlrev_b32 v2, 16, v101\n" \
  "v_and_b32 v3, 0xffff0000, v101\n" \
  "v_lshlrev_b32 v4, 16, v108\n" \
  "v_and_b32 v5, 0xffff0000, v108\n" \
  "v_lshlrev_b32 v6, 16, v109\n" \
  "v_and_b32 v7, 0xffff0000, v109\n" \
  "v_lshlrev_b32 v8, 16, v116\n" \
  "v_and_b32 v9, 0xffff0000, v116\n" \
  "v_lshlrev_b32 v10, 16, v117\n" \
  "v_and_b32 v11, 0xffff0000, v117\n" \
  "v_lshlrev_b32 v12, 16, v124\n" \
  "v_and_b32 v13, 0xffff0000, v124\n" \
  "v_lshlrev_b32 v14, 16, v125\n" \
  "v_and_b32 v15, 0xffff0000, v125\n" \
  "v_lshlrev_b32 v16, 16, v132\n" \
  "v_and_b32 v17, 0xffff0000, v132\n" \
  "v_lshlrev_b32 v18, 16, v133\n" \
  "v_and_b32 v19, 0xffff0000, v133\n" \
  "v_add_f32 v0, v0, v4\n" \
  "v_add_f32 v12, v12, v16\n" \
  "v_add_f32 v1, v1, v5\n" \
  "v_add_f32 v13, v13, v17\n" \
  "v_add_f32 v2, v2, v6\n" \
  "v_add_f32 v14, v14, v18\n" \
  "v_add_f32 v3, v3, v7\n" \
  "v_add_f32 v15, v15, v19\n" \
  "v_add_f32 v0, v0, v8\n" \
  "v_add_f32 v1, v1, v9\n" \
  "v_add_f32 v2, v2, v10\n" \
  "v_add_f32 v3, v3, v11\n" \
  "v_cvt_pk_bf16_f32 v80, v0, v1\n" \
  "v_cvt_pk_bf16_f32 v81, v2, v3\n" \
  "v_cvt_pk_bf16_f32 v82, v12, v13\n" \
  "v_cvt_pk_bf16_f32 v83, v14, v15\n" \
  "v_lshlrev_b32 v0, 16, v80\n" \
  "v_and_b32 v1, 0xffff0000, v80\n" \
  "v_lshlrev_b32 v4, 16, v82\n" \
  "v_and_b32 v5, 0xffff0000, v82\n" \
  "v_lshlrev_b32 v2, 16, v81\n" \
  "v_and_b32 v3, 0xffff0000, v81\n" \
  "v_lshlrev_b32 v6, 16, v83\n" \
  "v_and_b32 v7, 0xffff0000, v83\n" \
  "v_mul_f32 v8, s76, v4\n" \
  "v_mul_f32 v9, s76, v5\n" \
  "v_mul_f32 v10, s76, v6\n" \
  "v_mul_f32 v11, s76, v7\n" \
  "v_exp_f32 v8, v8\n" \
  "v_exp_f32 v9, v9\n" \
  "v_exp_f32 v10, v10\n" \
  "v_exp_f32 v11, v11\n" \
  "v_add_f32 v8, 1.0, v8\n" \
  "v_add_f32 v9, 1.0, v9\n" \
  "v_add_f32 v10, 1.0, v10\n" \
  "v_add_f32 v11, 1.0, v11\n" \
  "v_rcp_f32 v8, v8\n" \
  "v_rcp_f32 v9, v9\n" \
  "v_rcp_f32 v10, v10\n" \
  "v_rcp_f32 v11, v11\n" \
  "v_mul_f32 v4, v4, v8\n" \
  "v_mul_f32 v5, v5, v9\n" \
  "v_mul_f32 v6, v6, v10\n" \
  "v_mul_f32 v7, v7, v11\n" \
  "v_cvt_pk_bf16_f32 v80, v4, v5\n" \
  "v_cvt_pk_bf16_f32 v81, v6, v7\n" \
  "v_lshlrev_b32 v4, 16, v80\n" \
  "v_and_b32 v5, 0xffff0000, v80\n" \
  "v_lshlrev_b32 v6, 16, v81\n" \
  "v_and_b32 v7, 0xffff0000, v81\n" \
  "v_mul_f32 v0, v0, v4\n" \
  "v_mul_f32 v1, v1, v5\n" \
  "v_mul_f32 v2, v2, v6\n" \
  "v_mul_f32 v3, v3, v7\n" \
  "v_cvt_pk_bf16_f32 v80, v0, v1\n" \
  "v_cvt_pk_bf16_f32 v81, v2, v3\n" \
  "v_lshlrev_b32 v0, 16, v80\n" \
  "v_and_b32 v1, 0xffff0000, v80\n" \
  "v_lshlrev_b32 v4, 16, v144\n" \
  "v_and_b32 v5, 0xffff0000, v144\n" \
  "v_lshlrev_b32 v2, 16, v81\n" \
  "v_and_b32 v3, 0xffff0000, v81\n" \
  "v_lshlrev_b32 v6, 16, v145\n" \
  "v_and_b32 v7, 0xffff0000, v145\n" \
  "v_add_f32 v0, v0, v4\n" \
  "v_add_f32 v1, v1, v5\n" \
  "v_add_f32 v2, v2, v6\n" \
  "v_add_f32 v3, v3, v7\n" \
  "v_cvt_pk_bf16_f32 v84, v0, v1\n" \
  "v_cvt_pk_bf16_f32 v85, v2, v3\n" \
  "buffer_store_dwordx2 v[84:85], v169, s[64:67], 0 offen offset:0\n" \
  "v_lshlrev_b32 v20, 16, v102\n" \
  "v_and_b32 v21, 0xffff0000, v102\n" \
  "v_lshlrev_b32 v22, 16, v103\n" \
  "v_and_b32 v23, 0xffff0000, v103\n" \
  "v_lshlrev_b32 v24, 16, v110\n" \
  "v_and_b32 v25, 0xffff0000, v110\n" \
  "v_lshlrev_b32 v26, 16, v111\n" \
  "v_and_b32 v27, 0xffff0000, v111\n" \
  "v_lshlrev_b32 v28, 16, v118\n" \
  "v_and_b32 v29, 0xffff0000, v118\n" \
  "v_lshlrev_b32 v30, 16, v119\n" \
  "v_and_b32 v31, 0xffff0000, v119\n" \
  "v_lshlrev_b32 v32, 16, v126\n" \
  "v_and_b32 v33, 0xffff0000, v126\n" \
  "v_lshlrev_b32 v34, 16, v127\n" \
  "v_and_b32 v35, 0xffff0000, v127\n" \
  "v_lshlrev_b32 v36, 16, v134\n" \
  "v_and_b32 v37, 0xffff0000, v134\n" \
  "v_lshlrev_b32 v38, 16, v135\n" \
  "v_and_b32 v39, 0xffff0000, v135\n" \
  "v_add_f32 v20, v20, v24\n" \
  "v_add_f32 v32, v32, v36\n" \
  "v_add_f32 v21, v21, v25\n" \
  "v_add_f32 v33, v33, v37\n" \
  "v_add_f32 v22, v22, v26\n" \
  "v_add_f32 v34, v34, v38\n" \
  "v_add_f32 v23, v23, v27\n" \
  "v_add_f32 v35, v35, v39\n" \
  "v_add_f32 v20, v20, v28\n" \
  "v_add_f32 v21, v21, v29\n" \
  "v_add_f32 v22, v22, v30\n" \
  "v_add_f32 v23, v23, v31\n" \
  "v_cvt_pk_bf16_f32 v88, v20, v21\n" \
  "v_cvt_pk_bf16_f32 v89, v22, v23\n" \
  "v_cvt_pk_bf16_f32 v90, v32, v33\n" \
  "v_cvt_pk_bf16_f32 v91, v34, v35\n" \
  "v_lshlrev_b32 v20, 16, v88\n" \
  "v_and_b32 v21, 0xffff0000, v88\n" \
  "v_lshlrev_b32 v24, 16, v90\n" \
  "v_and_b32 v25, 0xffff0000, v90\n" \
  "v_lshlrev_b32 v22, 16, v89\n" \
  "v_and_b32 v23, 0xffff0000, v89\n" \
  "v_lshlrev_b32 v26, 16, v91\n" \
  "v_and_b32 v27, 0xffff0000, v91\n" \
  "v_mul_f32 v28, s76, v24\n" \
  "v_mul_f32 v29, s76, v25\n" \
  "v_mul_f32 v30, s76, v26\n" \
  "v_mul_f32 v31, s76, v27\n" \
  "v_exp_f32 v28, v28\n" \
  "v_exp_f32 v29, v29\n" \
  "v_exp_f32 v30, v30\n" \
  "v_exp_f32 v31, v31\n" \
  "v_add_f32 v28, 1.0, v28\n" \
  "v_add_f32 v29, 1.0, v29\n" \
  "v_add_f32 v30, 1.0, v30\n" \
  "v_add_f32 v31, 1.0, v31\n" \
  "v_rcp_f32 v28, v28\n" \
  "v_rcp_f32 v29, v29\n" \
  "v_rcp_f32 v30, v30\n" \
  "v_rcp_f32 v31, v31\n" \
  "v_mul_f32 v24, v24, v28\n" \
  "v_mul_f32 v25, v25, v29\n" \
  "v_mul_f32 v26, v26, v30\n" \
  "v_mul_f32 v27, v27, v31\n" \
  "v_cvt_pk_bf16_f32 v88, v24, v25\n" \
  "v_cvt_pk_bf16_f32 v89, v26, v27\n" \
  "v_lshlrev_b32 v24, 16, v88\n" \
  "v_and_b32 v25, 0xffff0000, v88\n" \
  "v_lshlrev_b32 v26, 16, v89\n" \
  "v_and_b32 v27, 0xffff0000, v89\n" \
  "v_mul_f32 v20, v20, v24\n" \
  "v_mul_f32 v21, v21, v25\n" \
  "v_mul_f32 v22, v22, v26\n" \
  "v_mul_f32 v23, v23, v27\n" \
  "v_cvt_pk_bf16_f32 v88, v20, v21\n" \
  "v_cvt_pk_bf16_f32 v89, v22, v23\n" \
  "v_lshlrev_b32 v20, 16, v88\n" \
  "v_and_b32 v21, 0xffff0000, v88\n" \
  "v_lshlrev_b32 v24, 16, v146\n" \
  "v_and_b32 v25, 0xffff0000, v146\n" \
  "v_lshlrev_b32 v22, 16, v89\n" \
  "v_and_b32 v23, 0xffff0000, v89\n" \
  "v_lshlrev_b32 v26, 16, v147\n" \
  "v_and_b32 v27, 0xffff0000, v147\n" \
  "v_add_f32 v20, v20, v24\n" \
  "v_add_f32 v21, v21, v25\n" \
  "v_add_f32 v22, v22, v26\n" \
  "v_add_f32 v23, v23, v27\n" \
  "v_cvt_pk_bf16_f32 v92, v20, v21\n" \
  "v_cvt_pk_bf16_f32 v93, v22, v23\n" \
  "buffer_store_dwordx2 v[92:93], v169, s[64:67], 0 offen offset:32\n" \
  "v_lshlrev_b32 v40, 16, v104\n" \
  "v_and_b32 v41, 0xffff0000, v104\n" \
  "v_lshlrev_b32 v42, 16, v105\n" \
  "v_and_b32 v43, 0xffff0000, v105\n" \
  "v_lshlrev_b32 v44, 16, v112\n" \
  "v_and_b32 v45, 0xffff0000, v112\n" \
  "v_lshlrev_b32 v46, 16, v113\n" \
  "v_and_b32 v47, 0xffff0000, v113\n" \
  "v_lshlrev_b32 v48, 16, v120\n" \
  "v_and_b32 v49, 0xffff0000, v120\n" \
  "v_lshlrev_b32 v50, 16, v121\n" \
  "v_and_b32 v51, 0xffff0000, v121\n" \
  "v_lshlrev_b32 v52, 16, v128\n" \
  "v_and_b32 v53, 0xffff0000, v128\n" \
  "v_lshlrev_b32 v54, 16, v129\n" \
  "v_and_b32 v55, 0xffff0000, v129\n" \
  "v_lshlrev_b32 v56, 16, v136\n" \
  "v_and_b32 v57, 0xffff0000, v136\n" \
  "v_lshlrev_b32 v58, 16, v137\n" \
  "v_and_b32 v59, 0xffff0000, v137\n" \
  "v_add_f32 v40, v40, v44\n" \
  "v_add_f32 v52, v52, v56\n" \
  "v_add_f32 v41, v41, v45\n" \
  "v_add_f32 v53, v53, v57\n" \
  "v_add_f32 v42, v42, v46\n" \
  "v_add_f32 v54, v54, v58\n" \
  "v_add_f32 v43, v43, v47\n" \
  "v_add_f32 v55, v55, v59\n" \
  "v_add_f32 v40, v40, v48\n" \
  "v_add_f32 v41, v41, v49\n" \
  "v_add_f32 v42, v42, v50\n" \
  "v_add_f32 v43, v43, v51\n" \
  "v_cvt_pk_bf16_f32 v80, v40, v41\n" \
  "v_cvt_pk_bf16_f32 v81, v42, v43\n" \
  "v_cvt_pk_bf16_f32 v82, v52, v53\n" \
  "v_cvt_pk_bf16_f32 v83, v54, v55\n" \
  "v_lshlrev_b32 v40, 16, v80\n" \
  "v_and_b32 v41, 0xffff0000, v80\n" \
  "v_lshlrev_b32 v44, 16, v82\n" \
  "v_and_b32 v45, 0xffff0000, v82\n" \
  "v_lshlrev_b32 v42, 16, v81\n" \
  "v_and_b32 v43, 0xffff0000, v81\n" \
  "v_lshlrev_b32 v46, 16, v83\n" \
  "v_and_b32 v47, 0xffff0000, v83\n" \
  "v_mul_f32 v48, s76, v44\n" \
  "v_mul_f32 v49, s76, v45\n" \
  "v_mul_f32 v50, s76, v46\n" \
  "v_mul_f32 v51, s76, v47\n" \
  "v_exp_f32 v48, v48\n" \
  "v_exp_f32 v49, v49\n" \
  "v_exp_f32 v50, v50\n" \
  "v_exp_f32 v51, v51\n" \
  "v_add_f32 v48, 1.0, v48\n" \
  "v_add_f32 v49, 1.0, v49\n" \
  "v_add_f32 v50, 1.0, v50\n" \
  "v_add_f32 v51, 1.0, v51\n" \
  "v_rcp_f32 v48, v48\n" \
  "v_rcp_f32 v49, v49\n" \
  "v_rcp_f32 v50, v50\n" \
  "v_rcp_f32 v51, v51\n" \
  "v_mul_f32 v44, v44, v48\n" \
  "v_mul_f32 v45, v45, v49\n" \
  "v_mul_f32 v46, v46, v50\n" \
  "v_mul_f32 v47, v47, v51\n" \
  "v_cvt_pk_bf16_f32 v80, v44, v45\n" \
  "v_cvt_pk_bf16_f32 v81, v46, v47\n" \
  "v_lshlrev_b32 v44, 16, v80\n" \
  "v_and_b32 v45, 0xffff0000, v80\n" \
  "v_lshlrev_b32 v46, 16, v81\n" \
  "v_and_b32 v47, 0xffff0000, v81\n" \
  "v_mul_f32 v40, v40, v44\n" \
  "v_mul_f32 v41, v41, v45\n" \
  "v_mul_f32 v42, v42, v46\n" \
  "v_mul_f32 v43, v43, v47\n" \
  "v_cvt_pk_bf16_f32 v80, v40, v41\n" \
  "v_cvt_pk_bf16_f32 v81, v42, v43\n" \
  "v_lshlrev_b32 v40, 16, v80\n" \
  "v_and_b32 v41, 0xffff0000, v80\n" \
  "v_lshlrev_b32 v44, 16, v148\n" \
  "v_and_b32 v45, 0xffff0000, v148\n" \
  "v_lshlrev_b32 v42, 16, v81\n" \
  "v_and_b32 v43, 0xffff0000, v81\n" \
  "v_lshlrev_b32 v46, 16, v149\n" \
  "v_and_b32 v47, 0xffff0000, v149\n" \
  "v_add_f32 v40, v40, v44\n" \
  "v_add_f32 v41, v41, v45\n" \
  "v_add_f32 v42, v42, v46\n" \
  "v_add_f32 v43, v43, v47\n" \
  "v_cvt_pk_bf16_f32 v84, v40, v41\n" \
  "v_cvt_pk_bf16_f32 v85, v42, v43\n" \
  "buffer_store_dwordx2 v[84:85], v170, s[64:67], 0 offen offset:0\n" \
  "v_lshlrev_b32 v60, 16, v106\n" \
  "v_and_b32 v61, 0xffff0000, v106\n" \
  "v_lshlrev_b32 v62, 16, v107\n" \
  "v_and_b32 v63, 0xffff0000, v107\n" \
  "v_lshlrev_b32 v64, 16, v114\n" \
  "v_and_b32 v65, 0xffff0000, v114\n" \
  "v_lshlrev_b32 v66, 16, v115\n" \
  "v_and_b32 v67, 0xffff0000, v115\n" \
  "v_lshlrev_b32 v68, 16, v122\n" \
  "v_and_b32 v69, 0xffff0000, v122\n" \
  "v_lshlrev_b32 v70, 16, v123\n" \
  "v_and_b32 v71, 0xffff0000, v123\n" \
  "v_lshlrev_b32 v72, 16, v130\n" \
  "v_and_b32 v73, 0xffff0000, v130\n" \
  "v_lshlrev_b32 v74, 16, v131\n" \
  "v_and_b32 v75, 0xffff0000, v131\n" \
  "v_lshlrev_b32 v76, 16, v138\n" \
  "v_and_b32 v77, 0xffff0000, v138\n" \
  "v_lshlrev_b32 v78, 16, v139\n" \
  "v_and_b32 v79, 0xffff0000, v139\n" \
  "v_add_f32 v60, v60, v64\n" \
  "v_add_f32 v72, v72, v76\n" \
  "v_add_f32 v61, v61, v65\n" \
  "v_add_f32 v73, v73, v77\n" \
  "v_add_f32 v62, v62, v66\n" \
  "v_add_f32 v74, v74, v78\n" \
  "v_add_f32 v63, v63, v67\n" \
  "v_add_f32 v75, v75, v79\n" \
  "v_add_f32 v60, v60, v68\n" \
  "v_add_f32 v61, v61, v69\n" \
  "v_add_f32 v62, v62, v70\n" \
  "v_add_f32 v63, v63, v71\n" \
  "v_cvt_pk_bf16_f32 v88, v60, v61\n" \
  "v_cvt_pk_bf16_f32 v89, v62, v63\n" \
  "v_cvt_pk_bf16_f32 v90, v72, v73\n" \
  "v_cvt_pk_bf16_f32 v91, v74, v75\n" \
  "v_lshlrev_b32 v60, 16, v88\n" \
  "v_and_b32 v61, 0xffff0000, v88\n" \
  "v_lshlrev_b32 v64, 16, v90\n" \
  "v_and_b32 v65, 0xffff0000, v90\n" \
  "v_lshlrev_b32 v62, 16, v89\n" \
  "v_and_b32 v63, 0xffff0000, v89\n" \
  "v_lshlrev_b32 v66, 16, v91\n" \
  "v_and_b32 v67, 0xffff0000, v91\n" \
  "v_mul_f32 v68, s76, v64\n" \
  "v_mul_f32 v69, s76, v65\n" \
  "v_mul_f32 v70, s76, v66\n" \
  "v_mul_f32 v71, s76, v67\n" \
  "v_exp_f32 v68, v68\n" \
  "v_exp_f32 v69, v69\n" \
  "v_exp_f32 v70, v70\n" \
  "v_exp_f32 v71, v71\n" \
  "v_add_f32 v68, 1.0, v68\n" \
  "v_add_f32 v69, 1.0, v69\n" \
  "v_add_f32 v70, 1.0, v70\n" \
  "v_add_f32 v71, 1.0, v71\n" \
  "v_rcp_f32 v68, v68\n" \
  "v_rcp_f32 v69, v69\n" \
  "v_rcp_f32 v70, v70\n" \
  "v_rcp_f32 v71, v71\n" \
  "v_mul_f32 v64, v64, v68\n" \
  "v_mul_f32 v65, v65, v69\n" \
  "v_mul_f32 v66, v66, v70\n" \
  "v_mul_f32 v67, v67, v71\n" \
  "v_cvt_pk_bf16_f32 v88, v64, v65\n" \
  "v_cvt_pk_bf16_f32 v89, v66, v67\n" \
  "v_lshlrev_b32 v64, 16, v88\n" \
  "v_and_b32 v65, 0xffff0000, v88\n" \
  "v_lshlrev_b32 v66, 16, v89\n" \
  "v_and_b32 v67, 0xffff0000, v89\n" \
  "v_mul_f32 v60, v60, v64\n" \
  "v_mul_f32 v61, v61, v65\n" \
  "v_mul_f32 v62, v62, v66\n" \
  "v_mul_f32 v63, v63, v67\n" \
  "v_cvt_pk_bf16_f32 v88, v60, v61\n" \
  "v_cvt_pk_bf16_f32 v89, v62, v63\n" \
  "v_lshlrev_b32 v60, 16, v88\n" \
  "v_and_b32 v61, 0xffff0000, v88\n" \
  "v_lshlrev_b32 v64, 16, v150\n" \
  "v_and_b32 v65, 0xffff0000, v150\n" \
  "v_lshlrev_b32 v62, 16, v89\n" \
  "v_and_b32 v63, 0xffff0000, v89\n" \
  "v_lshlrev_b32 v66, 16, v151\n" \
  "v_and_b32 v67, 0xffff0000, v151\n" \
  "v_add_f32 v60, v60, v64\n" \
  "v_add_f32 v61, v61, v65\n" \
  "v_add_f32 v62, v62, v66\n" \
  "v_add_f32 v63, v63, v67\n" \
  "v_cvt_pk_bf16_f32 v92, v60, v61\n" \
  "v_cvt_pk_bf16_f32 v93, v62, v63\n" \
  "buffer_store_dwordx2 v[92:93], v170, s[64:67], 0 offen offset:32\n" \
  "s_waitcnt vmcnt(0)\n" \
  "CEND%=:\n" \
  ""

#define CLOB_V "v0","v1","v2","v3","v4","v5","v6","v7","v8","v9","v10","v11","v12","v13","v14","v15","v16","v17","v18","v19","v20","v21","v22","v23","v24","v25","v26","v27","v28","v29","v30","v31","v32","v33","v34","v35","v36","v37","v38","v39","v40","v41","v42","v43","v44","v45","v46","v47","v48","v49","v50","v51","v52","v53","v54","v55","v56","v57","v58","v59","v60","v61","v62","v63","v64","v65","v66","v67","v68","v69","v70","v71","v72","v73","v74","v75","v76","v77","v78","v79","v80","v81","v82","v83","v84","v85","v86","v87","v88","v89","v90","v91","v92","v93","v94","v95","v96","v97","v98","v99","v100","v101","v102","v103","v104","v105","v106","v107","v108","v109","v110","v111","v112","v113","v114","v115","v116","v117","v118","v119","v120","v121","v122","v123","v124","v125","v126","v127","v128","v129","v130","v131","v132","v133","v134","v135","v136","v137","v138","v139","v140","v141","v142","v143","v144","v145","v146","v147","v148","v149","v150","v151","v152","v153","v154","v155","v156","v157","v158","v159","v160","v161","v162","v163","v164","v165","v166","v167","v168","v169","v170","v171","v172","v173","v174","v175","v176","v177","v178","v179","v180","v181","v182","v183","v184","v185","v186","v187","v188","v189","v190","v191","v192","v193","v194","v195","v196","v197","v198","v199","v200","v201","v202","v203","v204","v205","v206","v207","v208","v209","v210","v211","v212","v213","v214","v215","v216","v217","v218","v219","v220","v221","v222","v223","v224","v225","v226","v227","v228","v229","v230","v231","v232","v233","v234","v235","v236","v237","v238","v239","v240","v241","v242","v243","v244","v245","v246","v247","v248","v249","v250","v251","v252","v253","v254","v255"
#define CLOB_A "a0","a1","a2","a3","a4","a5","a6","a7","a8","a9","a10","a11","a12","a13","a14","a15","a16","a17","a18","a19","a20","a21","a22","a23","a24","a25","a26","a27","a28","a29","a30","a31","a32","a33","a34","a35","a36","a37","a38","a39","a40","a41","a42","a43","a44","a45","a46","a47","a48","a49","a50","a51","a52","a53","a54","a55","a56","a57","a58","a59","a60","a61","a62","a63","a64","a65","a66","a67","a68","a69","a70","a71","a72","a73","a74","a75","a76","a77","a78","a79","a80","a81","a82","a83","a84","a85","a86","a87","a88","a89","a90","a91","a92","a93","a94","a95","a96","a97","a98","a99","a100","a101","a102","a103","a104","a105","a106","a107","a108","a109","a110","a111","a112","a113","a114","a115","a116","a117","a118","a119","a120","a121","a122","a123","a124","a125","a126","a127","a128","a129","a130","a131","a132","a133","a134","a135","a136","a137","a138","a139","a140","a141","a142","a143","a144","a145","a146","a147","a148","a149","a150","a151","a152","a153","a154","a155","a156","a157","a158","a159","a160","a161","a162","a163","a164","a165","a166","a167","a168","a169","a170","a171","a172","a173","a174","a175","a176","a177","a178","a179","a180","a181","a182","a183","a184","a185","a186","a187","a188","a189","a190","a191","a192","a193","a194","a195","a196","a197","a198","a199","a200","a201","a202","a203","a204","a205","a206","a207","a208","a209","a210","a211","a212","a213","a214","a215","a216","a217","a218","a219","a220","a221","a222","a223","a224","a225","a226","a227","a228","a229","a230","a231","a232","a233","a234","a235","a236","a237","a238","a239","a240","a241","a242","a243","a244","a245","a246","a247","a248","a249","a250","a251","a252","a253","a254","a255"
#define CLOB_S "s40","s41","s42","s43","s44","s45","s46","s47","s48","s49","s50","s51","s52","s53","s54","s55","s56","s57","s58","s59","s60","s61","s62","s63","s64","s65","s66","s67","s68","s69","s70","s71","s72","s73","s74","s75","s76","s77","s78","s79","s80","s81","s82","s83","s84","s85","s86","s87","s88","s89","s90","s91","s92","s93","s94","s95","s96","s97","s98","s99"
#define CLOB_V3 "v0","v1","v2","v3","v4","v5","v6","v7","v8","v9","v10","v11","v12","v13","v14","v15","v16","v17","v18","v19","v20","v21","v22","v23","v24","v25","v26","v27","v28","v29","v30","v31","v32","v33","v34","v35","v36","v37","v38","v39","v40","v41","v42","v43","v44","v45","v46","v47","v48","v49","v50","v51","v52","v53","v54","v55","v56","v57","v58","v59","v60","v61","v62","v63","v64","v65","v66","v67","v68","v69","v70","v71","v72","v73","v74","v75","v76","v77","v78","v79","v80","v81","v82","v83","v84","v85","v86","v87","v88","v89","v90","v91","v92","v93","v94","v95","v96","v97","v98","v99","v100","v101","v102","v103","v104","v105","v106","v107","v108","v109","v110","v111","v112","v113","v114","v115","v116","v117","v118","v119","v120","v121","v122","v123","v124","v125","v126","v127","v128","v129","v130","v131","v132","v133","v134","v135","v136","v137","v138","v139","v140","v141","v142","v143","v144","v145","v146","v147","v148","v149","v150","v151","v152","v153","v154","v155","v156","v157","v158","v159","v160","v161","v162","v163","v164","v165","v166","v167","v168","v169","v170","v171","v172","v173","v174","v175"
#define CLOB_A3 "a0","a1","a2","a3","a4","a5","a6","a7","a8","a9","a10","a11","a12","a13","a14","a15","a16","a17","a18","a19","a20","a21","a22","a23","a24","a25","a26","a27","a28","a29","a30","a31","a32","a33","a34","a35","a36","a37","a38","a39","a40","a41","a42","a43","a44","a45","a46","a47","a48","a49","a50","a51","a52","a53","a54","a55","a56","a57","a58","a59","a60","a61","a62","a63","a64","a65","a66","a67","a68","a69","a70","a71","a72","a73","a74","a75","a76","a77","a78","a79","a80","a81","a82","a83","a84","a85","a86","a87","a88","a89","a90","a91","a92","a93","a94","a95","a96","a97","a98","a99","a100","a101","a102","a103","a104","a105","a106","a107","a108","a109","a110","a111","a112","a113","a114","a115","a116","a117","a118","a119","a120","a121","a122","a123","a124","a125","a126","a127","a128","a129","a130","a131","a132","a133","a134","a135","a136","a137","a138","a139","a140","a141","a142","a143","a144","a145","a146","a147","a148","a149","a150","a151","a152","a153","a154","a155","a156","a157","a158","a159","a160","a161","a162","a163","a164","a165","a166","a167","a168","a169","a170","a171","a172","a173","a174","a175","a176","a177","a178","a179","a180","a181","a182","a183","a184","a185","a186","a187","a188","a189","a190","a191","a192","a193","a194","a195","a196","a197","a198","a199","a200","a201","a202","a203","a204","a205","a206","a207","a208","a209","a210","a211","a212","a213","a214","a215","a216","a217","a218","a219","a220","a221","a222","a223","a224","a225","a226","a227","a228","a229","a230","a231","a232","a233","a234","a235","a236","a237","a238","a239","a240","a241","a242","a243","a244","a245","a246","a247","a248","a249","a250","a251","a252","a253","a254","a255"
#define CLOB_S3 "s40","s41","s42","s43","s44","s45","s46","s47","s48","s49","s50","s51","s52","s53","s54","s55","s56","s57","s58","s59","s60","s61","s62","s63","s64","s65","s66","s67","s68","s69","s70","s71","s72","s73","s74","s75","s76","s77","s78","s79"
#define LO(p) "s"(rfl((unsigned)(unsigned long long)(p)))
#define HI(p) "s"(rfl((unsigned)((unsigned long long)(p) >> 32)))

__global__ __launch_bounds__(256, 1)
void fused_kernel(const unsigned short* __restrict__ XN, const unsigned short* __restrict__ WQ, const unsigned short* __restrict__ WG, const unsigned short* __restrict__ WU,
                  const unsigned short* __restrict__ K, const unsigned short* __restrict__ V, unsigned short* __restrict__ O, unsigned short* __restrict__ H,
                  const char* __restrict__ WO, const char* __restrict__ WD) {
  const unsigned wid = rfl(threadIdx.x >> 6);
  const int bid = blockIdx.x; const unsigned h = (bid & 7) * 4 + ((bid >> 3) & 3), qb = bid >> 5;
  const unsigned row0 = qb * 256 + wid * 64;
  const char* Kh = (const char*)K + (size_t)h * SK * 256; const char* Vh = (const char*)V + (size_t)h * SK * 256;
  const unsigned hflag = h < 28 ? 1u : 0u, f0 = h < 28 ? h * 64 : 0;
  const char* wq = (const char*)WQ + h * 256; const char* wg = (const char*)WG + f0 * 2; const char* wu = (const char*)WU + f0 * 2;
  const unsigned qbase = 98304 + wid * 16384;
  const unsigned osoff = row0 * 8192 + h * 4096;
  const unsigned gw = bid * 4 + wid; const char* wrm = gw < 512 ? WO + gw * 8192u : gw < 736 ? WD + (gw - 512) * 8192u : WO;
  const unsigned thr = 0x5f800000u;
  asm volatile(FUSED_ASM :: LO(Kh), HI(Kh), LO(Vh), HI(Vh), LO(O), HI(O), "s"(wid), "s"(rfl(osoff)), "s"(rfl(qbase)), "s"(rfl(thr)),
     LO(XN), HI(XN), LO(wq), HI(wq), "s"(rfl(DM * HD * 2 - h * 256)), LO(wg), HI(wg), LO(wu), HI(wu), "s"(rfl(DM * DF * 2 - f0 * 2)), LO(H), HI(H),
     "s"(rfl(row0 * 1024)), "s"(rfl(row0 * 3584 + f0 * 32)), "s"(rfl(hflag)), LO(wrm), HI(wrm)
     : CLOB_V, CLOB_A, CLOB_S, "vcc", "scc", "m0", "memory");
}

__global__ __launch_bounds__(256, 1)
void final_kernel(const unsigned short* __restrict__ O, const unsigned short* __restrict__ H, const unsigned short* __restrict__ WO, const unsigned short* __restrict__ WD,
                  const unsigned short* __restrict__ X, unsigned short* __restrict__ OUT, char* __restrict__ PART, unsigned* __restrict__ CNT) {
  const unsigned wid = rfl(threadIdx.x >> 6);
  const unsigned bid = blockIdx.x, idx = bid >> 3, sp = (bid & 7) >> 1, bm = (bid & 1) * 8 + (idx >> 2), bn = idx & 3, tile = bm * 4 + bn;
  const char* wo = (const char*)WO + bn * 256; const char* wd = (const char*)WD + bn * 256;
  const char* part = PART + (size_t)tile * 5 * 32768; const unsigned* cnt = CNT + tile * 32;
  asm volatile(FIN_ASM :: LO(O), HI(O), LO(wo), HI(wo), "s"(rfl(HD * DM * 2 - bn * 256)), LO(H), HI(H), LO(wd), HI(wd), "s"(rfl(DF * DM * 2 - bn * 256)),
     LO(part), HI(part), LO(X), HI(X), LO(OUT), HI(OUT), "s"(wid), "s"(rfl(sp)), "s"(rfl(bm * 128)), "s"(rfl(bn * 256)), LO(cnt), HI(cnt)
     : CLOB_V3, CLOB_A3, CLOB_S3, "vcc", "scc", "m0", "memory");
}

extern "C" void kernel_launch(void* const* d_in, const int* in_sizes, int n_in, void* d_out, int out_size, void* d_ws, size_t ws_size, hipStream_t stream) {
  const unsigned short* x = (const unsigned short*)d_in[0];
  const unsigned short* kc = (const unsigned short*)d_in[1]; const unsigned short* vc = (const unsigned short*)d_in[2];
  const unsigned short* wq = (const unsigned short*)d_in[3]; const unsigned short* wo = (const unsigned short*)d_in[4];
  const unsigned short* wg = (const unsigned short*)d_in[6]; const unsigned short* wu = (const unsigned short*)d_in[7]; const unsigned short* wd = (const unsigned short*)d_in[8];
  char* ws = (char*)d_ws;
  unsigned short* xn = (unsigned short*)ws;
  unsigned short* o  = (unsigned short*)(ws + (2u << 20));
  unsigned short* h  = (unsigned short*)(ws + (20u << 20));
  (void)hipFuncSetAttribute((const void*)fused_kernel, hipFuncAttributeMaxDynamicSharedMemorySize, 163840);
  char* part = ws + (28u << 20);
  unsigned* cnt = (unsigned*)(ws + (50u << 20));
  (void)hipFuncSetAttribute((const void*)final_kernel, hipFuncAttributeMaxDynamicSharedMemorySize, 131072);
  ln_kernel<<<SQ / 4, 256, 0, stream>>>(x, xn, cnt);
  fused_kernel<<<256, 256, 163840, stream>>>(xn, wq, wg, wu, kc, vc, o, h, (const char*)wo, (const char*)wd);
  final_kernel<<<256, 256, 131072, stream>>>(o, h, wo, wd, x, (unsigned short*)d_out, part, cnt);
}
```
